```python
import torch

def _softmax(x, axis=-1):
    return torch.softmax(x, dim=axis)
def _gelu(x):
    return torch.nn.functional.gelu(x, approximate="tanh")
def _silu(x):
    return torch.nn.functional.silu(x)
def _sigmoid(x):
    return torch.sigmoid(x)
def _layernorm(x, axis=-1, eps=1e-5):
    m = x.mean(dim=axis, keepdim=True)
    v = x.var(dim=axis, keepdim=True, unbiased=False)
    return (x - m) / torch.sqrt(v + eps)
def _scatter_add(tgt, idx, src, axis=0):
    return tgt.clone().index_add_(axis, idx, src)
def _segment_sum(src, seg, n):
    out = torch.zeros((n,) + src.shape[1:], dtype=src.dtype)
    return out.index_add_(0, seg, src)
def _pad(x, pw):
    flat = tuple(v for pair in reversed(pw) for v in pair)
    return torch.nn.functional.pad(x, flat)

kernel_name = 'sequential_preLN__spectral_fnet__moe_arch_mod__prompt'

def setup_inputs():

    _w = (lambda a: (lambda t: t.to(torch.bfloat16) if t.is_floating_point() else t)(torch.as_tensor(a)))
    def _A_setup():
        B, S, D = 1, 2048, 512
        ns = torch.arange(S)[:, None] * torch.arange(S)[None, :]
        nd = torch.arange(D)[:, None] * torch.arange(D)[None, :]
        Ws_re = torch.cos(-2.0 * torch.pi * ns / S).to(float)
        Ws_im = torch.sin(-2.0 * torch.pi * ns / S).to(float)
        Wd_re = torch.cos(-2.0 * torch.pi * nd / D).to(float)
        Wd_im = torch.sin(-2.0 * torch.pi * nd / D).to(float)
        return {
            "x": _w((lambda *s: torch.randn(*s, dtype=torch.bfloat16) * 0.1)(B, S, D)),
            "dft_seq_re": _w(Ws_re),
            "dft_seq_im": _w(Ws_im),
            "dft_hid_re": _w(Wd_re),
            "dft_hid_im": _w(Wd_im),
        }
    _A_in = _A_setup()

    def _B_setup():
        B, S, D, F, C = 1, 2048, 512, 1792, 8
        return {
            "x": _w((lambda *s: torch.randn(*s, dtype=torch.bfloat16) * 0.1)(B, S, D)),
            "w_r": _w((lambda *s: torch.randn(*s, dtype=torch.bfloat16) * 1.6)(D)),
            "w_gate": _w((lambda *s: torch.randn(*s, dtype=torch.bfloat16) * 0.8)(D, F)),
            "w_up": _w((lambda *s: torch.randn(*s, dtype=torch.bfloat16) * 0.8)(D, F)),
            "w_down": _w((lambda *s: torch.randn(*s, dtype=torch.bfloat16) * 0.8)(F, D)),
        }
    _B_in = _B_setup()

    return {
        "A_x": _A_in["x"],
        "A_dft_seq_re": _A_in["dft_seq_re"],
        "A_dft_seq_im": _A_in["dft_seq_im"],
        "A_dft_hid_re": _A_in["dft_hid_re"],
        "A_dft_hid_im": _A_in["dft_hid_im"],
        "B_x": _B_in["x"],
        "B_w_r": _B_in["w_r"],
        "B_w_gate": _B_in["w_gate"],
        "B_w_up": _B_in["w_up"],
        "B_w_down": _B_in["w_down"],
    }

def reference(A_x, A_dft_seq_re, A_dft_seq_im, A_dft_hid_re, A_dft_hid_im, B_x, B_w_r, B_w_gate, B_w_up, B_w_down):
    def _A_ref(x, dft_seq_re, dft_seq_im, dft_hid_re, dft_hid_im):
        a = torch.einsum("bsd,ed->bse", x, dft_hid_re)
        b = torch.einsum("bsd,ed->bse", x, dft_hid_im)
        y = torch.einsum("ts,bse->bte", dft_seq_re, a) - torch.einsum("ts,bse->bte", dft_seq_im, b)
        return y

    def _B_ref(x, w_r, w_gate, w_up, w_down):
        B, S, D = x.shape
        C = 8
        r = x @ w_r
        idx = torch.argsort(-r, axis=-1)[:, :C]
        idx_e = (lambda x, n, axis: torch.repeat_interleave(x, n, dim=axis))(idx.reshape(B, C, 1), D, axis=2)
        x_sel = (lambda x, i, axis: torch.take_along_dim(x, i, dim=axis))(x, idx_e, axis=1)
        g = _silu(x_sel @ w_gate)
        u = x_sel @ w_up
        y = (g * u) @ w_down
        r_sel = _sigmoid((lambda x, i, axis: torch.take_along_dim(x, i, dim=axis))(r, idx, axis=1))
        y = y * r_sel.reshape(B, C, 1)
        b_off = (torch.arange(B) * S).reshape(B, 1)
        flat_idx = (idx + b_off).reshape(B * C)
        zero = (x * 0.0).reshape(B * S, D)
        upd = _scatter_add(zero, flat_idx, y.reshape(B * C, D), axis=0)
        return x + upd.reshape(B, S, D)

    x = A_x
    h = x + _A_ref(_layernorm(x, axis=-1), A_dft_seq_re, A_dft_seq_im, A_dft_hid_re, A_dft_hid_im)
    h2 = h + _B_ref(_layernorm(h, axis=-1), B_w_r, B_w_gate, B_w_up, B_w_down)
    return h2
```

```cpp
#include <hip/hip_runtime.h>
#include <hip/hip_bf16.h>
#include <stdint.h>
#include <utility>

typedef unsigned short u16;
typedef __attribute__((ext_vector_type(8))) short bf16x8;
typedef __attribute__((ext_vector_type(4))) short s16x4;
typedef __attribute__((ext_vector_type(4))) float f32x4;
typedef __attribute__((ext_vector_type(4))) unsigned u32x4;
typedef __attribute__((ext_vector_type(2))) unsigned u32x2;

constexpr int S = 2048, D = 512, F = 1792, CAP = 8;

__device__ __forceinline__ float bf2f(unsigned v) { return __uint_as_float(v << 16); }
__device__ __forceinline__ float bflo(unsigned v) { return __uint_as_float(v << 16); }
__device__ __forceinline__ float bfhi(unsigned v) { return __uint_as_float(v & 0xffff0000u); }
__device__ __forceinline__ unsigned f2bf(float f) { __bf16 b = (__bf16)f; return (unsigned)__builtin_bit_cast(u16, b); }
__device__ __forceinline__ float rbf(float f) { return bf2f(f2bf(f)); }
__device__ __forceinline__ unsigned pack2(float lo, float hi) { return f2bf(lo) | (f2bf(hi) << 16); }

#define WAIT_V(n) asm volatile("s_waitcnt vmcnt(%0)" ::"n"(n) : "memory")
#define WAIT_L(n) asm volatile("s_waitcnt lgkmcnt(%0)" ::"n"(n) : "memory")
#define GLDS16(src, dst) __builtin_amdgcn_global_load_lds((const unsigned*)(src), (unsigned*)(dst), 16, 0, 0)

constexpr size_t WS_ABT = 0;
constexpr size_t WS_Y = WS_ABT + (size_t)1024 * 2048 * 2;
constexpr size_t WS_R = WS_Y + (size_t)2 * 2048 * 512 * 2;
constexpr size_t WS_ST = WS_R + 2048 * 4;
constexpr size_t WS_ACT = WS_ST + 2048 * 8;
constexpr size_t WS_IDX = WS_ACT + 8 * 1792 * 2;

__global__ __launch_bounds__(512) void k1_ln_gemm1(const u16* __restrict__ x, const u16* __restrict__ wd_re,
                                                   const u16* __restrict__ wd_im, u16* __restrict__ abT) {
  __shared__ __attribute__((aligned(1024))) char lds[64 * 1024];
  const int b = blockIdx.x, xcd = b & 7, j = b >> 3;
  const int st = (xcd >> 1) * 4 + (j >> 3), et = (xcd & 1) * 8 + (j & 7);
  const int tid = threadIdx.x, w = tid >> 6, l = tid & 63, fr = l & 15, q = l >> 4;
  const u16* wsrc = (et < 8 ? wd_re + (size_t)et * 64 * D : wd_im + (size_t)(et - 8) * 64 * D);
  {
    const int r8 = l >> 3, ch = l & 7;
#pragma unroll
    for (int i = 0; i < 8; ++i) {
      const int row = i * 8 + r8;
      const int sch = ch ^ ((row >> 1) & 7);
      GLDS16(wsrc + (size_t)row * D + w * 64 + sch * 8, lds + w * 8192 + i * 1024);
    }
  }
  const int srow = st * 128 + w * 16 + fr;
  const u16* xp = x + (size_t)srow * D + q * 8;
  u32x4 xv[16];
#pragma unroll
  for (int ks = 0; ks < 16; ++ks) xv[ks] = *(const u32x4*)(xp + ks * 32);
  float s = 0.f;
#pragma unroll
  for (int ks = 0; ks < 16; ++ks)
#pragma unroll
    for (int e = 0; e < 4; ++e) s += bflo(xv[ks][e]) + bfhi(xv[ks][e]);
  s += __shfl_xor(s, 16); s += __shfl_xor(s, 32);
  const float mean = s * (1.0f / D);
  float v = 0.f;
#pragma unroll
  for (int ks = 0; ks < 16; ++ks)
#pragma unroll
    for (int e = 0; e < 4; ++e) { float a = bflo(xv[ks][e]) - mean, c = bfhi(xv[ks][e]) - mean; v += a * a + c * c; }
  v += __shfl_xor(v, 16); v += __shfl_xor(v, 32);
  const float mb = rbf(mean);
  const float sd = rbf(sqrtf(rbf(rbf(v * (1.0f / D)) + 1e-5f)));
#pragma unroll
  for (int ks = 0; ks < 16; ++ks)
#pragma unroll
    for (int e = 0; e < 4; ++e) {
      float a = rbf(bflo(xv[ks][e]) - mb) / sd, c = rbf(bfhi(xv[ks][e]) - mb) / sd;
      xv[ks][e] = pack2(a, c);
    }
  WAIT_V(0);
  __syncthreads();
  f32x4 acc[4] = {};
#pragma unroll
  for (int ks = 0; ks < 16; ++ks) {
#pragma unroll
    for (int n = 0; n < 4; ++n) {
      const int row = n * 16 + fr;
      const int ch = ((ks & 1) * 4 + q) ^ ((row >> 1) & 7);
      bf16x8 bfr = *(const bf16x8*)(lds + (ks >> 1) * 8192 + row * 128 + ch * 16);
      acc[n] = __builtin_amdgcn_mfma_f32_16x16x32_bf16(__builtin_bit_cast(bf16x8, xv[ks]), bfr, acc[n], 0, 0, 0);
    }
  }
#pragma unroll
  for (int n = 0; n < 4; ++n) {
    const int e = et * 64 + n * 16 + fr;
    u32x2 o = {pack2(acc[n][0], acc[n][1]), pack2(acc[n][2], acc[n][3])};
    *(u32x2*)(abT + (size_t)e * S + st * 128 + w * 16 + q * 4) = o;
  }
}

constexpr int NS = 6, STAGE_B = 24 * 1024, NKT = 32;

struct K2Ctx {
  const u16* asrc[4];
  const u16* bsrc[2];
  char* lds;
  int w;
  int aoff[4][2];
  int boff[2][2];
};

template <int T>
__device__ __forceinline__ void k2_issue(const K2Ctx& c) {
  char* base = c.lds + (T % NS) * STAGE_B;
#pragma unroll
  for (int i = 0; i < 4; ++i) GLDS16(c.asrc[i] + T * 64, base + (c.w * 4 + i) * 1024);
#pragma unroll
  for (int i = 0; i < 2; ++i) GLDS16(c.bsrc[i] + T * 64, base + 16384 + (c.w * 2 + i) * 1024);
}

template <int T>
__device__ __forceinline__ void k2_step(const K2Ctx& c, f32x4 (&acc)[2][4]) {
  constexpr int inflight = (NKT - 1 - T) < (NS - 2) ? (NKT - 1 - T) : (NS - 2);
  WAIT_V(inflight * 6);
  WAIT_L(0);
  __builtin_amdgcn_s_barrier();
  if constexpr (T + NS - 1 < NKT) k2_issue<T + NS - 1>(c);
  const char* base = c.lds + (T % NS) * STAGE_B;
#pragma unroll
  for (int ks = 0; ks < 2; ++ks) {
    bf16x8 wf[4], af[2];
#pragma unroll
    for (int m = 0; m < 4; ++m) wf[m] = *(const bf16x8*)(base + c.aoff[m][ks]);
#pragma unroll
    for (int n = 0; n < 2; ++n) af[n] = *(const bf16x8*)(base + c.boff[n][ks]);
#pragma unroll
    for (int n = 0; n < 2; ++n)
#pragma unroll
      for (int m = 0; m < 4; ++m)
        acc[n][m] = __builtin_amdgcn_mfma_f32_16x16x32_bf16(af[n], wf[m], acc[n][m], 0, 0, 0);
  }
}

template <int... Ts>
__device__ __forceinline__ void k2_steps(const K2Ctx& c, f32x4 (&acc)[2][4], std::integer_sequence<int, Ts...>) {
  (k2_step<Ts>(c, acc), ...);
}
template <int... Ts>
__device__ __forceinline__ void k2_prol(const K2Ctx& c, std::integer_sequence<int, Ts...>) {
  (k2_issue<Ts>(c), ...);
}

__global__ __launch_bounds__(256) void k2_gemm2(const u16* __restrict__ ws_re, const u16* __restrict__ ws_im,
                                                const u16* __restrict__ abT, u16* __restrict__ y) {
  __shared__ __attribute__((aligned(1024))) char lds[NS * STAGE_B];
  const int b = blockIdx.x, xcd = b & 7, j = b >> 3;
  const int cc = xcd >> 2, tt = (xcd & 3) * 4 + (j >> 3), et = j & 7;
  const int tid = threadIdx.x, w = tid >> 6, l = tid & 63, fr = l & 15, q = l >> 4;
  const int wt = w >> 1, we = w & 1;
  K2Ctx c;
  c.lds = lds; c.w = w;
  const u16* wsrc = (cc ? ws_im : ws_re) + (size_t)tt * 128 * S;
  const u16* bsrc = abT + (size_t)(cc * 512 + et * 64) * S;
  {
    const int r8 = l >> 3, ch = l & 7;
#pragma unroll
    for (int i = 0; i < 4; ++i) {
      const int row = (w * 4 + i) * 8 + r8;
      c.asrc[i] = wsrc + (size_t)row * S + ((ch ^ ((row >> 1) & 7)) * 8);
    }
#pragma unroll
    for (int i = 0; i < 2; ++i) {
      const int row = (w * 2 + i) * 8 + r8;
      c.bsrc[i] = bsrc + (size_t)row * S + ((ch ^ ((row >> 1) & 7)) * 8);
    }
  }
#pragma unroll
  for (int ks = 0; ks < 2; ++ks) {
#pragma unroll
    for (int m = 0; m < 4; ++m) { const int row = wt * 64 + m * 16 + fr; c.aoff[m][ks] = row * 128 + (((ks * 4 + q) ^ ((row >> 1) & 7)) * 16); }
#pragma unroll
    for (int n = 0; n < 2; ++n) { const int row = we * 32 + n * 16 + fr; c.boff[n][ks] = 16384 + row * 128 + (((ks * 4 + q) ^ ((row >> 1) & 7)) * 16); }
  }
  f32x4 acc[2][4] = {};
  k2_prol(c, std::make_integer_sequence<int, NS - 1>{});
  k2_steps(c, acc, std::make_integer_sequence<int, NKT>{});
  u16* yo = y + (size_t)cc * S * D;
#pragma unroll
  for (int n = 0; n < 2; ++n)
#pragma unroll
    for (int m = 0; m < 4; ++m) {
      const int t = tt * 128 + wt * 64 + m * 16 + fr;
      const int e = et * 64 + we * 32 + n * 16 + q * 4;
      u32x2 o = {pack2(acc[n][m][0], acc[n][m][1]), pack2(acc[n][m][2], acc[n][m][3])};
      *(u32x2*)(yo + (size_t)t * D + e) = o;
    }
}

__device__ __forceinline__ float wave_sum(float v) {
#pragma unroll
  for (int m = 32; m >= 1; m >>= 1) v += __shfl_xor(v, m);
  return v;
}

__global__ __launch_bounds__(256) void k3_ln2(const u16* __restrict__ x, const u16* __restrict__ y, const u16* __restrict__ w_r,
                                              u16* __restrict__ out, unsigned* __restrict__ rkey, float2* __restrict__ stats) {
  const int tid = threadIdx.x, w = tid >> 6, l = tid & 63;
  const u32x4 wr = *(const u32x4*)(w_r + l * 8);
#pragma unroll
  for (int rr = 0; rr < 2; ++rr) {
    const int t = blockIdx.x * 8 + w * 2 + rr;
    const u32x4 xv = *(const u32x4*)(x + (size_t)t * D + l * 8);
    const u32x4 yr = *(const u32x4*)(y + (size_t)t * D + l * 8);
    const u32x4 yi = *(const u32x4*)(y + (size_t)S * D + (size_t)t * D + l * 8);
    float h[8];
#pragma unroll
    for (int e = 0; e < 4; ++e) {
      h[2 * e] = rbf(bflo(xv[e]) + rbf(bflo(yr[e]) - bflo(yi[e])));
      h[2 * e + 1] = rbf(bfhi(xv[e]) + rbf(bfhi(yr[e]) - bfhi(yi[e])));
    }
    float s = 0.f;
#pragma unroll
    for (int e = 0; e < 8; ++e) s += h[e];
    s = wave_sum(s);
    const float mean = s * (1.0f / D);
    float v = 0.f;
#pragma unroll
    for (int e = 0; e < 8; ++e) { float a = h[e] - mean; v += a * a; }
    v = wave_sum(v);
    const float mb = rbf(mean);
    const float sd = rbf(sqrtf(rbf(rbf(v * (1.0f / D)) + 1e-5f)));
    float hn[8], r = 0.f;
#pragma unroll
    for (int e = 0; e < 8; ++e) hn[e] = rbf(rbf(h[e] - mb) / sd);
#pragma unroll
    for (int e = 0; e < 4; ++e) r += hn[2 * e] * bflo(wr[e]) + hn[2 * e + 1] * bfhi(wr[e]);
    r = wave_sum(r);
    u32x4 o;
#pragma unroll
    for (int e = 0; e < 4; ++e) o[e] = pack2(h[2 * e] + hn[2 * e], h[2 * e + 1] + hn[2 * e + 1]);
    *(u32x4*)(out + (size_t)t * D + l * 8) = o;
    if (l == 0) {
      unsigned rb = f2bf(r);
      unsigned ord = (rb & 0x8000u) ? (~rb & 0xffffu) : (rb | 0x8000u);
      rkey[t] = (ord << 16) | (unsigned)(0xffff - t);
      stats[t] = make_float2(mb, sd);
    }
  }
}

__device__ __forceinline__ unsigned wave_max_u32(unsigned v) {
#pragma unroll
  for (int m = 32; m >= 1; m >>= 1) { unsigned o = __shfl_xor(v, m); v = v > o ? v : o; }
  return v;
}

__global__ __launch_bounds__(256) void k4_gateup(const u16* __restrict__ x, const u16* __restrict__ y, const unsigned* __restrict__ rkey,
                                                 const float2* __restrict__ stats, const u16* __restrict__ w_gate,
                                                 const u16* __restrict__ w_up, u16* __restrict__ act, unsigned* __restrict__ idx_out) {
  constexpr int XS_STRIDE = 1024 + 16;
  constexpr int OFF_XS = 32768, OFF_RED = OFF_XS + 16 * XS_STRIDE, OFF_CAND = OFF_RED + 4 * 2 * 128 * 4;
  __shared__ __attribute__((aligned(1024))) char lds[OFF_CAND + 256];
  const int tid = threadIdx.x, w = tid >> 6, l = tid & 63, fr = l & 15, q = l >> 4;
  const int f0 = blockIdx.x * 16;
#pragma unroll
  for (int i = 0; i < 4; ++i) {
    const int row = (w * 4 + i) * 32 + (l >> 1);
    GLDS16(w_gate + (size_t)row * F + f0 + (l & 1) * 8, lds + (w * 4 + i) * 1024);
    GLDS16(w_up + (size_t)row * F + f0 + (l & 1) * 8, lds + 16384 + (w * 4 + i) * 1024);
  }
  u32x4 k0 = *(const u32x4*)(rkey + tid * 8), k1 = *(const u32x4*)(rkey + tid * 8 + 4);
  unsigned kk[8] = {k0[0], k0[1], k0[2], k0[3], k1[0], k1[1], k1[2], k1[3]};
  unsigned* cand = (unsigned*)(lds + OFF_CAND);
  unsigned mine = 0;
#pragma unroll
  for (int it = 0; it < 8; ++it) {
    unsigned m = 0;
#pragma unroll
    for (int e = 0; e < 8; ++e) m = kk[e] > m ? kk[e] : m;
    m = wave_max_u32(m);
#pragma unroll
    for (int e = 0; e < 8; ++e) kk[e] = (kk[e] == m) ? 0u : kk[e];
    if (l == it) mine = m;
  }
  if (l < 8) cand[w * 8 + l] = mine;
  for (int i = tid; i < 8 * XS_STRIDE / 16; i += 256) *(u32x4*)(lds + OFF_XS + 8 * XS_STRIDE + i * 16) = (u32x4){0, 0, 0, 0};
  __syncthreads();
  unsigned cv = cand[l & 31];
  int rank = 0;
  for (int i = 0; i < 32; ++i) rank += (cand[i] > cv) ? 1 : 0;
  __syncthreads();
  if (w == 0 && l < 32 && rank < 8) cand[32 + rank] = cv;
  __syncthreads();
#pragma unroll
  for (int rr = 0; rr < 2; ++rr) {
    const int tok = w * 2 + rr;
    const unsigned key = cand[32 + tok];
    const int t = 0xffff - (int)(key & 0xffffu);
    const u32x4 xv = *(const u32x4*)(x + (size_t)t * D + l * 8);
    const u32x4 yr = *(const u32x4*)(y + (size_t)t * D + l * 8);
    const u32x4 yi = *(const u32x4*)(y + (size_t)S * D + (size_t)t * D + l * 8);
    const float2 st = stats[t];
    u32x4 o;
#pragma unroll
    for (int e = 0; e < 4; ++e) {
      float h0 = rbf(bflo(xv[e]) + rbf(bflo(yr[e]) - bflo(yi[e])));
      float h1 = rbf(bfhi(xv[e]) + rbf(bfhi(yr[e]) - bfhi(yi[e])));
      o[e] = pack2(rbf(h0 - st.x) / st.y, rbf(h1 - st.x) / st.y);
    }
    *(u32x4*)(lds + OFF_XS + tok * XS_STRIDE + l * 16) = o;
  }
  if (blockIdx.x == 0 && tid < 8) idx_out[tid] = cand[32 + tid];
  WAIT_V(0);
  __syncthreads();
  f32x4 ag = {}, au = {};
#pragma unroll
  for (int i = 0; i < 4; ++i) {
    const int ks = w * 4 + i;
    bf16x8 a = *(const bf16x8*)(lds + OFF_XS + fr * XS_STRIDE + ks * 64 + q * 16);
    const int r0 = ks * 32 + q * 8 + (fr >> 2);
    const int coff = (fr & 3) * 8;
    typedef __attribute__((ext_vector_type(4))) short v4i16_t;
    v4i16_t g0 = __builtin_amdgcn_ds_read_tr16_b64_v4i16((__attribute__((address_space(3))) v4i16_t*)(lds + r0 * 32 + coff));
    v4i16_t g1 = __builtin_amdgcn_ds_read_tr16_b64_v4i16((__attribute__((address_space(3))) v4i16_t*)(lds + (r0 + 4) * 32 + coff));
    v4i16_t u0 = __builtin_amdgcn_ds_read_tr16_b64_v4i16((__attribute__((address_space(3))) v4i16_t*)(lds + 16384 + r0 * 32 + coff));
    v4i16_t u1 = __builtin_amdgcn_ds_read_tr16_b64_v4i16((__attribute__((address_space(3))) v4i16_t*)(lds + 16384 + (r0 + 4) * 32 + coff));
    bf16x8 bg = {g0[0], g0[1], g0[2], g0[3], g1[0], g1[1], g1[2], g1[3]};
    bf16x8 bu = {u0[0], u0[1], u0[2], u0[3], u1[0], u1[1], u1[2], u1[3]};
    ag = __builtin_amdgcn_mfma_f32_16x16x32_bf16(a, bg, ag, 0, 0, 0);
    au = __builtin_amdgcn_mfma_f32_16x16x32_bf16(a, bu, au, 0, 0, 0);
  }
  float* red = (float*)(lds + OFF_RED);
  if (q < 2) {
#pragma unroll
    for (int jj = 0; jj < 4; ++jj) {
      red[(w * 2 + 0) * 128 + (q * 4 + jj) * 16 + fr] = ag[jj];
      red[(w * 2 + 1) * 128 + (q * 4 + jj) * 16 + fr] = au[jj];
    }
  }
  __syncthreads();
  if (tid < 128) {
    float g = 0.f, u = 0.f;
#pragma unroll
    for (int ww = 0; ww < 4; ++ww) { g += red[(ww * 2) * 128 + tid]; u += red[(ww * 2 + 1) * 128 + tid]; }
    g = rbf(g); u = rbf(u);
    g = rbf(g / (1.0f + expf(-g)));
    act[(tid >> 4) * F + f0 + (tid & 15)] = (u16)f2bf(g * u);
  }
}

__global__ __launch_bounds__(512) void k5_down(const u16* __restrict__ x, const u16* __restrict__ y, const float2* __restrict__ stats,
                                               const u16* __restrict__ act, const unsigned* __restrict__ idx_in,
                                               const u16* __restrict__ w_down, u16* __restrict__ out) {
  constexpr int AS_STRIDE = F * 2 + 16;
  constexpr int OFF_AS = F * 32, OFF_RED = OFF_AS + 16 * AS_STRIDE;
  __shared__ __attribute__((aligned(1024))) char lds[OFF_RED + 8 * 128 * 4];
  const int tid = threadIdx.x, w = tid >> 6, l = tid & 63, fr = l & 15, q = l >> 4;
  const int c0 = blockIdx.x * 16;
#pragma unroll
  for (int i = 0; i < 7; ++i) {
    const int row = (w * 7 + i) * 32 + (l >> 1);
    GLDS16(w_down + (size_t)row * D + c0 + (l & 1) * 8, lds + (w * 7 + i) * 1024);
  }
  for (int i = l; i < 224; i += 64) {
    *(u32x4*)(lds + OFF_AS + w * AS_STRIDE + i * 16) = *(const u32x4*)(act + w * F + i * 8);
    *(u32x4*)(lds + OFF_AS + (8 + w) * AS_STRIDE + i * 16) = (u32x4){0, 0, 0, 0};
  }
  WAIT_V(0);
  __syncthreads();
  f32x4 acc = {};
#pragma unroll
  for (int i = 0; i < 7; ++i) {
    const int ks = w * 7 + i;
    bf16x8 a = *(const bf16x8*)(lds + OFF_AS + fr * AS_STRIDE + ks * 64 + q * 16);
    const int r0 = ks * 32 + q * 8 + (fr >> 2);
    const int coff = (fr & 3) * 8;
    typedef __attribute__((ext_vector_type(4))) short v4i16_t;
    v4i16_t g0 = __builtin_amdgcn_ds_read_tr16_b64_v4i16((__attribute__((address_space(3))) v4i16_t*)(lds + r0 * 32 + coff));
    v4i16_t g1 = __builtin_amdgcn_ds_read_tr16_b64_v4i16((__attribute__((address_space(3))) v4i16_t*)(lds + (r0 + 4) * 32 + coff));
    bf16x8 bg = {g0[0], g0[1], g0[2], g0[3], g1[0], g1[1], g1[2], g1[3]};
    acc = __builtin_amdgcn_mfma_f32_16x16x32_bf16(a, bg, acc, 0, 0, 0);
  }
  float* red = (float*)(lds + OFF_RED);
  if (q < 2) {
#pragma unroll
    for (int jj = 0; jj < 4; ++jj) red[w * 128 + (q * 4 + jj) * 16 + fr] = acc[jj];
  }
  __syncthreads();
  if (tid < 128) {
    float s = 0.f;
#pragma unroll
    for (int ww = 0; ww < 8; ++ww) s += red[ww * 128 + tid];
    const unsigned key = idx_in[tid >> 4];
    const int t = 0xffff - (int)(key & 0xffffu);
    const unsigned ord = key >> 16;
    const unsigned rb = (ord & 0x8000u) ? (ord & 0x7fffu) : (~ord & 0xffffu);
    const float r = bf2f(rb);
    const float sig = rbf(1.0f / (1.0f + expf(-r)));
    const float upd = rbf(rbf(s) * sig);
    const int col = c0 + (tid & 15);
    const size_t o = (size_t)t * D + col;
    const float h = rbf(bf2f(x[o]) + rbf(bf2f(y[o]) - bf2f(y[(size_t)S * D + o])));
    const float2 st = stats[t];
    const float hn = rbf(rbf(h - st.x) / st.y);
    out[o] = (u16)f2bf(h + rbf(hn + upd));
  }
}

extern "C" void kernel_launch(void* const* d_in, const int* in_sizes, int n_in, void* d_out, int out_size, void* d_ws,
                              size_t ws_size, hipStream_t stream) {
  const u16* x = (const u16*)d_in[0];
  const u16* ws_re = (const u16*)d_in[1];
  const u16* ws_im = (const u16*)d_in[2];
  const u16* wd_re = (const u16*)d_in[3];
  const u16* wd_im = (const u16*)d_in[4];
  const u16* w_r = (const u16*)d_in[6];
  const u16* w_gate = (const u16*)d_in[7];
  const u16* w_up = (const u16*)d_in[8];
  const u16* w_down = (const u16*)d_in[9];
  u16* out = (u16*)d_out;
  char* ws = (char*)d_ws;
  u16* abT = (u16*)(ws + WS_ABT);
  u16* y = (u16*)(ws + WS_Y);
  unsigned* rkey = (unsigned*)(ws + WS_R);
  float2* stats = (float2*)(ws + WS_ST);
  u16* act = (u16*)(ws + WS_ACT);
  unsigned* idx = (unsigned*)(ws + WS_IDX);
  k1_ln_gemm1<<<256, 512, 0, stream>>>(x, wd_re, wd_im, abT);
  k2_gemm2<<<256, 256, 0, stream>>>(ws_re, ws_im, abT, y);
  k3_ln2<<<256, 256, 0, stream>>>(x, y, w_r, out, rkey, stats);
  k4_gateup<<<F / 16, 256, 0, stream>>>(x, y, rkey, stats, w_gate, w_up, act, idx);
  k5_down<<<D / 16, 512, 0, stream>>>(x, y, stats, act, idx, w_down, out);
}
```

```cpp
#include <hip/hip_runtime.h>
#include <hip/hip_bf16.h>
#include <stdint.h>
#include <utility>

typedef unsigned short u16;
typedef __attribute__((ext_vector_type(8))) short bf16x8;
typedef __attribute__((ext_vector_type(4))) short v4i16_t;
typedef __attribute__((ext_vector_type(4))) float f32x4;
typedef __attribute__((ext_vector_type(4))) unsigned u32x4;
typedef __attribute__((ext_vector_type(2))) unsigned u32x2;
#define GAS __attribute__((address_space(1)))
#define LAS __attribute__((address_space(3)))
typedef GAS unsigned gu32;
typedef GAS unsigned long long gu64;
#define RLX_AGENT __ATOMIC_RELAXED, __HIP_MEMORY_SCOPE_AGENT

constexpr int S = 2048, D = 512, F = 1792;

__device__ __forceinline__ float bf2f(unsigned v) { return __uint_as_float(v << 16); }
__device__ __forceinline__ float bflo(unsigned v) { return __uint_as_float(v << 16); }
__device__ __forceinline__ float bfhi(unsigned v) { return __uint_as_float(v & 0xffff0000u); }
__device__ __forceinline__ unsigned f2bf(float f) { __bf16 b = (__bf16)f; return (unsigned)__builtin_bit_cast(u16, b); }
__device__ __forceinline__ float rbf(float f) { return bf2f(f2bf(f)); }
__device__ __forceinline__ unsigned pack2(float lo, float hi) { return f2bf(lo) | (f2bf(hi) << 16); }

#define WAIT_V(n) asm volatile("s_waitcnt vmcnt(%0)" ::"n"(n) : "memory")
#define WAIT_L(n) asm volatile("s_waitcnt lgkmcnt(%0)" ::"n"(n) : "memory")
#define BAR() __builtin_amdgcn_s_barrier()
#define GLDS16(src, dst, aux) __builtin_amdgcn_global_load_lds((const unsigned*)(src), (unsigned*)(dst), 16, 0, aux)
#define TOUCH(reg, ptr) asm volatile("global_load_dword %0, %1, off" : "+v"(reg) : "v"(ptr) : "memory")
#define KEEP(reg) asm volatile("" ::"v"(reg))

template <int CTRL, int RM> __device__ __forceinline__ float dpp_f(float v) {
  return __builtin_bit_cast(float, __builtin_amdgcn_update_dpp(0, __builtin_bit_cast(int, v), CTRL, RM, 0xf, true));
}
__device__ __forceinline__ float wave_sum(float v) {
  v += dpp_f<0x111, 0xf>(v); v += dpp_f<0x112, 0xf>(v); v += dpp_f<0x114, 0xf>(v); v += dpp_f<0x118, 0xf>(v);
  v += dpp_f<0x142, 0xa>(v); v += dpp_f<0x143, 0xc>(v);
  return __builtin_bit_cast(float, __builtin_amdgcn_readlane(__builtin_bit_cast(int, v), 63));
}
template <int CTRL, int RM> __device__ __forceinline__ unsigned dpp_u(unsigned v) {
  return (unsigned)__builtin_amdgcn_update_dpp(0, (int)v, CTRL, RM, 0xf, true);
}
__device__ __forceinline__ unsigned umax(unsigned a, unsigned b) { return a > b ? a : b; }
__device__ __forceinline__ unsigned wave_max_u32(unsigned v) {
  v = umax(v, dpp_u<0x111, 0xf>(v)); v = umax(v, dpp_u<0x112, 0xf>(v)); v = umax(v, dpp_u<0x114, 0xf>(v)); v = umax(v, dpp_u<0x118, 0xf>(v));
  v = umax(v, dpp_u<0x142, 0xa>(v)); v = umax(v, dpp_u<0x143, 0xc>(v));
  return (unsigned)__builtin_amdgcn_readlane((int)v, 63);
}

#define STAMPW(k, i, wv)
#define STAMP(k, i) STAMPW(k, i, 0)

constexpr unsigned WS_ABT = 0;
constexpr unsigned WS_Y = WS_ABT + 1024u * 2048 * 2;
constexpr unsigned WS_F1 = WS_Y + 4u * 2048 * 512 * 2;
constexpr unsigned WS_F2 = WS_F1 + 2048;
constexpr unsigned WS_GR = WS_F2 + 2048;
constexpr unsigned WS_GA = WS_GR + 1024 * 8;
constexpr unsigned WS_EP = WS_GA + 224 * 256;
constexpr unsigned WS_END = WS_EP + 256;

constexpr int SPIN_MAX = 1 << 18;

constexpr int L_QR = 81920;
constexpr int L_RST = 73728;
constexpr int L_TAG = 74752;
constexpr int L_W = 16384;
constexpr int L_T2 = 131072;
constexpr int XS_STRIDE = 1024 + 16;
constexpr int L_XS = 81920;
constexpr int L_RED = L_XS + 8 * XS_STRIDE;
constexpr int L_MISC = L_RED + 8 * 128 * 4;
constexpr int L_HH = L_MISC + 1024;
constexpr int AS_STRIDE = F * 2 + 16;
constexpr int L_AS = L_HH + 1024;
static_assert(L_AS + 8 * AS_STRIDE <= L_T2, "lds");

struct Ring1 {
  static constexpr int slot(int i) { return i < 3 ? i * 24576 : L_QR + (i - 3) * 24576; }
  static constexpr int p(int T) { return slot(T % 5); }
  static constexpr int q(int T) { return slot(T % 5) + 16384; }
};
struct Ring2 {
  static constexpr int p(int T) { return (T % 5) * 16384; }
  static constexpr int q(int T) { return L_QR + (T % 5) * 16384; }
};

template <int BP, int BQ, int NKT, class RG, bool STATS>
struct Cons {
  static constexpr int TP = BP / 32, TQ = BQ / 32;
  struct Ctx { const char* lds; int poff[TP][2]; int qoff[TQ][2]; };
  struct Fr { bf16x8 pf[2][TP], qf[2][TQ]; };
  struct St { f32x4 s1[2], s2[2], sw[TQ]; };
  template <int T> static __device__ __forceinline__ void readf(const Ctx& c, Fr& f) {
#pragma unroll
    for (int ks = 0; ks < 2; ++ks) {
#pragma unroll
      for (int m = 0; m < TP; ++m) f.pf[ks][m] = *(const bf16x8*)(c.lds + RG::p(T) + c.poff[m][ks]);
#pragma unroll
      for (int n = 0; n < TQ; ++n) f.qf[ks][n] = *(const bf16x8*)(c.lds + RG::q(T) + c.qoff[n][ks]);
    }
  }
  template <int T> static __device__ __forceinline__ void step(const Ctx& c, f32x4 (&acc)[TP][TQ], Fr& cur, Fr& nxt, St& st) {
    if constexpr (T + 1 < NKT) {
      WAIT_L(0);
      BAR();
      __builtin_amdgcn_sched_barrier(0);
      if constexpr (!STATS && T == 0) STAMP(1, 1);
      if constexpr (!STATS && T == 7) STAMP(1, 5);
      if constexpr (STATS && T == 0) STAMP(0, 1);
      readf<T + 1>(c, nxt);
    }
#pragma unroll
    for (int ks = 0; ks < 2; ++ks) {
#pragma unroll
      for (int m = 0; m < TP; ++m)
#pragma unroll
        for (int n = 0; n < TQ; ++n) acc[m][n] = __builtin_amdgcn_mfma_f32_16x16x32_bf16(cur.pf[ks][m], cur.qf[ks][n], acc[m][n], 0, 0, 0);
      if constexpr (STATS) {
        const bf16x8 ones = {0x3f80, 0x3f80, 0x3f80, 0x3f80, 0x3f80, 0x3f80, 0x3f80, 0x3f80};
#pragma unroll
        for (int m = 0; m < 2; ++m) {
          st.s1[m] = __builtin_amdgcn_mfma_f32_16x16x32_bf16(cur.pf[ks][m], ones, st.s1[m], 0, 0, 0);
          st.s2[m] = __builtin_amdgcn_mfma_f32_16x16x32_bf16(cur.pf[ks][m], cur.pf[ks][m], st.s2[m], 0, 0, 0);
        }
#pragma unroll
        for (int n = 0; n < TQ; ++n) st.sw[n] = __builtin_amdgcn_mfma_f32_16x16x32_bf16(ones, cur.qf[ks][n], st.sw[n], 0, 0, 0);
      }
    }
    if constexpr (T + 1 < NKT) {
#pragma unroll
      for (int i = 0; i < 2 * (TP + TQ); ++i) { __builtin_amdgcn_sched_group_barrier(0x100, 1, 0); __builtin_amdgcn_sched_group_barrier(0x8, 1, 0); }
    }
    __builtin_amdgcn_sched_barrier(0);
  }
  template <int... Ts>
  static __device__ __forceinline__ void steps(const Ctx& c, f32x4 (&acc)[TP][TQ], Fr& f0, Fr& f1, St& st, std::integer_sequence<int, Ts...>) {
    ((Ts & 1 ? step<Ts>(c, acc, f1, f0, st) : step<Ts>(c, acc, f0, f1, st)), ...);
  }
  static __device__ __forceinline__ void run(const char* lds, int wp, int wq, int rot, int l, f32x4 (&acc)[TP][TQ], St& st) {
    Ctx c; c.lds = lds;
    const int fr = l & 15, q = l >> 4;
#pragma unroll
    for (int ks = 0; ks < 2; ++ks) {
#pragma unroll
      for (int m = 0; m < TP; ++m) { const int row = wp * (BP / 2) + ((m + rot) & (TP - 1)) * 16 + fr; c.poff[m][ks] = row * 128 + (((ks * 4 + q) ^ ((row >> 1) & 7)) * 16); }
#pragma unroll
      for (int n = 0; n < TQ; ++n) { const int row = wq * (BQ / 2) + n * 16 + fr; c.qoff[n][ks] = row * 128 + (((ks * 4 + q) ^ ((row >> 1) & 7)) * 16); }
    }
    Fr f0, f1;
    BAR();
    readf<0>(c, f0);
    steps(c, acc, f0, f1, st, std::make_integer_sequence<int, NKT>{});
  }
};

struct L1Ctx { const u16* psrc[8]; const u16* qsrc[4]; char* lds; int w; };
template <int T> __device__ __forceinline__ void l1_issue(const L1Ctx& c) {
#pragma unroll
  for (int i = 0; i < 8; ++i) GLDS16(c.psrc[i] + T * 64, c.lds + Ring1::p(T) + (c.w * 8 + i) * 1024, 0);
#pragma unroll
  for (int i = 0; i < 4; ++i) GLDS16(c.qsrc[i] + T * 64, c.lds + Ring1::q(T) + (c.w * 4 + i) * 1024, 0);
}
template <int T> __device__ __forceinline__ void l1_step(const L1Ctx& c) {
  constexpr int upto = (T + 3 < 7) ? (T + 3) : 7;
  WAIT_V((upto - (T + 1)) * 12);
  BAR();
  if constexpr (T + 4 < 8) l1_issue<T + 4>(c);
}
template <int... Ts> __device__ __forceinline__ void l1_steps(const L1Ctx& c, std::integer_sequence<int, Ts...>) { (l1_step<Ts>(c), ...); }

struct L2Ctx { const u16* psrc[8]; const u16* qsrc[8]; char* lds; int w; };
template <int T> __device__ __forceinline__ void l2_issueP(const L2Ctx& c) {
#pragma unroll
  for (int i = 0; i < 8; ++i) GLDS16(c.psrc[i] + T * 64, c.lds + Ring2::p(T) + (c.w * 8 + i) * 1024, 16);
}
template <int T> __device__ __forceinline__ void l2_issueQ(const L2Ctx& c) {
#pragma unroll
  for (int i = 0; i < 8; ++i) GLDS16(c.qsrc[i] + T * 64, c.lds + Ring2::q(T) + (c.w * 8 + i) * 1024, 0);
}
constexpr int l2_cnt(int s) { return s < 5 ? 8 : 16; }
constexpr int l2_younger(int T) { int up = T + 4 < 15 ? T + 4 : 15; int n = 0; for (int s = T + 2; s <= up; ++s) n += l2_cnt(s); return n; }
template <int T> __device__ __forceinline__ void l2_step(const L2Ctx& c) {
  WAIT_V(l2_younger(T));
  BAR();
  if constexpr (T + 5 < 16) { l2_issueP<T + 5>(c); l2_issueQ<T + 5>(c); }
}
template <int... Ts> __device__ __forceinline__ void l2_steps(const L2Ctx& c, std::integer_sequence<int, Ts...>) { (l2_step<Ts>(c), ...); }

__device__ __forceinline__ u32x4 widen16(u32x2 a, u32x2 c) {
  const auto s0 = __builtin_amdgcn_permlane16_swap(a[0], c[0], false, false);
  const auto s1 = __builtin_amdgcn_permlane16_swap(a[1], c[1], false, false);
  return (u32x4){s0[0], s1[0], s0[1], s1[1]};
}

__device__ __forceinline__ void poll_words(const unsigned* p, unsigned tag) {
  for (int it = 0; it < SPIN_MAX; ++it) {
    const unsigned v = __hip_atomic_load((gu32*)p, RLX_AGENT);
    if (__all(v == tag)) break;
    __builtin_amdgcn_s_sleep(1);
  }
  __builtin_amdgcn_fence(__ATOMIC_ACQUIRE, "wavefront");
}

__device__ __forceinline__ unsigned add_pk(unsigned a, unsigned b) { return pack2(bflo(a) + bflo(b), bfhi(a) + bfhi(b)); }
__device__ __forceinline__ unsigned sub_pk(unsigned a, unsigned b) { return pack2(bflo(a) - bflo(b), bfhi(a) - bfhi(b)); }
template <class R>
__device__ __forceinline__ void load_h(const u32x4 xv, R rs, int t, int l, float (&h)[8]) {
  const unsigned o = (unsigned)(t * D + l * 8) * 2;
  const u32x4 y0 = __builtin_amdgcn_raw_buffer_load_b128(rs, WS_Y + o, 0, 16);
  const u32x4 y1 = __builtin_amdgcn_raw_buffer_load_b128(rs, WS_Y + 1u * S * D * 2 + o, 0, 16);
  const u32x4 y2 = __builtin_amdgcn_raw_buffer_load_b128(rs, WS_Y + 2u * S * D * 2 + o, 0, 16);
  const u32x4 y3 = __builtin_amdgcn_raw_buffer_load_b128(rs, WS_Y + 3u * S * D * 2 + o, 0, 16);
#pragma unroll
  for (int e = 0; e < 4; ++e) {
    const unsigned hv = add_pk(xv[e], sub_pk(add_pk(y0[e], y1[e]), add_pk(y2[e], y3[e])));
    h[2 * e] = bflo(hv); h[2 * e + 1] = bfhi(hv);
  }
}
__device__ __forceinline__ u32x4 layernorm8(const float (&h)[8]) {
  const float s = ((h[0] + h[1]) + (h[2] + h[3])) + ((h[4] + h[5]) + (h[6] + h[7]));
  const float mean = wave_sum(s) * (1.0f / D);
  float v = 0.f, v2 = 0.f;
#pragma unroll
  for (int e = 0; e < 4; ++e) { const float a = h[2 * e] - mean, c = h[2 * e + 1] - mean; v += a * a; v2 += c * c; }
  v = wave_sum(v + v2);
  const float mb = rbf(mean);
  const float sd = rbf(sqrtf(rbf(rbf(v * (1.0f / D)) + 1e-5f)));
  const float inv = 1.0f / sd;
  u32x4 o;
#pragma unroll
  for (int e = 0; e < 4; ++e) {
    const unsigned d = pack2(h[2 * e] - mb, h[2 * e + 1] - mb);
    const float a = bflo(d), c = bfhi(d);
    float qa = a * inv, qc = c * inv;
    qa = fmaf(fmaf(-qa, sd, a), inv, qa); qc = fmaf(fmaf(-qc, sd, c), inv, qc);
    o[e] = pack2(qa, qc);
  }
  return o;
}

__global__ __launch_bounds__(512) void fused(const u16* __restrict__ x, const u16* __restrict__ ws_re, const u16* __restrict__ ws_im,
                                             const u16* __restrict__ wd_re, const u16* __restrict__ wd_im, const u16* __restrict__ w_r,
                                             const u16* __restrict__ w_gate, const u16* __restrict__ w_up, const u16* __restrict__ w_down,
                                             u16* __restrict__ out, char* __restrict__ ws) {
  __shared__ __attribute__((aligned(1024))) char lds[163840];
  const int b = blockIdx.x, xcd = b & 7, j = b >> 3;
  const int cc = xcd >> 2, et = j & 3, tt = (xcd & 3) * 4 + ((j >> 2) & 3), kh = j >> 4;
  const int g1 = (cc * 4 + et) * 2 + kh;
  const int e1 = cc * 8 + et * 2 + (tt & 1), s1 = kh * 8 + (tt >> 1);
  const int tid = threadIdx.x, w = __builtin_amdgcn_readfirstlane(tid >> 6), l = tid & 63, fr = l & 15, q = l >> 4;
  const u16* wsb = cc ? ws_im : ws_re;
  const u16* wdb = cc ? wd_im : wd_re;
  const bool down = j >= 28;
  const int fb = xcd * 28 + j, db = xcd * 4 + (j - 28);
  const auto rs = __builtin_amdgcn_make_buffer_rsrc(ws, 0, WS_END, 0x00020000);
  unsigned tag;
  STAMP(0, 0);

  if (w < 4) {
    const int wp = w >> 1, wq = w & 1;
    unsigned r0 = 0;
    {
      const int row = tid >> 3, ln = tid & 7;
      const int xs_tile = ((j >> 3) & 1) * 8 + (xcd & 3) * 2 + ((j >> 2) & 1);
      const u16* p = j < 16 ? x + (size_t)(xs_tile * 128 + (j & 3) * 32 + row) * D + ln * 64 : wdb + (size_t)((j - 16) * 32 + row) * D + ln * 64;
      TOUCH(r0, p);
    }
    {
      using C1 = Cons<128, 64, 8, Ring1, true>;
      f32x4 acc[4][2] = {};
      C1::St st = {};
      C1::run(lds, wp, wq, 2 * wq, l, acc, st);
      STAMP(0, 2);
      float2* rst = (float2*)(lds + L_RST);
      if ((fr >> 2) == q) {
#pragma unroll
        for (int m = 0; m < 2; ++m) {
          const int jj = fr & 3;
          const float s1v = jj == 0 ? st.s1[m][0] : jj == 1 ? st.s1[m][1] : jj == 2 ? st.s1[m][2] : st.s1[m][3];
          const float s2v = jj == 0 ? st.s2[m][0] : jj == 1 ? st.s2[m][1] : jj == 2 ? st.s2[m][2] : st.s2[m][3];
          const float mean = s1v * (1.0f / D);
          const float var = fmaxf(s2v * (1.0f / D) - mean * mean, 0.f);
          const float sd = rbf(sqrtf(rbf(rbf(var) + 1e-5f)));
          rst[wp * 64 + (2 * wq + m) * 16 + fr] = make_float2(rbf(mean), 1.0f / sd);
        }
      }
      WAIT_L(0);
      BAR();
      tag = __builtin_amdgcn_readfirstlane(*(const unsigned*)(lds + L_TAG));
      u32x2 pk[4][2];
#pragma unroll
      for (int m = 0; m < 4; ++m) {
        const int p0 = wp * 64 + ((m + 2 * wq) & 3) * 16 + q * 4;
        const f32x4 sa = *(const f32x4*)(rst + p0), sb = *(const f32x4*)(rst + p0 + 2);
#pragma unroll
        for (int n = 0; n < 2; ++n) {
          const float sw = st.sw[n][0];
          const float v0 = (acc[m][n][0] - sa[0] * sw) * sa[1], v1 = (acc[m][n][1] - sa[2] * sw) * sa[3];
          const float v2 = (acc[m][n][2] - sb[0] * sw) * sb[1], v3 = (acc[m][n][3] - sb[2] * sw) * sb[3];
          pk[m][n] = (u32x2){pack2(v0, v1), pack2(v2, v3)};
        }
      }
      {
        const int chunk = (q >> 1) + 2 * (q & 1);
#pragma unroll
        for (int n = 0; n < 2; ++n)
#pragma unroll
          for (int mp = 0; mp < 2; ++mp) {
            const int row = wq * 32 + n * 16 + fr;
            const int scol = wp * 64 + ((2 * mp + 2 * wq) & 3) * 16 + chunk * 8;
            __builtin_amdgcn_raw_buffer_store_b128(widen16(pk[2 * mp][n], pk[2 * mp + 1][n]), rs,
                                                   WS_ABT + (unsigned)((e1 * 64 + row) * S + s1 * 128 + scol) * 2, 0, 16);
          }
      }
      STAMP(0, 3);
      WAIT_V(0);
      KEEP(r0);
      BAR();
      if (tid == 0) __hip_atomic_store((gu32*)(ws + WS_F1) + g1 * 32 + tt, tag, RLX_AGENT);
      STAMP(0, 4);
      BAR();
    }
    using C2 = Cons<128, 128, 16, Ring2, false>;
    f32x4 acc[4][4] = {};
    C2::St st;
    C2::run(lds, wp, wq, 0, l, acc, st);
    STAMP(1, 2);
    {
      const unsigned yo = WS_Y + (unsigned)(cc * 2 + kh) * S * D * 2;
      const int chunk = (q >> 1) + 2 * (q & 1);
#pragma unroll
      for (int n = 0; n < 4; ++n)
#pragma unroll
        for (int mp = 0; mp < 2; ++mp) {
          const u32x2 a = {pack2(acc[2 * mp][n][0], acc[2 * mp][n][1]), pack2(acc[2 * mp][n][2], acc[2 * mp][n][3])};
          const u32x2 c = {pack2(acc[2 * mp + 1][n][0], acc[2 * mp + 1][n][1]), pack2(acc[2 * mp + 1][n][2], acc[2 * mp + 1][n][3])};
          const int row = wq * 64 + n * 16 + fr;
          __builtin_amdgcn_raw_buffer_store_b128(widen16(a, c), rs, yo + (unsigned)((tt * 128 + row) * D + et * 128 + wp * 64 + mp * 32 + chunk * 8) * 2, 0, 16);
        }
    }
    STAMP(1, 3);
    WAIT_V(0);
  } else if (w < 6) {
    const int lw = w - 4;
    const int r8 = l >> 3, ch = l & 7;
    {
      L1Ctx c; c.lds = lds; c.w = lw;
      const u16* P = x + (size_t)s1 * 128 * D;
      const u16* Q = wdb + (size_t)(e1 & 7) * 64 * D;
#pragma unroll
      for (int i = 0; i < 8; ++i) { const int row = (lw * 8 + i) * 8 + r8; c.psrc[i] = P + (size_t)row * D + ((ch ^ ((row >> 1) & 7)) * 8); }
#pragma unroll
      for (int i = 0; i < 4; ++i) { const int row = (lw * 4 + i) * 8 + r8; c.qsrc[i] = Q + (size_t)row * D + ((ch ^ ((row >> 1) & 7)) * 8); }
      l1_issue<0>(c); l1_issue<1>(c); l1_issue<2>(c);
      WAIT_V(24);
      BAR();
      l1_issue<3>(c);
      l1_steps(c, std::make_integer_sequence<int, 7>{});
    }
    L2Ctx c2;
    c2.lds = lds; c2.w = lw;
    {
      const u16* P = (const u16*)(ws + WS_ABT) + (size_t)(cc * 512 + et * 128) * S + kh * 1024;
      const u16* Q = wsb + (size_t)tt * 128 * S + kh * 1024;
#pragma unroll
      for (int i = 0; i < 8; ++i) {
        const int row = (lw * 8 + i) * 8 + r8;
        const size_t o = (size_t)row * S + ((ch ^ ((row >> 1) & 7)) * 8);
        c2.psrc[i] = P + o; c2.qsrc[i] = Q + o;
      }
    }
    BAR();
    tag = __builtin_amdgcn_readfirstlane(*(const unsigned*)(lds + L_TAG));
    BAR();
    if (lw == 0) poll_words((const unsigned*)(ws + WS_F1) + g1 * 32 + fr, tag);
    STAMPW(1, 0, 4);
    BAR();
    l2_issueP<0>(c2); l2_issueP<1>(c2); l2_issueP<2>(c2);
    WAIT_V(16);
    BAR();
    l2_issueP<3>(c2); l2_issueP<4>(c2);
    l2_steps(c2, std::make_integer_sequence<int, 15>{});
  } else if (w == 6) {
    const int r8 = l >> 3, ch = l & 7;
    const unsigned ep = __hip_atomic_load((gu32*)(ws + WS_EP), RLX_AGENT);
    const u16* Q = wsb + (size_t)tt * 128 * S + kh * 1024;
    const u16* qa = Q + (size_t)r8 * S + ((ch ^ (r8 >> 1)) * 8);
    const u16* qb = Q + (size_t)(8 + r8) * S + ((ch ^ (4 + (r8 >> 1))) * 8);
    BAR();
#pragma unroll
    for (int T = 0; T < 10; ++T) {
      BAR();
      if (T == 0) { tag = __builtin_amdgcn_readfirstlane(ep * 1664525u + 1013904223u); *(unsigned*)(lds + L_TAG) = tag; WAIT_L(0); }
      if (T >= 4 && T < 9) {
#pragma unroll
        for (int i = 0; i < 8; ++i) {
          GLDS16(qa + (size_t)i * 16 * S + (T - 4) * 64, lds + L_QR + (T - 4) * 16384 + (2 * i) * 1024, 0);
          GLDS16(qb + (size_t)i * 16 * S + (T - 4) * 64, lds + L_QR + (T - 4) * 16384 + (2 * i + 1) * 1024, 0);
        }
      }
    }
    WAIT_V(0);
    BAR();
    const u16* wsrc = down ? w_down + (size_t)(l >> 1) * D + db * 16 + (l & 1) * 8 : ((l & 1) ? w_up : w_gate) + (size_t)(l >> 1) * F + fb * 8;
    const int wstride = down ? D : F;
#pragma unroll
    for (int T = 0; T < 15; ++T) {
      BAR();
      if (T >= 11 && (T == 11 || down)) {
#pragma unroll
        for (int i = 0; i < (T == 14 ? 8 : 16); ++i) {
          const int piece = (T - 11) * 16 + i;
          GLDS16(wsrc + (size_t)piece * 32 * wstride, lds + L_W + piece * 1024, 0);
        }
      }
    }
  } else {
    unsigned r[4] = {0, 0, 0, 0};
    const u16* wb = wsb + (size_t)((xcd & 3) * 512 + j * 16 + fr) * S + ((l >> 4) & 1) * 1024 + (l >> 5) * 64;
    BAR();
#pragma unroll
    for (int T = 0; T < 10; ++T) {
      BAR();
      if (T == 2) TOUCH(r[0], wb);
      if (T == 4) TOUCH(r[1], wb + 128);
      if (T == 6) TOUCH(r[0], wb + 256);
      if (T == 7) { TOUCH(r[1], wb + 384); tag = __builtin_amdgcn_readfirstlane(*(const unsigned*)(lds + L_TAG)); }
      if (T == 9) TOUCH(r[0], wb + 512);
    }
    BAR();
#pragma unroll
    for (int T = 0; T < 15; ++T) {
      BAR();
      if (T == 1) TOUCH(r[1], wb + 640);
      if (T == 3) TOUCH(r[0], wb + 768);
      if (T == 5) TOUCH(r[1], wb + 896);
      if (T >= 6 && T < 9) {
        const int li = j * 192 + (T - 6) * 64 + l;
        const char* p2;
        if (li < 4096) { const int mrow = (li & 2047) >> 2, c4 = li & 3; p2 = (const char*)((li < 2048 ? w_gate : w_up) + (size_t)mrow * F + xcd * 224 + c4 * 64); }
        else { const int rr = li - 4096; p2 = (const char*)(w_down + (size_t)(rr < F ? rr : 0) * D + xcd * 64); }
        TOUCH(r[2], p2);
      }
    }
    WAIT_V(0);
    KEEP(r[0]); KEEP(r[1]); KEEP(r[2]);
  }
  const int t_own = tt * 128 + g1 * 8 + w;
  const u32x4 wr = *(const u32x4*)(w_r + l * 8);
  const u32x4 x_own = *(const u32x4*)(x + (size_t)t_own * D + l * 8);
  {
    BAR();
    if (tid == 0) __hip_atomic_store((gu32*)(ws + WS_F2) + tt * 32 + g1, tag, RLX_AGENT);
    STAMP(1, 4);
    if (w == 0) poll_words((const unsigned*)(ws + WS_F2) + tt * 32 + fr, tag);
    BAR();
  }
  STAMP(2, 0);
  unsigned* misc = (unsigned*)(lds + L_MISC);
  u32x4 own;
  {
    float h[8];
    load_h(x_own, rs, t_own, l, h);
    const u32x4 hn = layernorm8(h);
    float r = 0.f, r2 = 0.f;
#pragma unroll
    for (int e = 0; e < 4; ++e) { r += bflo(hn[e]) * bflo(wr[e]); r2 += bfhi(hn[e]) * bfhi(wr[e]); }
    r = wave_sum(r + r2);
    const unsigned rb = f2bf(r);
    const unsigned ord = (rb & 0x8000u) ? (~rb & 0xffffu) : (rb | 0x8000u);
    if (l == 0) { ((u16*)misc)[w] = (u16)ord; if (w == 0) misc[8] = 0; }
    WAIT_L(0);
    BAR();
    if (tid < 4) __hip_atomic_store((gu64*)(ws + WS_GR) + (tt * 16 + g1) * 4 + tid, ((unsigned long long)tag << 32) | misc[tid], RLX_AGENT);
#pragma unroll
    for (int e = 0; e < 4; ++e) own[e] = pack2(h[2 * e] + bflo(hn[e]), h[2 * e + 1] + bfhi(hn[e]));
  }
  STAMP(2, 1);
  unsigned kk[4];
  {
    u32x4 a;
    for (int it = 0; it < SPIN_MAX; ++it) {
      asm volatile("" ::: "memory");
      a = __builtin_amdgcn_raw_buffer_load_b128(rs, WS_GR + (unsigned)tid * 16, 0, 16);
      if (__all(a[1] == tag && a[3] == tag)) break;
      __builtin_amdgcn_s_sleep(1);
    }
    __builtin_amdgcn_fence(__ATOMIC_ACQUIRE, "wavefront");
    const unsigned ib = 0xffffu - (unsigned)tid * 4;
    kk[0] = (a[0] << 16) | ib; kk[1] = (a[0] & 0xffff0000u) | (ib - 1); kk[2] = (a[2] << 16) | (ib - 2); kk[3] = (a[2] & 0xffff0000u) | (ib - 3);
  }
  STAMP(2, 2);
  {
    const unsigned m = wave_max_u32(umax(umax(kk[0], kk[1]), umax(kk[2], kk[3])));
    if (l == 0) misc[16 + w] = m;
  }
  WAIT_L(0);
  BAR();
  if (b == 0 && tid == 0) __hip_atomic_store((gu32*)(ws + WS_EP), tag, RLX_AGENT);
  {
    const u32x4 ma = *(const u32x4*)(misc + 16), mb2 = *(const u32x4*)(misc + 20);
    const unsigned lo = min(min(min(ma[0], ma[1]), min(ma[2], ma[3])), min(min(mb2[0], mb2[1]), min(mb2[2], mb2[3])));
    if (l < 8) misc[32 + w * 8 + l] = 0u;
    unsigned base = 0;
#pragma unroll
    for (int e = 0; e < 4; ++e) {
      const unsigned long long bm = __ballot(kk[e] >= lo);
      const unsigned pos = base + __builtin_amdgcn_mbcnt_hi((unsigned)(bm >> 32), __builtin_amdgcn_mbcnt_lo((unsigned)bm, 0u));
      if (kk[e] >= lo && pos < 8) misc[32 + w * 8 + pos] = kk[e];
      base += (unsigned)__builtin_popcountll(bm);
    }
    if (base > 8 && l == 0) misc[8] = 1u;
  }
  WAIT_L(0);
  BAR();
  unsigned key;
  bool sel_own;
  {
    const unsigned cv = misc[32 + l];
    const unsigned ovf = __builtin_amdgcn_readfirstlane(misc[8]);
    if (ovf == 0) {
      int rank = 0;
      unsigned long long bm = __ballot(cv != 0u);
      while (bm) {
        const int i = __builtin_ctzll(bm);
        bm &= bm - 1;
        rank += ((unsigned)__builtin_amdgcn_readlane((int)cv, i) > cv) ? 1 : 0;
      }
      const unsigned long long bw = __ballot(cv != 0u && rank == w);
      key = (unsigned)__builtin_amdgcn_readlane((int)cv, __builtin_ctzll(bw));
      sel_own = __any(cv != 0u && rank < 8 && (int)(cv & 0xffffu) == 0xffff - t_own);
    } else {
      unsigned mine = 0;
#pragma unroll 1
      for (int it = 0; it < 8; ++it) {
        const unsigned m = wave_max_u32(umax(umax(kk[0], kk[1]), umax(kk[2], kk[3])));
#pragma unroll
        for (int e = 0; e < 4; ++e) kk[e] = (kk[e] == m) ? 0u : kk[e];
        if (l == it) mine = m;
      }
      if (l < 8) misc[96 + w * 8 + l] = mine;
      WAIT_L(0);
      BAR();
      unsigned cv = misc[96 + l];
      key = 0; sel_own = false;
#pragma unroll 1
      for (int it = 0; it < 8; ++it) {
        const unsigned m = wave_max_u32(cv);
        if (it == w) key = m;
        sel_own |= (int)(m & 0xffffu) == 0xffff - t_own;
        cv = (cv == m) ? 0u : cv;
      }
    }
  }
  STAMP(2, 3);
  const int t_sel = 0xffff - (int)(key & 0xffffu);
  {
    float h[8];
    const u32x4 xs = *(const u32x4*)(x + (size_t)t_sel * D + l * 8);
    load_h(xs, rs, t_sel, l, h);
    if (!sel_own) *(u32x4*)(out + (size_t)t_own * D + l * 8) = own;
    const u32x4 hn = layernorm8(h);
    if (!down) {
      *(u32x4*)(lds + L_XS + w * XS_STRIDE + l * 16) = hn;
    } else {
      if ((l >> 1) == db) {
        float2* hh = (float2*)(lds + L_HH) + w * 16 + (l & 1) * 8;
#pragma unroll
        for (int e = 0; e < 4; ++e) { hh[2 * e] = make_float2(h[2 * e], bflo(hn[e])); hh[2 * e + 1] = make_float2(h[2 * e + 1], bfhi(hn[e])); }
      }
      if (l == 0) misc[24 + w] = key;
    }
  }
  STAMP(2, 4);
  float* red = (float*)(lds + L_RED);
  if (!down) {
    WAIT_L(0);
    BAR();
    f32x4 a4 = {};
#pragma unroll
    for (int i = 0; i < 2; ++i) {
      const int ks = w * 2 + i;
      const bf16x8 a = *(const bf16x8*)(lds + L_XS + (fr & 7) * XS_STRIDE + ks * 64 + q * 16);
      const int r0 = ks * 32 + q * 8 + (fr >> 2);
      const int coff = (fr & 3) * 8;
      const v4i16_t g0 = __builtin_amdgcn_ds_read_tr16_b64_v4i16((LAS v4i16_t*)(lds + L_W + r0 * 32 + coff));
      const v4i16_t g1v = __builtin_amdgcn_ds_read_tr16_b64_v4i16((LAS v4i16_t*)(lds + L_W + (r0 + 4) * 32 + coff));
      const bf16x8 bg = {g0[0], g0[1], g0[2], g0[3], g1v[0], g1v[1], g1v[2], g1v[3]};
      a4 = __builtin_amdgcn_mfma_f32_16x16x32_bf16(a, bg, a4, 0, 0, 0);
    }
    if (q < 2) {
#pragma unroll
      for (int jj = 0; jj < 4; ++jj) red[w * 128 + (q * 4 + jj) * 16 + fr] = a4[jj];
    }
    WAIT_L(0);
    BAR();
    if (tid < 64) {
      const int tok = tid >> 3, f = tid & 7;
      float g = 0.f, u = 0.f;
#pragma unroll
      for (int ww = 0; ww < 8; ++ww) { g += red[ww * 128 + tok * 16 + f]; u += red[ww * 128 + tok * 16 + 8 + f]; }
      g = rbf(g); u = rbf(u);
      g = rbf(g / (1.0f + expf(-g)));
      const unsigned a = f2bf(g * u);
      const unsigned hi = dpp_u<0xB1, 0xf>(a);
      if (!(f & 1)) __hip_atomic_store((gu64*)(ws + WS_GA) + fb * 32 + (tid >> 1), ((unsigned long long)tag << 32) | (a | (hi << 16)), RLX_AGENT);
    }
    STAMP(3, 0);
  } else {
    for (int it = 0; it < SPIN_MAX; ++it) {
      asm volatile("" ::: "memory");
      bool ok = true;
#pragma unroll
      for (int i = 0; i < 7; ++i) {
        const int rec = w * 28 + i * 4 + q;
        const u32x4 a = __builtin_amdgcn_raw_buffer_load_b128(rs, WS_GA + (unsigned)rec * 256 + fr * 16, 0, 16);
        ok &= (a[1] == tag) & (a[3] == tag);
        *(u32x2*)(lds + L_AS + (fr >> 1) * AS_STRIDE + rec * 16 + (fr & 1) * 8) = (u32x2){a[0], a[2]};
      }
      if (__all(ok)) break;
      __builtin_amdgcn_s_sleep(1);
    }
    STAMP(3, 1);
    WAIT_L(0);
    BAR();
    f32x4 a4 = {};
#pragma unroll
    for (int i = 0; i < 7; ++i) {
      const int ks = w * 7 + i;
      const bf16x8 a = *(const bf16x8*)(lds + L_AS + (fr & 7) * AS_STRIDE + ks * 64 + q * 16);
      const int r0 = ks * 32 + q * 8 + (fr >> 2);
      const int coff = (fr & 3) * 8;
      const v4i16_t g0 = __builtin_amdgcn_ds_read_tr16_b64_v4i16((LAS v4i16_t*)(lds + L_W + r0 * 32 + coff));
      const v4i16_t g1v = __builtin_amdgcn_ds_read_tr16_b64_v4i16((LAS v4i16_t*)(lds + L_W + (r0 + 4) * 32 + coff));
      const bf16x8 bg = {g0[0], g0[1], g0[2], g0[3], g1v[0], g1v[1], g1v[2], g1v[3]};
      a4 = __builtin_amdgcn_mfma_f32_16x16x32_bf16(a, bg, a4, 0, 0, 0);
    }
    if (q < 2) {
#pragma unroll
      for (int jj = 0; jj < 4; ++jj) red[w * 128 + (q * 4 + jj) * 16 + fr] = a4[jj];
    }
    WAIT_L(0);
    BAR();
    if (tid < 128) {
      float s = 0.f;
#pragma unroll
      for (int ww = 0; ww < 8; ++ww) s += red[ww * 128 + tid];
      const unsigned k2 = misc[24 + (tid >> 4)];
      const int t = 0xffff - (int)(k2 & 0xffffu);
      const unsigned ord = k2 >> 16;
      const unsigned rb = (ord & 0x8000u) ? (ord & 0x7fffu) : (~ord & 0xffffu);
      const float sig = rbf(1.0f / (1.0f + expf(-bf2f(rb))));
      const float upd = rbf(rbf(s) * sig);
      const float2 hh = ((const float2*)(lds + L_HH))[tid];
      out[(size_t)t * D + db * 16 + (tid & 15)] = (u16)f2bf(hh.x + rbf(hh.y + upd));
    }
    STAMP(3, 2);
  }
}

extern "C" void kernel_launch(void* const* d_in, const int* in_sizes, int n_in, void* d_out, int out_size, void* d_ws,
                              size_t ws_size, hipStream_t stream) {
  fused<<<256, 512, 0, stream>>>((const u16*)d_in[0], (const u16*)d_in[1], (const u16*)d_in[2], (const u16*)d_in[3], (const u16*)d_in[4],
                                 (const u16*)d_in[6], (const u16*)d_in[7], (const u16*)d_in[8], (const u16*)d_in[9], (u16*)d_out, (char*)d_ws);
}
```

```cpp
#include <hip/hip_runtime.h>
#include <hip/hip_bf16.h>
#include <stdint.h>
#include <utility>

typedef unsigned short u16;
typedef __attribute__((ext_vector_type(8))) short bf16x8;
typedef __attribute__((ext_vector_type(4))) short v4i16_t;
typedef __attribute__((ext_vector_type(4))) float f32x4;
typedef __attribute__((ext_vector_type(4))) unsigned u32x4;
typedef __attribute__((ext_vector_type(2))) unsigned u32x2;
#define GAS __attribute__((address_space(1)))
#define LAS __attribute__((address_space(3)))
typedef GAS unsigned gu32;
typedef GAS unsigned long long gu64;
#define RLX_AGENT __ATOMIC_RELAXED, __HIP_MEMORY_SCOPE_AGENT

constexpr int S = 2048, D = 512, F = 1792;

__device__ __forceinline__ float bf2f(unsigned v) { return __uint_as_float(v << 16); }
__device__ __forceinline__ float bflo(unsigned v) { return __uint_as_float(v << 16); }
__device__ __forceinline__ float bfhi(unsigned v) { return __uint_as_float(v & 0xffff0000u); }
__device__ __forceinline__ unsigned f2bf(float f) { __bf16 b = (__bf16)f; return (unsigned)__builtin_bit_cast(u16, b); }
__device__ __forceinline__ float rbf(float f) { return bf2f(f2bf(f)); }
__device__ __forceinline__ unsigned pack2(float lo, float hi) { return f2bf(lo) | (f2bf(hi) << 16); }

#define WAIT_V(n) asm volatile("s_waitcnt vmcnt(%0)" ::"n"(n) : "memory")
#define WAIT_L(n) asm volatile("s_waitcnt lgkmcnt(%0)" ::"n"(n) : "memory")
#define BAR() __builtin_amdgcn_s_barrier()
#define GLDS16(src, dst, aux) __builtin_amdgcn_global_load_lds((const unsigned*)(src), (unsigned*)(dst), 16, 0, aux)
#define TOUCH(reg, ptr) asm volatile("global_load_dword %0, %1, off" : "+v"(reg) : "v"(ptr) : "memory")
#define TOUCH_NT(reg, ptr) asm volatile("global_load_dword %0, %1, off nt" : "+v"(reg) : "v"(ptr) : "memory")
#define KEEP(reg) asm volatile("" ::"v"(reg))

template <int CTRL, int RM> __device__ __forceinline__ float dpp_f(float v) {
  return __builtin_bit_cast(float, __builtin_amdgcn_update_dpp(0, __builtin_bit_cast(int, v), CTRL, RM, 0xf, true));
}
__device__ __forceinline__ float wave_sum(float v) {
  v += dpp_f<0x111, 0xf>(v); v += dpp_f<0x112, 0xf>(v); v += dpp_f<0x114, 0xf>(v); v += dpp_f<0x118, 0xf>(v);
  v += dpp_f<0x142, 0xa>(v); v += dpp_f<0x143, 0xc>(v);
  return __builtin_bit_cast(float, __builtin_amdgcn_readlane(__builtin_bit_cast(int, v), 63));
}
template <int CTRL, int RM> __device__ __forceinline__ unsigned dpp_u(unsigned v) {
  return (unsigned)__builtin_amdgcn_update_dpp(0, (int)v, CTRL, RM, 0xf, true);
}
__device__ __forceinline__ unsigned umax(unsigned a, unsigned b) { return a > b ? a : b; }
__device__ __forceinline__ unsigned wave_max_u32(unsigned v) {
  v = umax(v, dpp_u<0x111, 0xf>(v)); v = umax(v, dpp_u<0x112, 0xf>(v)); v = umax(v, dpp_u<0x114, 0xf>(v)); v = umax(v, dpp_u<0x118, 0xf>(v));
  v = umax(v, dpp_u<0x142, 0xa>(v)); v = umax(v, dpp_u<0x143, 0xc>(v));
  return (unsigned)__builtin_amdgcn_readlane((int)v, 63);
}

#define STAMP_DECL
#define STAMPW(k, i, wv)
#define STAMP_FLUSH
#define STAMP(k, i) STAMPW(k, i, 0)

constexpr unsigned WS_ABT = 0;
constexpr unsigned WS_Y = WS_ABT + 1024u * 2048 * 2;
constexpr unsigned WS_F1 = WS_Y + 4u * 2048 * 512 * 2;
constexpr unsigned WS_F2 = WS_F1 + 4096;
constexpr unsigned WS_GR = WS_F2 + 2048;
constexpr unsigned WS_GA = WS_GR + 1024 * 8;
constexpr unsigned WS_EP = WS_GA + 224 * 256;
constexpr unsigned WS_HN = WS_EP + 256;
constexpr unsigned WS_END = WS_HN + 2048u * 2048;

constexpr int SPIN_MAX = 1 << 18;
#define AUX1 0
#define CPF 1
#define FMODE 1
#define HNPUB 0
#define QMODE 0
#define NPLAIN 8
#define T1MODE 1
#define FFNSTEP 2

constexpr int L_QR = 81920;
constexpr int L_RST = 65536;
constexpr int L_TAG = 66560;
constexpr int L_W = 16384;
constexpr int L_T2 = 131072;
constexpr int XS_STRIDE = 1024 + 16;
constexpr int L_XS = 81920;
constexpr int L_RED = L_XS + 8 * XS_STRIDE;
constexpr int L_MISC = L_RED + 8 * 128 * 4;
constexpr int L_HH = L_MISC + 1024;
constexpr int AS_STRIDE = F * 2 + 16;
constexpr int L_AS = L_HH + 1024;
static_assert(L_AS + 8 * AS_STRIDE <= L_T2, "lds");

struct Ring2 {
  static constexpr int p(int T) { return (T % 5) * 16384; }
  static constexpr int q(int T) { return L_QR + ((T + 3) % 5) * 16384; }
};
constexpr int koff(int T) { return T < 8 ? T * 128 : (T - 8) * 128 + 64; }
constexpr int x1slot(int s) { return L_QR + (s % 5) * 16384; }

template <int BP, int BQ, int NKT, class RG, bool STATS>
struct Cons {
  static constexpr int TP = BP / 32, TQ = BQ / 32;
  struct Ctx { const char* lds; int poff[TP][2]; int qoff[TQ][2]; };
  struct Fr { bf16x8 pf[2][TP], qf[2][TQ]; };
  struct St { f32x4 s1[2], s2[2], sw[TQ]; };
  template <int T> static __device__ __forceinline__ void readf(const Ctx& c, Fr& f) {
#pragma unroll
    for (int ks = 0; ks < 2; ++ks) {
#pragma unroll
      for (int m = 0; m < TP; ++m) f.pf[ks][m] = *(const bf16x8*)(c.lds + RG::p(T) + c.poff[m][ks]);
#pragma unroll
      for (int n = 0; n < TQ; ++n) f.qf[ks][n] = *(const bf16x8*)(c.lds + RG::q(T) + c.qoff[n][ks]);
    }
  }
  template <int T> static __device__ __forceinline__ void step(const Ctx& c, f32x4 (&acc)[TP][TQ], Fr& cur, Fr& nxt, St& st) {
    if constexpr (T + 1 < NKT) {
      WAIT_L(0);
      BAR();
      __builtin_amdgcn_sched_barrier(0);
      readf<T + 1>(c, nxt);
    }
    if constexpr (T + 1 == NKT) return;
#pragma unroll
    for (int ks = 0; ks < 2; ++ks) {
#pragma unroll
      for (int m = 0; m < TP; ++m)
#pragma unroll
        for (int n = 0; n < TQ; ++n) acc[m][n] = __builtin_amdgcn_mfma_f32_16x16x32_bf16(cur.pf[ks][m], cur.qf[ks][n], acc[m][n], 0, 0, 0);
      if constexpr (STATS) {
        const bf16x8 ones = {0x3f80, 0x3f80, 0x3f80, 0x3f80, 0x3f80, 0x3f80, 0x3f80, 0x3f80};
#pragma unroll
        for (int m = 0; m < 2; ++m) {
          st.s1[m] = __builtin_amdgcn_mfma_f32_16x16x32_bf16(cur.pf[ks][m], ones, st.s1[m], 0, 0, 0);
          st.s2[m] = __builtin_amdgcn_mfma_f32_16x16x32_bf16(cur.pf[ks][m], cur.pf[ks][m], st.s2[m], 0, 0, 0);
        }
#pragma unroll
        for (int n = 0; n < TQ; ++n) st.sw[n] = __builtin_amdgcn_mfma_f32_16x16x32_bf16(ones, cur.qf[ks][n], st.sw[n], 0, 0, 0);
      }
    }
    if constexpr (T + 1 < NKT) {
#pragma unroll
      for (int i = 0; i < 2 * (TP + TQ); ++i) { __builtin_amdgcn_sched_group_barrier(0x100, 1, 0); __builtin_amdgcn_sched_group_barrier(0x8, 1, 0); }
    }
    __builtin_amdgcn_sched_barrier(0);
  }
  template <int... Ts>
  static __device__ __forceinline__ void steps(const Ctx& c, f32x4 (&acc)[TP][TQ], Fr& f0, Fr& f1, St& st, std::integer_sequence<int, Ts...>) {
    ((Ts & 1 ? step<Ts>(c, acc, f1, f0, st) : step<Ts>(c, acc, f0, f1, st)), ...);
  }
  static __device__ __forceinline__ void run(const char* lds, int wp, int wq, int rot, int l, f32x4 (&acc)[TP][TQ], St& st, Fr& f1) {
    Ctx c; c.lds = lds;
    const int fr = l & 15, q = l >> 4;
#pragma unroll
    for (int ks = 0; ks < 2; ++ks) {
#pragma unroll
      for (int m = 0; m < TP; ++m) { const int row = wp * (BP / 2) + ((m + rot) & (TP - 1)) * 16 + fr; c.poff[m][ks] = row * 128 + (((ks * 4 + q) ^ ((row >> 1) & 7)) * 16); }
#pragma unroll
      for (int n = 0; n < TQ; ++n) { const int row = wq * (BQ / 2) + n * 16 + fr; c.qoff[n][ks] = row * 128 + (((ks * 4 + q) ^ ((row >> 1) & 7)) * 16); }
    }
    Fr f0;
    BAR();
    readf<0>(c, f0);
    steps(c, acc, f0, f1, st, std::make_integer_sequence<int, NKT>{});
  }
};

struct L1Ctx { const u16* xsrc[4]; const u16* wsrc[4]; char* lds; int w; };
template <int s> __device__ __forceinline__ void l1_issue(const L1Ctx& c) {
#pragma unroll
  for (int h = 0; h < 2; ++h) {
#pragma unroll
    for (int i = 0; i < 4; ++i) GLDS16(c.xsrc[i] + (size_t)(s >> 2) * 64 * D + (s & 3) * 128 + h * 64, c.lds + x1slot(s) + h * 8192 + (c.w * 4 + i) * 1024, AUX1);
    if constexpr (s < 4) {
#pragma unroll
      for (int i = 0; i < 4; ++i) GLDS16(c.wsrc[i] + s * 128 + h * 64, c.lds + (2 * s + h) * 8192 + (c.w * 4 + i) * 1024, AUX1);
    }
  }
}
struct Fr1 { bf16x8 pf[4][2], qf[4][2]; };
struct C1Ctx { const char* lds; int poff[2][2]; int qoff[2][2]; };
template <int s> __device__ __forceinline__ void c1_read(const C1Ctx& c, Fr1& f) {
#pragma unroll
  for (int ks = 0; ks < 4; ++ks) {
#pragma unroll
    for (int m = 0; m < 2; ++m) f.pf[ks][m] = *(const bf16x8*)(c.lds + x1slot(s) + (ks >> 1) * 8192 + c.poff[m][ks & 1]);
#pragma unroll
    for (int n = 0; n < 2; ++n) f.qf[ks][n] = *(const bf16x8*)(c.lds + (2 * (s & 3) + (ks >> 1)) * 8192 + c.qoff[n][ks & 1]);
  }
}
struct Acc1 { f32x4 acc[2][2], s1, s2; };
template <int s> __device__ __forceinline__ void c1_step(const C1Ctx& c, Acc1& a, f32x4 (&sw)[2], Fr1& cur, Fr1& nxt) {
  if constexpr (s == 7) WAIT_V(0);
  WAIT_L(0);
  BAR();
  __builtin_amdgcn_sched_barrier(0);
  if constexpr (s + 1 < 8) c1_read<s + 1>(c, nxt);
  const bf16x8 ones = {0x3f80, 0x3f80, 0x3f80, 0x3f80, 0x3f80, 0x3f80, 0x3f80, 0x3f80};
#pragma unroll
  for (int ks = 0; ks < 4; ++ks) {
#pragma unroll
    for (int m = 0; m < 2; ++m)
#pragma unroll
      for (int n = 0; n < 2; ++n) a.acc[m][n] = __builtin_amdgcn_mfma_f32_16x16x32_bf16(cur.pf[ks][m], cur.qf[ks][n], a.acc[m][n], 0, 0, 0);
    a.s1 = __builtin_amdgcn_mfma_f32_16x16x32_bf16(cur.pf[ks][0], ones, a.s1, 0, 0, 0);
    a.s2 = __builtin_amdgcn_mfma_f32_16x16x32_bf16(cur.pf[ks][0], cur.pf[ks][0], a.s2, 0, 0, 0);
    if constexpr (s < 4) {
#pragma unroll
      for (int n = 0; n < 2; ++n) sw[n] = __builtin_amdgcn_mfma_f32_16x16x32_bf16(ones, cur.qf[ks][n], sw[n], 0, 0, 0);
    }
  }
  if constexpr (s + 1 < 8) {
#pragma unroll
    for (int i = 0; i < 16; ++i) { __builtin_amdgcn_sched_group_barrier(0x100, 1, 0); __builtin_amdgcn_sched_group_barrier(0x8, 1, 0); }
  }
  __builtin_amdgcn_sched_barrier(0);
}

struct L2Ctx { const u16* psrc[8]; const u16* qsrc[8]; char* lds; int w; };
template <int T> __device__ __forceinline__ void l2_issueP(const L2Ctx& c) {
#pragma unroll
  for (int i = 0; i < 8; ++i) GLDS16(c.psrc[i] + koff(T), c.lds + Ring2::p(T) + (c.w * 8 + i) * 1024, 16);
}
template <int T, int AUX> __device__ __forceinline__ void l2_issueQ(const L2Ctx& c) {
#pragma unroll
  for (int i = 0; i < 8; ++i) GLDS16(c.qsrc[i] + koff(T), c.lds + Ring2::q(T) + (c.w * 8 + i) * 1024, AUX);
}
constexpr int l2_cnt(int s) { return s < 5 ? 8 : 16; }
constexpr int l2_younger(int T) { int up = T + 4 < 15 ? T + 4 : 15; int n = 0; for (int s = T + 2; s <= up; ++s) n += l2_cnt(s); return n; }
template <int T, int AUX> __device__ __forceinline__ void l2_step(const L2Ctx& c) {
  WAIT_V(l2_younger(T));
  BAR();
  if constexpr (T + 5 < 16) { l2_issueP<T + 5>(c); l2_issueQ<T + 5, AUX>(c); }
}
template <int AUX, int... Ts> __device__ __forceinline__ void l2_steps(const L2Ctx& c, std::integer_sequence<int, Ts...>) { (l2_step<Ts, AUX>(c), ...); }

__device__ __forceinline__ u32x4 widen16(u32x2 a, u32x2 c) {
  const auto s0 = __builtin_amdgcn_permlane16_swap(a[0], c[0], false, false);
  const auto s1 = __builtin_amdgcn_permlane16_swap(a[1], c[1], false, false);
  return (u32x4){s0[0], s1[0], s0[1], s1[1]};
}

__device__ __forceinline__ void poll_words(const unsigned* p, unsigned tag) {
  for (int it = 0; it < SPIN_MAX; ++it) {
    const unsigned v = __hip_atomic_load((gu32*)p, RLX_AGENT);
    if (__all(v == tag)) break;
    __builtin_amdgcn_s_sleep(1);
  }
  __builtin_amdgcn_fence(__ATOMIC_ACQUIRE, "wavefront");
}

__device__ __forceinline__ unsigned add_pk(unsigned a, unsigned b) { return pack2(bflo(a) + bflo(b), bfhi(a) + bfhi(b)); }
__device__ __forceinline__ unsigned sub_pk(unsigned a, unsigned b) { return pack2(bflo(a) - bflo(b), bfhi(a) - bfhi(b)); }
template <class R>
__device__ __forceinline__ void load_h(const u32x4 xv, R rs, int t, int l, float (&h)[8]) {
  const unsigned o = (unsigned)(t * D + l * 8) * 2;
  const u32x4 y0 = __builtin_amdgcn_raw_buffer_load_b128(rs, WS_Y + o, 0, 16);
  const u32x4 y1 = __builtin_amdgcn_raw_buffer_load_b128(rs, WS_Y + 1u * S * D * 2 + o, 0, 16);
  const u32x4 y2 = __builtin_amdgcn_raw_buffer_load_b128(rs, WS_Y + 2u * S * D * 2 + o, 0, 16);
  const u32x4 y3 = __builtin_amdgcn_raw_buffer_load_b128(rs, WS_Y + 3u * S * D * 2 + o, 0, 16);
#pragma unroll
  for (int e = 0; e < 4; ++e) {
    const unsigned hv = add_pk(xv[e], sub_pk(add_pk(y0[e], y1[e]), add_pk(y2[e], y3[e])));
    h[2 * e] = bflo(hv); h[2 * e + 1] = bfhi(hv);
  }
}
__device__ __forceinline__ u32x4 layernorm8(const float (&h)[8]) {
  const float pv = __builtin_bit_cast(float, __builtin_amdgcn_readfirstlane(__builtin_bit_cast(int, h[0])));
  float s = 0.f, s2 = 0.f, v = 0.f, v2 = 0.f;
#pragma unroll
  for (int e = 0; e < 4; ++e) { const float a = h[2 * e] - pv, c = h[2 * e + 1] - pv; s += a; s2 += c; v += a * a; v2 += c * c; }
  const float md = wave_sum(s + s2) * (1.0f / D);
  v = fmaxf(wave_sum(v + v2) - md * md * (float)D, 0.f);
  const float mean = pv + md;
  const float mb = rbf(mean);
  const float sd = rbf(sqrtf(rbf(rbf(v * (1.0f / D)) + 1e-5f)));
  const float inv = 1.0f / sd;
  u32x4 o;
#pragma unroll
  for (int e = 0; e < 4; ++e) {
    const unsigned d = pack2(h[2 * e] - mb, h[2 * e + 1] - mb);
    const float a = bflo(d), c = bfhi(d);
    float qa = a * inv, qc = c * inv;
    qa = fmaf(fmaf(-qa, sd, a), inv, qa); qc = fmaf(fmaf(-qc, sd, c), inv, qc);
    o[e] = pack2(qa, qc);
  }
  return o;
}

__global__ __launch_bounds__(512) void fused(const u16* __restrict__ x, const u16* __restrict__ ws_re, const u16* __restrict__ ws_im,
                                             const u16* __restrict__ wd_re, const u16* __restrict__ wd_im, const u16* __restrict__ w_r,
                                             const u16* __restrict__ w_gate, const u16* __restrict__ w_up, const u16* __restrict__ w_down,
                                             u16* __restrict__ out, char* __restrict__ ws) {
  __shared__ __attribute__((aligned(1024))) char lds[163840];
  const int b = blockIdx.x, xcd = b & 7, j = b >> 3;
  const int cc = xcd >> 2, et = j & 3, tt = (xcd & 3) * 4 + ((j >> 2) & 3), kh = j >> 4;
  const int g1 = (cc * 4 + et) * 2 + kh;
  const int e1 = cc * 8 + et * 2 + (tt & 1), s1 = kh * 8 + (tt >> 1);
  const int tid = threadIdx.x, w = __builtin_amdgcn_readfirstlane(tid >> 6), l = tid & 63, fr = l & 15, q = l >> 4;
  const u16* wsb = cc ? ws_im : ws_re;
  const u16* wdb = cc ? wd_im : wd_re;
  const bool down = j >= 28;
  const int fb = xcd * 28 + j, db = xcd * 4 + (j - 28);
  const auto rs = __builtin_amdgcn_make_buffer_rsrc(ws, 0, WS_END, 0x00020000);
  unsigned tag;
  STAMP_DECL;
  STAMP(0, 0);
  unsigned long long code_lo; unsigned code_len;
  asm volatile("s_getpc_b64 %0\n.Lfused_code_lo:\n\ts_mov_b32 %1, .Lfused_code_hi-.Lfused_code_lo" : "=s"(code_lo), "=s"(code_len));

  if (w < 4) {
    const int wp = w >> 1, wq = w & 1;
    unsigned r0 = 0;
    {
      const int row = tid >> 3, ln = tid & 7;
      const int xs_tile = ((j >> 3) & 1) * 8 + (xcd & 3) * 2 + ((j >> 2) & 1);
      const u16* p = j < 16 ? x + (size_t)(xs_tile * 128 + (j & 3) * 32 + row) * D + ln * 64 : wdb + (size_t)((j - 16) * 32 + row) * D + ln * 64;
      if (T1MODE == 1) TOUCH(r0, p);
      if (T1MODE == 2) TOUCH_NT(r0, p);
    }
    {
      C1Ctx c; c.lds = lds;
#pragma unroll
      for (int ks = 0; ks < 2; ++ks) {
#pragma unroll
        for (int m = 0; m < 2; ++m) { const int row = wp * 32 + (m ^ wq) * 16 + fr; c.poff[m][ks] = row * 128 + (((ks * 4 + q) ^ ((row >> 1) & 7)) * 16); }
#pragma unroll
        for (int n = 0; n < 2; ++n) { const int row = wq * 32 + n * 16 + fr; c.qoff[n][ks] = row * 128 + (((ks * 4 + q) ^ ((row >> 1) & 7)) * 16); }
      }
      f32x4 sw[2] = {};
      Fr1 f0, f1;
      auto epilogue = [&](const Acc1& a, int pass) {
        float2* rst = (float2*)(lds + L_RST) + pass * 64;
        if ((fr >> 2) == q) {
          const int jj = fr & 3;
          const float s1v = jj == 0 ? a.s1[0] : jj == 1 ? a.s1[1] : jj == 2 ? a.s1[2] : a.s1[3];
          const float s2v = jj == 0 ? a.s2[0] : jj == 1 ? a.s2[1] : jj == 2 ? a.s2[2] : a.s2[3];
          const float mean = s1v * (1.0f / D);
          const float var = fmaxf(s2v * (1.0f / D) - mean * mean, 0.f);
          const float sd = rbf(sqrtf(rbf(rbf(var) + 1e-5f)));
          rst[wp * 32 + wq * 16 + fr] = make_float2(rbf(mean), 1.0f / sd);
        }
        WAIT_L(0);
        BAR();
        u32x2 pk[2][2];
#pragma unroll
        for (int m = 0; m < 2; ++m) {
          const int p0 = wp * 32 + (m ^ wq) * 16 + q * 4;
          const f32x4 sa = *(const f32x4*)(rst + p0), sb = *(const f32x4*)(rst + p0 + 2);
#pragma unroll
          for (int n = 0; n < 2; ++n) {
            const float swv = sw[n][0];
            const float v0 = (a.acc[m][n][0] - sa[0] * swv) * sa[1], v1 = (a.acc[m][n][1] - sa[2] * swv) * sa[3];
            const float v2 = (a.acc[m][n][2] - sb[0] * swv) * sb[1], v3 = (a.acc[m][n][3] - sb[2] * swv) * sb[3];
            pk[m][n] = (u32x2){pack2(v0, v1), pack2(v2, v3)};
          }
        }
        const int scol = s1 * 128 + pass * 64 + wp * 32 + ((q & 1) ^ wq) * 16 + (q >> 1) * 8;
#pragma unroll
        for (int n = 0; n < 2; ++n)
          __builtin_amdgcn_raw_buffer_store_b128(widen16(pk[0][n], pk[1][n]), rs, WS_ABT + (unsigned)((e1 * 64 + wq * 32 + n * 16 + fr) * S + scol) * 2, 0, 16);
      };
      BAR();
      c1_read<0>(c, f0);
      {
        Acc1 a = {};
        c1_step<0>(c, a, sw, f0, f1); c1_step<1>(c, a, sw, f1, f0); c1_step<2>(c, a, sw, f0, f1); c1_step<3>(c, a, sw, f1, f0);
        STAMP(0, 2);
        epilogue(a, 0);
        STAMP(0, 3);
      }
      {
        Acc1 a = {};
        c1_step<4>(c, a, sw, f0, f1); c1_step<5>(c, a, sw, f1, f0); c1_step<6>(c, a, sw, f0, f1); c1_step<7>(c, a, sw, f1, f0);
        STAMP(0, 4);
        epilogue(a, 1);
        STAMP(0, 5);
      }
      tag = __builtin_amdgcn_readfirstlane(*(const unsigned*)(lds + L_TAG));
      WAIT_V(0);
      KEEP(r0);
      STAMP(0, 6);
      BAR();
    }
    using C2 = Cons<128, 128, 16, Ring2, false>;
    f32x4 acc[4][4] = {};
    C2::St st;
    C2::Fr fl;
    C2::run(lds, wp, wq, 0, l, acc, st, fl);
    STAMP(1, 2);
    {
      const unsigned yo = WS_Y + (unsigned)(cc * 2 + kh) * S * D * 2;
      const int chunk = (q >> 1) + 2 * (q & 1);
#pragma unroll
      for (int n = 0; n < 4; ++n) {
#pragma unroll
        for (int ks = 0; ks < 2; ++ks)
#pragma unroll
          for (int m = 0; m < 4; ++m) acc[m][n] = __builtin_amdgcn_mfma_f32_16x16x32_bf16(fl.pf[ks][m], fl.qf[ks][n], acc[m][n], 0, 0, 0);
#pragma unroll
        for (int mp = 0; mp < 2; ++mp) {
          const u32x2 a = {pack2(acc[2 * mp][n][0], acc[2 * mp][n][1]), pack2(acc[2 * mp][n][2], acc[2 * mp][n][3])};
          const u32x2 c = {pack2(acc[2 * mp + 1][n][0], acc[2 * mp + 1][n][1]), pack2(acc[2 * mp + 1][n][2], acc[2 * mp + 1][n][3])};
          const int row = wq * 64 + n * 16 + fr;
          __builtin_amdgcn_raw_buffer_store_b128(widen16(a, c), rs, yo + (unsigned)((tt * 128 + row) * D + et * 128 + wp * 64 + mp * 32 + chunk * 8) * 2, 0, 16);
        }
      }
    }
    STAMP(1, 3);
    WAIT_V(0);
    STAMP(1, 6);
  } else if (w < 6) {
    const int lw = w - 4;
    const int r8 = l >> 3, ch = l & 7;
    {
      L1Ctx c; c.lds = lds; c.w = lw;
      const u16* P = x + (size_t)s1 * 128 * D;
      const u16* Q = wdb + (size_t)(e1 & 7) * 64 * D;
#pragma unroll
      for (int i = 0; i < 4; ++i) {
        const int row = (lw * 4 + i) * 8 + r8;
        const size_t o = (size_t)row * D + ((ch ^ ((row >> 1) & 7)) * 8);
        c.xsrc[i] = P + o; c.wsrc[i] = Q + o;
      }
      l1_issue<0>(c); l1_issue<1>(c);
      WAIT_V(16); STAMPW(0, 1, 4); BAR();
      l1_issue<2>(c);
      WAIT_V(16); BAR();
      l1_issue<3>(c);
      WAIT_V(16); BAR();
      l1_issue<4>(c); l1_issue<5>(c);
      WAIT_V(16); BAR();
      l1_issue<6>(c); l1_issue<7>(c);
      WAIT_V(24); BAR();
      BAR();
      WAIT_V(16); BAR();
      WAIT_V(8); BAR();
      WAIT_V(0); BAR();
    }
    L2Ctx c2;
    c2.lds = lds; c2.w = lw;
    {
      const u16* P = (const u16*)(ws + WS_ABT) + (size_t)(cc * 512 + et * 128) * S + kh * 1024;
      const u16* Q = wsb + (size_t)tt * 128 * S + kh * 1024;
#pragma unroll
      for (int i = 0; i < 8; ++i) {
        const int row = (lw * 8 + i) * 8 + r8;
        const size_t o = (size_t)row * S + ((ch ^ ((row >> 1) & 7)) * 8);
        c2.psrc[i] = P + o; c2.qsrc[i] = Q + o;
      }
    }
    tag = __builtin_amdgcn_readfirstlane(*(const unsigned*)(lds + L_TAG));
    BAR();
    BAR();
    poll_words((const unsigned*)(ws + WS_F1) + g1 * 32 + fr, tag);
    STAMPW(1, 0, 4);
    l2_issueP<0>(c2); l2_issueP<1>(c2); l2_issueP<2>(c2);
    BAR();
    WAIT_V(16);
    STAMPW(1, 1, 4);
    BAR();
    l2_issueP<3>(c2); l2_issueP<4>(c2);
    l2_steps<0>(c2, std::make_integer_sequence<int, 15>{});
  } else if (w == 6) {
    const int r8 = l >> 3, ch = l & 7;
    const unsigned ep = __hip_atomic_load((gu32*)(ws + WS_EP), RLX_AGENT);
    const u16* Q = wsb + (size_t)tt * 128 * S + kh * 1024;
    const u16* qa = Q + (size_t)r8 * S + ((ch ^ (r8 >> 1)) * 8);
    const u16* qb = Q + (size_t)(8 + r8) * S + ((ch ^ (4 + (r8 >> 1))) * 8);
    auto issueQ = [&](int T) {
#pragma unroll
      for (int i = 0; i < 8; ++i) {
        if (QMODE == 0 || (QMODE == 1 && 2 * i < NPLAIN)) {
          GLDS16(qa + (size_t)i * 16 * S + T * 128, lds + L_QR + ((T + 3) % 5) * 16384 + (2 * i) * 1024, 0);
          GLDS16(qb + (size_t)i * 16 * S + T * 128, lds + L_QR + ((T + 3) % 5) * 16384 + (2 * i + 1) * 1024, 0);
        } else {
          GLDS16(qa + (size_t)i * 16 * S + T * 128, lds + L_QR + ((T + 3) % 5) * 16384 + (2 * i) * 1024, 2);
          GLDS16(qb + (size_t)i * 16 * S + T * 128, lds + L_QR + ((T + 3) % 5) * 16384 + (2 * i + 1) * 1024, 2);
        }
      }
    };
    BAR(); BAR();
    tag = __builtin_amdgcn_readfirstlane(ep * 1664525u + 1013904223u);
    *(unsigned*)(lds + L_TAG) = tag;
    WAIT_L(0);
    BAR(); BAR(); BAR();
    BAR();
    BAR(); issueQ(0);
    BAR(); issueQ(1);
    BAR(); issueQ(2);
    BAR(); BAR();
    BAR(); issueQ(3); issueQ(4);
    WAIT_V(32);
    BAR();
    const unsigned* fB = (const unsigned*)(ws + WS_F1) + 512 + g1 * 32 + fr;
    BAR(); BAR();
    const unsigned vB = __hip_atomic_load((gu32*)fB, RLX_AGENT);
    WAIT_V(17);
    BAR();
    if (!__all(vB == tag)) poll_words(fB, tag);
    WAIT_V(0);
    BAR();
#pragma unroll
    for (int T = 4; T < 15; ++T) BAR();
  } else {
    unsigned r[4] = {0, 0, 0, 0};
    constexpr int LPI = 64 / (2 * NPLAIN) > 0 ? 64 / (2 * NPLAIN) : 1;
    const int pr = j * NPLAIN + (l % NPLAIN);
    const int prow = (pr / (NPLAIN * 8)) * 128 + (pr % (NPLAIN * 8));
    const u16* wb = wsb + (size_t)((xcd & 3) * 512 + prow) * S + ((l / NPLAIN) & 1) * 1024 + (l / (2 * NPLAIN)) * 128;
#define TW(i) TOUCH(r[(i) & 1], wb + ((i) % (8 / LPI)) * (LPI * 128) + ((i) / (8 / LPI)) * 64)
    auto ffn_ptr = [&](int i) -> const char* {
      const int li = j * 192 + i * 64 + l;
      if (li < 4096) { const int mrow = (li & 2047) >> 2, c4 = li & 3; return (const char*)((li < 2048 ? w_gate : w_up) + (size_t)mrow * F + xcd * 224 + c4 * 64); }
      const int rr = li - 4096;
      return (const char*)(w_down + (size_t)(rr < F ? rr : 0) * D + xcd * 64);
    };
    if (CPF) {
      const unsigned off = (unsigned)(j * 16 + fr) * 128u;
      const char* cp = (const char*)code_lo + (off < code_len ? off : 0u);
      if (l < 16) TOUCH(r[3], cp);
    }
    const char* fp0 = ffn_ptr(0); const char* fp1 = ffn_ptr(1); const char* fp2 = ffn_ptr(2);
    BAR(); if (QMODE == 1) TW(0); if (FMODE == 2) TOUCH(r[2], fp0);
    BAR(); if (FMODE == 2) TOUCH(r[2], fp1);
    BAR(); if (QMODE == 1) TW(1); if (FMODE == 1) TOUCH(r[2], fp0); if (FMODE == 2) TOUCH(r[2], fp2);
    tag = __builtin_amdgcn_readfirstlane(*(const unsigned*)(lds + L_TAG));
    BAR(); if (FMODE == 1) TOUCH(r[2], fp1);
    BAR(); if (QMODE == 1) TW(2); if (FMODE == 1) TOUCH(r[2], fp2);
    BAR();
    BAR(); if (QMODE == 1) TW(3);
    BAR(); BAR();
    BAR();
    if (l == 0) __hip_atomic_store((gu32*)(ws + WS_F1) + g1 * 32 + tt, tag, RLX_AGENT);
    BAR();
    BAR();
    if (l == 0) __hip_atomic_store((gu32*)(ws + WS_F1) + 512 + g1 * 32 + tt, tag, RLX_AGENT);
#pragma unroll
    for (int T = 0; T < 16; ++T) {
      BAR();
    }
    WAIT_V(0);
    KEEP(r[0]); KEEP(r[1]); KEEP(r[2]); KEEP(r[3]);
  }
  const int t_own = tt * 128 + g1 * 8 + w;
  unsigned rf = 0;
  const u32x4 wr = *(const u32x4*)(w_r + l * 8);
  const u32x4 x_own = *(const u32x4*)(x + (size_t)t_own * D + l * 8);
  {
    BAR();
    if (tid == 0) __hip_atomic_store((gu32*)(ws + WS_F2) + tt * 32 + g1, tag, RLX_AGENT);
    STAMP(1, 4);
    if (w == 0) poll_words((const unsigned*)(ws + WS_F2) + tt * 32 + fr, tag);
    if (FMODE == 0) {
    if (w >= 5) {
      const int li = j * 192 + (w - 5) * 64 + l;
      const char* p2;
      if (li < 4096) { const int mrow = (li & 2047) >> 2, c4 = li & 3; p2 = (const char*)((li < 2048 ? w_gate : w_up) + (size_t)mrow * F + xcd * 224 + c4 * 64); }
      else { const int rr = li - 4096; p2 = (const char*)(w_down + (size_t)(rr < F ? rr : 0) * D + xcd * 64); }
      TOUCH(rf, p2);
    }
    }
    BAR();
  }
  STAMP(2, 0);
  unsigned* misc = (unsigned*)(lds + L_MISC);
  u32x4 own;
  {
    float h[8];
    load_h(x_own, rs, t_own, l, h);
    STAMP(2, 7);
    const u32x4 hn = layernorm8(h);
    float r = 0.f, r2 = 0.f;
#pragma unroll
    for (int e = 0; e < 4; ++e) { r += bflo(hn[e]) * bflo(wr[e]); r2 += bfhi(hn[e]) * bfhi(wr[e]); }
    r = wave_sum(r + r2);
    const unsigned rb = f2bf(r);
    const unsigned ord = (rb & 0x8000u) ? (~rb & 0xffffu) : (rb | 0x8000u);
    if (l == 0) { ((u16*)misc)[w] = (u16)ord; if (w == 0) misc[8] = 0; }
    WAIT_L(0);
    BAR();
    if (tid < 4) __hip_atomic_store((gu64*)(ws + WS_GR) + (tt * 16 + g1) * 4 + tid, ((unsigned long long)tag << 32) | misc[tid], RLX_AGENT);
    if (HNPUB) {
      __builtin_amdgcn_raw_buffer_store_b128((u32x4){hn[0], tag, hn[1], tag}, rs, WS_HN + (unsigned)t_own * 2048 + l * 32, 0, 16);
      __builtin_amdgcn_raw_buffer_store_b128((u32x4){hn[2], tag, hn[3], tag}, rs, WS_HN + (unsigned)t_own * 2048 + l * 32 + 16, 0, 16);
    }
#pragma unroll
    for (int e = 0; e < 4; ++e) own[e] = pack2(h[2 * e] + bflo(hn[e]), h[2 * e + 1] + bfhi(hn[e]));
  }
  KEEP(rf);
  {
    const u16* wsrc = down ? w_down + (size_t)(l >> 1) * D + db * 16 + (l & 1) * 8 : ((l & 1) ? w_up : w_gate) + (size_t)(l >> 1) * F + fb * 8;
    if (down) {
#pragma unroll
      for (int i = 0; i < 7; ++i) GLDS16(wsrc + (size_t)(w * 7 + i) * 32 * D, lds + L_W + (w * 7 + i) * 1024, 0);
    } else {
#pragma unroll
      for (int i = 0; i < 2; ++i) GLDS16(wsrc + (size_t)(w * 2 + i) * 32 * F, lds + L_W + (w * 2 + i) * 1024, 0);
    }
  }
  STAMP(2, 1);
  unsigned kk[4];
  {
    u32x4 a;
    for (int it = 0; it < SPIN_MAX; ++it) {
      asm volatile("" ::: "memory");
      a = __builtin_amdgcn_raw_buffer_load_b128(rs, WS_GR + (unsigned)tid * 16, 0, 16);
      if (__all(a[1] == tag && a[3] == tag)) break;
      __builtin_amdgcn_s_sleep(1);
    }
    __builtin_amdgcn_fence(__ATOMIC_ACQUIRE, "wavefront");
    const unsigned ib = 0xffffu - (unsigned)tid * 4;
    kk[0] = (a[0] << 16) | ib; kk[1] = (a[0] & 0xffff0000u) | (ib - 1); kk[2] = (a[2] << 16) | (ib - 2); kk[3] = (a[2] & 0xffff0000u) | (ib - 3);
  }
  STAMP(2, 2);
  {
    const unsigned m = wave_max_u32(umax(umax(kk[0], kk[1]), umax(kk[2], kk[3])));
    if (l == 0) misc[16 + w] = m;
  }
  WAIT_L(0);
  BAR();
  STAMP(2, 5);
  if (b == 0 && tid == 0) __hip_atomic_store((gu32*)(ws + WS_EP), tag, RLX_AGENT);
  {
    const u32x4 ma = *(const u32x4*)(misc + 16), mb2 = *(const u32x4*)(misc + 20);
    const unsigned lo = min(min(min(ma[0], ma[1]), min(ma[2], ma[3])), min(min(mb2[0], mb2[1]), min(mb2[2], mb2[3])));
    if (l < 16) misc[64 + w * 16 + l] = 0u;
    unsigned base = 0;
#pragma unroll
    for (int e = 0; e < 4; ++e) {
      const unsigned long long bm = __ballot(kk[e] >= lo);
      const unsigned pos = base + __builtin_amdgcn_mbcnt_hi((unsigned)(bm >> 32), __builtin_amdgcn_mbcnt_lo((unsigned)bm, 0u));
      if (kk[e] >= lo && pos < 16) misc[64 + w * 16 + pos] = kk[e];
      base += (unsigned)__builtin_popcountll(bm);
    }
    if (base > 16 && l == 0) misc[8] = 1u;
  }
  WAIT_L(0);
  BAR();
  STAMP(2, 6);
  unsigned key;
  bool sel_own;
  {
    const unsigned ovf = __builtin_amdgcn_readfirstlane(misc[8]);
    if (ovf == 0) {
      const unsigned cv = misc[64 + w * 16 + (l >> 2)];
      int rank = 0;
#pragma unroll
      for (int i = 0; i < 8; ++i) {
        const u32x4 c4 = *(const u32x4*)(misc + 64 + (l & 3) * 32 + i * 4);
#pragma unroll
        for (int e = 0; e < 4; ++e) rank += (c4[e] > cv) ? 1 : 0;
      }
      rank += (int)dpp_u<0xB1, 0xf>((unsigned)rank);
      rank += (int)dpp_u<0x4E, 0xf>((unsigned)rank);
      if (cv != 0u && rank < 8 && (l & 3) == 0) misc[32 + rank] = cv;
      WAIT_L(0);
      BAR();
      const unsigned sv = misc[32 + (l & 7)];
      key = (unsigned)__builtin_amdgcn_readlane((int)sv, w);
      sel_own = __any((int)(sv & 0xffffu) == 0xffff - t_own);
    } else {
      unsigned mine = 0;
#pragma unroll 1
      for (int it = 0; it < 8; ++it) {
        const unsigned m = wave_max_u32(umax(umax(kk[0], kk[1]), umax(kk[2], kk[3])));
#pragma unroll
        for (int e = 0; e < 4; ++e) kk[e] = (kk[e] == m) ? 0u : kk[e];
        if (l == it) mine = m;
      }
      if (l < 8) misc[192 + w * 8 + l] = mine;
      WAIT_L(0);
      BAR();
      unsigned cv = misc[192 + l];
      key = 0; sel_own = false;
#pragma unroll 1
      for (int it = 0; it < 8; ++it) {
        const unsigned m = wave_max_u32(cv);
        if (it == w) key = m;
        sel_own |= (int)(m & 0xffffu) == 0xffff - t_own;
        cv = (cv == m) ? 0u : cv;
      }
    }
  }
  STAMP(2, 3);
  const int t_sel = 0xffff - (int)(key & 0xffffu);
  if (HNPUB && !down) {
    u32x4 a, c;
    for (int it = 0; it < SPIN_MAX; ++it) {
      asm volatile("" ::: "memory");
      a = __builtin_amdgcn_raw_buffer_load_b128(rs, WS_HN + (unsigned)t_sel * 2048 + l * 32, 0, 16);
      c = __builtin_amdgcn_raw_buffer_load_b128(rs, WS_HN + (unsigned)t_sel * 2048 + l * 32 + 16, 0, 16);
      if (__all(a[1] == tag && a[3] == tag && c[1] == tag && c[3] == tag)) break;
      __builtin_amdgcn_s_sleep(1);
    }
    *(u32x4*)(lds + L_XS + w * XS_STRIDE + l * 16) = (u32x4){a[0], a[2], c[0], c[2]};
    if (!sel_own) *(u32x4*)(out + (size_t)t_own * D + l * 8) = own;
  } else {
    float h[8];
    const u32x4 xs = *(const u32x4*)(x + (size_t)t_sel * D + l * 8);
    load_h(xs, rs, t_sel, l, h);
    if (!sel_own) *(u32x4*)(out + (size_t)t_own * D + l * 8) = own;
    const u32x4 hn = layernorm8(h);
    if (!down) {
      *(u32x4*)(lds + L_XS + w * XS_STRIDE + l * 16) = hn;
    } else {
      if ((l >> 1) == db) {
        float2* hh = (float2*)(lds + L_HH) + w * 16 + (l & 1) * 8;
#pragma unroll
        for (int e = 0; e < 4; ++e) { hh[2 * e] = make_float2(h[2 * e], bflo(hn[e])); hh[2 * e + 1] = make_float2(h[2 * e + 1], bfhi(hn[e])); }
      }
      if (l == 0) misc[24 + w] = key;
    }
  }
  STAMP(2, 4);
  float* red = (float*)(lds + L_RED);
  if (!down) {
    WAIT_L(0);
    BAR();
    if (w == 0) {
      f32x4 a4[2] = {};
      const char* ap = lds + L_XS + (fr & 7) * XS_STRIDE + q * 16;
      const char* bp = lds + L_W + (q * 8 + (fr >> 2)) * 32 + (fr & 3) * 8;
#pragma unroll
      for (int ks = 0; ks < 16; ++ks) {
        const bf16x8 a = *(const bf16x8*)(ap + ks * 64);
        const v4i16_t g0 = __builtin_amdgcn_ds_read_tr16_b64_v4i16((LAS v4i16_t*)(bp + ks * 1024));
        const v4i16_t g1v = __builtin_amdgcn_ds_read_tr16_b64_v4i16((LAS v4i16_t*)(bp + ks * 1024 + 128));
        const bf16x8 bg = {g0[0], g0[1], g0[2], g0[3], g1v[0], g1v[1], g1v[2], g1v[3]};
        a4[ks & 1] = __builtin_amdgcn_mfma_f32_16x16x32_bf16(a, bg, a4[ks & 1], 0, 0, 0);
      }
      const f32x4 sum = a4[0] + a4[1];
      const bool hi = fr >= 8;
      unsigned actb[2];
#pragma unroll
      for (int k = 0; k < 2; ++k) {
        const float send = hi ? sum[k] : sum[2 + k];
        const float recv = dpp_f<0x128, 0xf>(send);
        const float g = rbf(hi ? recv : sum[k]), u = rbf(hi ? sum[2 + k] : recv);
        const float sg = rbf(g / (1.0f + expf(-g)));
        actb[k] = f2bf(sg * u);
      }
      const unsigned n0 = dpp_u<0xB1, 0xf>(actb[0]), n1 = dpp_u<0xB1, 0xf>(actb[1]);
      const unsigned val = (fr & 1) ? (n1 | (actb[1] << 16)) : (actb[0] | (n0 << 16));
      const int tok = q * 4 + (hi ? 2 : 0) + (fr & 1);
      if (q < 2) __hip_atomic_store((gu64*)(ws + WS_GA) + fb * 32 + tok * 4 + ((fr & 7) >> 1), ((unsigned long long)tag << 32) | val, RLX_AGENT);
    }
    STAMP(3, 0);
  } else {
    for (int it = 0; it < SPIN_MAX; ++it) {
      asm volatile("" ::: "memory");
      bool ok = true;
#pragma unroll
      for (int i = 0; i < 7; ++i) {
        const int rec = w * 28 + i * 4 + q;
        const u32x4 a = __builtin_amdgcn_raw_buffer_load_b128(rs, WS_GA + (unsigned)rec * 256 + fr * 16, 0, 16);
        ok &= (a[1] == tag) & (a[3] == tag);
        *(u32x2*)(lds + L_AS + (fr >> 1) * AS_STRIDE + rec * 16 + (fr & 1) * 8) = (u32x2){a[0], a[2]};
      }
      if (__all(ok)) break;
      __builtin_amdgcn_s_sleep(1);
    }
    STAMP(3, 1);
    WAIT_L(0);
    BAR();
    f32x4 a4 = {};
#pragma unroll
    for (int i = 0; i < 7; ++i) {
      const int ks = w * 7 + i;
      const bf16x8 a = *(const bf16x8*)(lds + L_AS + (fr & 7) * AS_STRIDE + ks * 64 + q * 16);
      const int r0 = ks * 32 + q * 8 + (fr >> 2);
      const int coff = (fr & 3) * 8;
      const v4i16_t g0 = __builtin_amdgcn_ds_read_tr16_b64_v4i16((LAS v4i16_t*)(lds + L_W + r0 * 32 + coff));
      const v4i16_t g1v = __builtin_amdgcn_ds_read_tr16_b64_v4i16((LAS v4i16_t*)(lds + L_W + (r0 + 4) * 32 + coff));
      const bf16x8 bg = {g0[0], g0[1], g0[2], g0[3], g1v[0], g1v[1], g1v[2], g1v[3]};
      a4 = __builtin_amdgcn_mfma_f32_16x16x32_bf16(a, bg, a4, 0, 0, 0);
    }
    if (q < 2) {
#pragma unroll
      for (int jj = 0; jj < 4; ++jj) red[w * 128 + (q * 4 + jj) * 16 + fr] = a4[jj];
    }
    WAIT_L(0);
    BAR();
    if (tid < 128) {
      float s = 0.f;
#pragma unroll
      for (int ww = 0; ww < 8; ++ww) s += red[ww * 128 + tid];
      const unsigned k2 = misc[24 + (tid >> 4)];
      const int t = 0xffff - (int)(k2 & 0xffffu);
      const unsigned ord = k2 >> 16;
      const unsigned rb = (ord & 0x8000u) ? (ord & 0x7fffu) : (~ord & 0xffffu);
      const float sig = rbf(1.0f / (1.0f + expf(-bf2f(rb))));
      const float upd = rbf(rbf(s) * sig);
      const float2 hh = ((const float2*)(lds + L_HH))[tid];
      out[(size_t)t * D + db * 16 + (tid & 15)] = (u16)f2bf(hh.x + rbf(hh.y + upd));
    }
    STAMP(3, 2);
  }
  STAMP_FLUSH;
  asm volatile(".Lfused_code_hi:");
}

extern "C" void kernel_launch(void* const* d_in, const int* in_sizes, int n_in, void* d_out, int out_size, void* d_ws,
                              size_t ws_size, hipStream_t stream) {
  fused<<<256, 512, 0, stream>>>((const u16*)d_in[0], (const u16*)d_in[1], (const u16*)d_in[2], (const u16*)d_in[3], (const u16*)d_in[4],
                                 (const u16*)d_in[6], (const u16*)d_in[7], (const u16*)d_in[8], (const u16*)d_in[9], (u16*)d_out, (char*)d_ws);
}
```

```cpp
#include <hip/hip_runtime.h>
#include <hip/hip_bf16.h>
#include <stdint.h>
#include <utility>

typedef unsigned short u16;
typedef __attribute__((ext_vector_type(8))) short bf16x8;
typedef __attribute__((ext_vector_type(4))) short v4i16_t;
typedef __attribute__((ext_vector_type(4))) float f32x4;
typedef __attribute__((ext_vector_type(4))) unsigned u32x4;
typedef __attribute__((ext_vector_type(2))) unsigned u32x2;
#define GAS __attribute__((address_space(1)))
#define LAS __attribute__((address_space(3)))
typedef GAS unsigned gu32;
typedef GAS unsigned long long gu64;
#define RLX_AGENT __ATOMIC_RELAXED, __HIP_MEMORY_SCOPE_AGENT

constexpr int S = 2048, D = 512, F = 1792;

__device__ __forceinline__ float bf2f(unsigned v) { return __uint_as_float(v << 16); }
__device__ __forceinline__ float bflo(unsigned v) { return __uint_as_float(v << 16); }
__device__ __forceinline__ float bfhi(unsigned v) { return __uint_as_float(v & 0xffff0000u); }
__device__ __forceinline__ unsigned f2bf(float f) { __bf16 b = (__bf16)f; return (unsigned)__builtin_bit_cast(u16, b); }
__device__ __forceinline__ float rbf(float f) { return bf2f(f2bf(f)); }
__device__ __forceinline__ unsigned pack2(float lo, float hi) { return f2bf(lo) | (f2bf(hi) << 16); }

#define WAIT_V(n) asm volatile("s_waitcnt vmcnt(%0)" ::"n"(n) : "memory")
#define WAIT_L(n) asm volatile("s_waitcnt lgkmcnt(%0)" ::"n"(n) : "memory")
#define BAR() __builtin_amdgcn_s_barrier()
#define GLDS16(src, dst, aux) __builtin_amdgcn_global_load_lds((const unsigned*)(src), (unsigned*)(dst), 16, 0, aux)
#define TOUCH(reg, ptr) asm volatile("global_load_dword %0, %1, off" : "+v"(reg) : "v"(ptr) : "memory")
#define TOUCH_NT(reg, ptr) asm volatile("global_load_dword %0, %1, off nt" : "+v"(reg) : "v"(ptr) : "memory")
#define KEEP(reg) asm volatile("" ::"v"(reg))

template <int CTRL, int RM> __device__ __forceinline__ float dpp_f(float v) {
  return __builtin_bit_cast(float, __builtin_amdgcn_update_dpp(0, __builtin_bit_cast(int, v), CTRL, RM, 0xf, true));
}
__device__ __forceinline__ float wave_sum(float v) {
  v += dpp_f<0x111, 0xf>(v); v += dpp_f<0x112, 0xf>(v); v += dpp_f<0x114, 0xf>(v); v += dpp_f<0x118, 0xf>(v);
  v += dpp_f<0x142, 0xa>(v); v += dpp_f<0x143, 0xc>(v);
  return __builtin_bit_cast(float, __builtin_amdgcn_readlane(__builtin_bit_cast(int, v), 63));
}
template <int CTRL, int RM> __device__ __forceinline__ unsigned dpp_u(unsigned v) {
  return (unsigned)__builtin_amdgcn_update_dpp(0, (int)v, CTRL, RM, 0xf, true);
}
__device__ __forceinline__ unsigned umax(unsigned a, unsigned b) { return a > b ? a : b; }
__device__ __forceinline__ unsigned wave_max_u32(unsigned v) {
  v = umax(v, dpp_u<0x111, 0xf>(v)); v = umax(v, dpp_u<0x112, 0xf>(v)); v = umax(v, dpp_u<0x114, 0xf>(v)); v = umax(v, dpp_u<0x118, 0xf>(v));
  v = umax(v, dpp_u<0x142, 0xa>(v)); v = umax(v, dpp_u<0x143, 0xc>(v));
  return (unsigned)__builtin_amdgcn_readlane((int)v, 63);
}

#define STAMP_DECL
#define STAMPW(k, i, wv)
#define STAMP_FLUSH
#define STAMP(k, i) STAMPW(k, i, 0)

constexpr unsigned WS_ABT = 0;
constexpr unsigned WS_Y = WS_ABT + 1024u * 2048 * 2;
constexpr unsigned WS_F1 = WS_Y + 4u * 2048 * 512 * 2;
constexpr unsigned WS_F2 = WS_F1 + 4096;
constexpr unsigned WS_GR = WS_F2 + 2048;
constexpr unsigned WS_GA = WS_GR + 1024 * 8;
constexpr unsigned WS_EP = WS_GA + 224 * 256;
constexpr unsigned WS_HN = WS_EP + 256;
constexpr unsigned WS_END = WS_HN + 2048u * 2048;

constexpr int SPIN_MAX = 1 << 18;
#define AUX1 0
#define CPF 1
#define FMODE 1
#define HNPUB 0
#define TS 3
#define QMODE 0
#define NPLAIN 8
#define T1MODE 1
#define FFNSTEP 2

constexpr int L_QR = 81920;
constexpr int L_RST = 65536;
constexpr int L_TAG = 66560;
constexpr int L_W = 16384;
constexpr int L_T2 = 131072;
constexpr int XS_STRIDE = 1024 + 16;
constexpr int L_XS = 81920;
constexpr int L_RED = L_XS + 8 * XS_STRIDE;
constexpr int L_MISC = L_RED + 8 * 128 * 4;
constexpr int L_HH = L_MISC + 1024;
constexpr int AS_STRIDE = F * 2 + 16;
constexpr int L_AS = L_HH + 1024;
static_assert(L_AS + 8 * AS_STRIDE <= L_T2, "lds");

struct Ring2 {
  static constexpr int p(int T) { return (T % 5) * 16384; }
  static constexpr int q(int T) { return L_QR + ((T + 3) % 5) * 16384; }
};
constexpr int koff(int T) { return T < 8 ? T * 128 : (T - 8) * 128 + 64; }
constexpr int x1slot(int s) { return L_QR + (s % 5) * 16384; }

template <int BP, int BQ, int NKT, class RG, bool STATS>
struct Cons {
  static constexpr int TP = BP / 32, TQ = BQ / 32;
  struct Ctx { const char* lds; int poff[TP][2]; int qoff[TQ][2]; };
  struct Fr { bf16x8 pf[2][TP], qf[2][TQ]; };
  struct St { f32x4 s1[2], s2[2], sw[TQ]; };
  template <int T> static __device__ __forceinline__ void readf(const Ctx& c, Fr& f) {
#pragma unroll
    for (int ks = 0; ks < 2; ++ks) {
#pragma unroll
      for (int m = 0; m < TP; ++m) f.pf[ks][m] = *(const bf16x8*)(c.lds + RG::p(T) + c.poff[m][ks]);
#pragma unroll
      for (int n = 0; n < TQ; ++n) f.qf[ks][n] = *(const bf16x8*)(c.lds + RG::q(T) + c.qoff[n][ks]);
    }
  }
  template <int T> static __device__ __forceinline__ void step(const Ctx& c, f32x4 (&acc)[TP][TQ], Fr& cur, Fr& nxt, St& st) {
    if constexpr (T + 1 < NKT) {
      WAIT_L(0);
      BAR();
      __builtin_amdgcn_sched_barrier(0);
      readf<T + 1>(c, nxt);
    }
    if constexpr (T + 1 == NKT) return;
#pragma unroll
    for (int ks = 0; ks < 2; ++ks) {
#pragma unroll
      for (int m = 0; m < TP; ++m)
#pragma unroll
        for (int n = 0; n < TQ; ++n) acc[m][n] = __builtin_amdgcn_mfma_f32_16x16x32_bf16(cur.pf[ks][m], cur.qf[ks][n], acc[m][n], 0, 0, 0);
      if constexpr (STATS) {
        const bf16x8 ones = {0x3f80, 0x3f80, 0x3f80, 0x3f80, 0x3f80, 0x3f80, 0x3f80, 0x3f80};
#pragma unroll
        for (int m = 0; m < 2; ++m) {
          st.s1[m] = __builtin_amdgcn_mfma_f32_16x16x32_bf16(cur.pf[ks][m], ones, st.s1[m], 0, 0, 0);
          st.s2[m] = __builtin_amdgcn_mfma_f32_16x16x32_bf16(cur.pf[ks][m], cur.pf[ks][m], st.s2[m], 0, 0, 0);
        }
#pragma unroll
        for (int n = 0; n < TQ; ++n) st.sw[n] = __builtin_amdgcn_mfma_f32_16x16x32_bf16(ones, cur.qf[ks][n], st.sw[n], 0, 0, 0);
      }
    }
    if constexpr (T + 1 < NKT) {
#pragma unroll
      for (int i = 0; i < 2 * (TP + TQ); ++i) { __builtin_amdgcn_sched_group_barrier(0x100, 1, 0); __builtin_amdgcn_sched_group_barrier(0x8, 1, 0); }
    }
    __builtin_amdgcn_sched_barrier(0);
  }
  template <int... Ts>
  static __device__ __forceinline__ void steps(const Ctx& c, f32x4 (&acc)[TP][TQ], Fr& f0, Fr& f1, St& st, std::integer_sequence<int, Ts...>) {
    ((Ts & 1 ? step<Ts>(c, acc, f1, f0, st) : step<Ts>(c, acc, f0, f1, st)), ...);
  }
  static __device__ __forceinline__ void run(const char* lds, int wp, int wq, int rot, int l, f32x4 (&acc)[TP][TQ], St& st, Fr& f1) {
    Ctx c; c.lds = lds;
    const int fr = l & 15, q = l >> 4;
#pragma unroll
    for (int ks = 0; ks < 2; ++ks) {
#pragma unroll
      for (int m = 0; m < TP; ++m) { const int row = wp * (BP / 2) + ((m + rot) & (TP - 1)) * 16 + fr; c.poff[m][ks] = row * 128 + (((ks * 4 + q) ^ ((row >> 1) & 7)) * 16); }
#pragma unroll
      for (int n = 0; n < TQ; ++n) { const int row = wq * (BQ / 2) + n * 16 + fr; c.qoff[n][ks] = row * 128 + (((ks * 4 + q) ^ ((row >> 1) & 7)) * 16); }
    }
    Fr f0;
    BAR();
    readf<0>(c, f0);
    steps(c, acc, f0, f1, st, std::make_integer_sequence<int, NKT>{});
  }
};

struct L1Ctx { const u16* xsrc[4]; const u16* wsrc[4]; char* lds; int w; };
template <int s> __device__ __forceinline__ void l1_issue(const L1Ctx& c) {
#pragma unroll
  for (int h = 0; h < 2; ++h) {
#pragma unroll
    for (int i = 0; i < 4; ++i) GLDS16(c.xsrc[i] + (size_t)(s >> 2) * 64 * D + (s & 3) * 128 + h * 64, c.lds + x1slot(s) + h * 8192 + (c.w * 4 + i) * 1024, AUX1);
    if constexpr (s < 4) {
#pragma unroll
      for (int i = 0; i < 4; ++i) GLDS16(c.wsrc[i] + s * 128 + h * 64, c.lds + (2 * s + h) * 8192 + (c.w * 4 + i) * 1024, AUX1);
    }
  }
}
struct Fr1 { bf16x8 pf[4][2], qf[4][2]; };
struct C1Ctx { const char* lds; int poff[2][2]; int qoff[2][2]; };
template <int s> __device__ __forceinline__ void c1_read(const C1Ctx& c, Fr1& f) {
#pragma unroll
  for (int ks = 0; ks < 4; ++ks) {
#pragma unroll
    for (int m = 0; m < 2; ++m) f.pf[ks][m] = *(const bf16x8*)(c.lds + x1slot(s) + (ks >> 1) * 8192 + c.poff[m][ks & 1]);
#pragma unroll
    for (int n = 0; n < 2; ++n) f.qf[ks][n] = *(const bf16x8*)(c.lds + (2 * (s & 3) + (ks >> 1)) * 8192 + c.qoff[n][ks & 1]);
  }
}
struct Acc1 { f32x4 acc[2][2], s1, s2; };
template <int s> __device__ __forceinline__ void c1_step(const C1Ctx& c, Acc1& a, f32x4 (&sw)[2], Fr1& cur, Fr1& nxt) {
  if constexpr (s == 7) WAIT_V(0);
  WAIT_L(0);
  BAR();
  __builtin_amdgcn_sched_barrier(0);
  if constexpr (s + 1 < 8) c1_read<s + 1>(c, nxt);
  const bf16x8 ones = {0x3f80, 0x3f80, 0x3f80, 0x3f80, 0x3f80, 0x3f80, 0x3f80, 0x3f80};
#pragma unroll
  for (int ks = 0; ks < 4; ++ks) {
#pragma unroll
    for (int m = 0; m < 2; ++m)
#pragma unroll
      for (int n = 0; n < 2; ++n) a.acc[m][n] = __builtin_amdgcn_mfma_f32_16x16x32_bf16(cur.pf[ks][m], cur.qf[ks][n], a.acc[m][n], 0, 0, 0);
    a.s1 = __builtin_amdgcn_mfma_f32_16x16x32_bf16(cur.pf[ks][0], ones, a.s1, 0, 0, 0);
    a.s2 = __builtin_amdgcn_mfma_f32_16x16x32_bf16(cur.pf[ks][0], cur.pf[ks][0], a.s2, 0, 0, 0);
    if constexpr (s < 4) {
#pragma unroll
      for (int n = 0; n < 2; ++n) sw[n] = __builtin_amdgcn_mfma_f32_16x16x32_bf16(ones, cur.qf[ks][n], sw[n], 0, 0, 0);
    }
  }
  if constexpr (s + 1 < 8) {
#pragma unroll
    for (int i = 0; i < 16; ++i) { __builtin_amdgcn_sched_group_barrier(0x100, 1, 0); __builtin_amdgcn_sched_group_barrier(0x8, 1, 0); }
  }
  __builtin_amdgcn_sched_barrier(0);
}

struct L2Ctx { const u16* psrc[8]; const u16* qsrc[8]; char* lds; int w; };
template <int T> __device__ __forceinline__ void l2_issueP(const L2Ctx& c) {
#pragma unroll
  for (int i = 0; i < 8; ++i) GLDS16(c.psrc[i] + koff(T), c.lds + Ring2::p(T) + (c.w * 8 + i) * 1024, 16);
}
template <int T, int AUX> __device__ __forceinline__ void l2_issueQ(const L2Ctx& c) {
#pragma unroll
  for (int i = 0; i < 8; ++i) GLDS16(c.qsrc[i] + koff(T), c.lds + Ring2::q(T) + (c.w * 8 + i) * 1024, AUX);
}
constexpr int l2_cnt(int s) { return s < 5 ? 8 : 16; }
constexpr int l2_younger(int T) { int up = T + 4 < 15 ? T + 4 : 15; int n = 0; for (int s = T + 2; s <= up; ++s) n += l2_cnt(s); return n; }
template <int T, int AUX> __device__ __forceinline__ void l2_step(const L2Ctx& c) {
  WAIT_V(l2_younger(T));
  BAR();
  if constexpr (T + 5 < 16) { l2_issueP<T + 5>(c); l2_issueQ<T + 5, AUX>(c); }
}
template <int AUX, int... Ts> __device__ __forceinline__ void l2_steps(const L2Ctx& c, std::integer_sequence<int, Ts...>) { (l2_step<Ts, AUX>(c), ...); }

__device__ __forceinline__ u32x4 widen16(u32x2 a, u32x2 c) {
  const auto s0 = __builtin_amdgcn_permlane16_swap(a[0], c[0], false, false);
  const auto s1 = __builtin_amdgcn_permlane16_swap(a[1], c[1], false, false);
  return (u32x4){s0[0], s1[0], s0[1], s1[1]};
}

__device__ __forceinline__ void poll_words(const unsigned* p, unsigned tag) {
  for (int it = 0; it < SPIN_MAX; ++it) {
    const unsigned v = __hip_atomic_load((gu32*)p, RLX_AGENT);
    if (__all(v == tag)) break;
    __builtin_amdgcn_s_sleep(1);
  }
  __builtin_amdgcn_fence(__ATOMIC_ACQUIRE, "wavefront");
}

__device__ __forceinline__ unsigned add_pk(unsigned a, unsigned b) { return pack2(bflo(a) + bflo(b), bfhi(a) + bfhi(b)); }
__device__ __forceinline__ unsigned sub_pk(unsigned a, unsigned b) { return pack2(bflo(a) - bflo(b), bfhi(a) - bfhi(b)); }
template <class R>
__device__ __forceinline__ void load_h(const u32x4 xv, R rs, int t, int l, float (&h)[8]) {
  const unsigned o = (unsigned)(t * D + l * 8) * 2;
  const u32x4 y0 = __builtin_amdgcn_raw_buffer_load_b128(rs, WS_Y + o, 0, 16);
  const u32x4 y1 = __builtin_amdgcn_raw_buffer_load_b128(rs, WS_Y + 1u * S * D * 2 + o, 0, 16);
  const u32x4 y2 = __builtin_amdgcn_raw_buffer_load_b128(rs, WS_Y + 2u * S * D * 2 + o, 0, 16);
  const u32x4 y3 = __builtin_amdgcn_raw_buffer_load_b128(rs, WS_Y + 3u * S * D * 2 + o, 0, 16);
#pragma unroll
  for (int e = 0; e < 4; ++e) {
    const unsigned hv = add_pk(xv[e], sub_pk(add_pk(y0[e], y1[e]), add_pk(y2[e], y3[e])));
    h[2 * e] = bflo(hv); h[2 * e + 1] = bfhi(hv);
  }
}
__device__ __forceinline__ u32x4 layernorm8(const float (&h)[8]) {
  const float pv = __builtin_bit_cast(float, __builtin_amdgcn_readfirstlane(__builtin_bit_cast(int, h[0])));
  float s = 0.f, s2 = 0.f, v = 0.f, v2 = 0.f;
#pragma unroll
  for (int e = 0; e < 4; ++e) { const float a = h[2 * e] - pv, c = h[2 * e + 1] - pv; s += a; s2 += c; v += a * a; v2 += c * c; }
  const float md = wave_sum(s + s2) * (1.0f / D);
  v = fmaxf(wave_sum(v + v2) - md * md * (float)D, 0.f);
  const float mean = pv + md;
  const float mb = rbf(mean);
  const float sd = rbf(sqrtf(rbf(rbf(v * (1.0f / D)) + 1e-5f)));
  const float inv = 1.0f / sd;
  u32x4 o;
#pragma unroll
  for (int e = 0; e < 4; ++e) {
    const unsigned d = pack2(h[2 * e] - mb, h[2 * e + 1] - mb);
    const float a = bflo(d), c = bfhi(d);
    float qa = a * inv, qc = c * inv;
    qa = fmaf(fmaf(-qa, sd, a), inv, qa); qc = fmaf(fmaf(-qc, sd, c), inv, qc);
    o[e] = pack2(qa, qc);
  }
  return o;
}

__global__ __launch_bounds__(512) void fused(const u16* __restrict__ x, const u16* __restrict__ ws_re, const u16* __restrict__ ws_im,
                                             const u16* __restrict__ wd_re, const u16* __restrict__ wd_im, const u16* __restrict__ w_r,
                                             const u16* __restrict__ w_gate, const u16* __restrict__ w_up, const u16* __restrict__ w_down,
                                             u16* __restrict__ out, char* __restrict__ ws) {
  __shared__ __attribute__((aligned(1024))) char lds[163840];
  const int b = blockIdx.x, xcd = b & 7, j = b >> 3;
  const int cc = xcd >> 2, et = j & 3, tt = (xcd & 3) * 4 + ((j >> 2) & 3), kh = j >> 4;
  const int g1 = (cc * 4 + et) * 2 + kh;
  const int e1 = cc * 8 + et * 2 + (tt & 1), s1 = kh * 8 + (tt >> 1);
  const int tid = threadIdx.x, w = __builtin_amdgcn_readfirstlane(tid >> 6), l = tid & 63, fr = l & 15, q = l >> 4;
  const u16* wsb = cc ? ws_im : ws_re;
  const u16* wdb = cc ? wd_im : wd_re;
  const bool down = j >= 28;
  const int fb = xcd * 28 + j, db = xcd * 4 + (j - 28);
  const auto rs = __builtin_amdgcn_make_buffer_rsrc(ws, 0, WS_END, 0x00020000);
  unsigned tag;
  STAMP_DECL;
  STAMP(0, 0);
  unsigned long long code_lo; unsigned code_len;
  asm volatile("s_getpc_b64 %0\n.Lfused_code_lo:\n\ts_mov_b32 %1, .Lfused_code_hi-.Lfused_code_lo" : "=s"(code_lo), "=s"(code_len));

  if (w < 4) {
    const int wp = w >> 1, wq = w & 1;
    unsigned r0 = 0;
    {
      const int row = tid >> 3, ln = tid & 7;
      const int xs_tile = ((j >> 3) & 1) * 8 + (xcd & 3) * 2 + ((j >> 2) & 1);
      const u16* p = j < 16 ? x + (size_t)(xs_tile * 128 + (j & 3) * 32 + row) * D + ln * 64 : wdb + (size_t)((j - 16) * 32 + row) * D + ln * 64;
      if (T1MODE == 1) TOUCH(r0, p);
      if (T1MODE == 2) TOUCH_NT(r0, p);
    }
    {
      C1Ctx c; c.lds = lds;
#pragma unroll
      for (int ks = 0; ks < 2; ++ks) {
#pragma unroll
        for (int m = 0; m < 2; ++m) { const int row = wp * 32 + (m ^ wq) * 16 + fr; c.poff[m][ks] = row * 128 + (((ks * 4 + q) ^ ((row >> 1) & 7)) * 16); }
#pragma unroll
        for (int n = 0; n < 2; ++n) { const int row = wq * 32 + n * 16 + fr; c.qoff[n][ks] = row * 128 + (((ks * 4 + q) ^ ((row >> 1) & 7)) * 16); }
      }
      f32x4 sw[2] = {};
      Fr1 f0, f1;
      auto epilogue = [&](const Acc1& a, int pass) {
        float2* rst = (float2*)(lds + L_RST) + pass * 64;
        if ((fr >> 2) == q) {
          const int jj = fr & 3;
          const float s1v = jj == 0 ? a.s1[0] : jj == 1 ? a.s1[1] : jj == 2 ? a.s1[2] : a.s1[3];
          const float s2v = jj == 0 ? a.s2[0] : jj == 1 ? a.s2[1] : jj == 2 ? a.s2[2] : a.s2[3];
          const float mean = s1v * (1.0f / D);
          const float var = fmaxf(s2v * (1.0f / D) - mean * mean, 0.f);
          const float sd = rbf(sqrtf(rbf(rbf(var) + 1e-5f)));
          rst[wp * 32 + wq * 16 + fr] = make_float2(rbf(mean), 1.0f / sd);
        }
        WAIT_L(0);
        BAR();
        u32x2 pk[2][2];
#pragma unroll
        for (int m = 0; m < 2; ++m) {
          const int p0 = wp * 32 + (m ^ wq) * 16 + q * 4;
          const f32x4 sa = *(const f32x4*)(rst + p0), sb = *(const f32x4*)(rst + p0 + 2);
#pragma unroll
          for (int n = 0; n < 2; ++n) {
            const float swv = sw[n][0];
            const float v0 = (a.acc[m][n][0] - sa[0] * swv) * sa[1], v1 = (a.acc[m][n][1] - sa[2] * swv) * sa[3];
            const float v2 = (a.acc[m][n][2] - sb[0] * swv) * sb[1], v3 = (a.acc[m][n][3] - sb[2] * swv) * sb[3];
            pk[m][n] = (u32x2){pack2(v0, v1), pack2(v2, v3)};
          }
        }
        const int scol = s1 * 128 + pass * 64 + wp * 32 + ((q & 1) ^ wq) * 16 + (q >> 1) * 8;
#pragma unroll
        for (int n = 0; n < 2; ++n)
          __builtin_amdgcn_raw_buffer_store_b128(widen16(pk[0][n], pk[1][n]), rs, WS_ABT + (unsigned)((e1 * 64 + wq * 32 + n * 16 + fr) * S + scol) * 2, 0, 16);
      };
      BAR();
      c1_read<0>(c, f0);
      {
        Acc1 a = {};
        c1_step<0>(c, a, sw, f0, f1); c1_step<1>(c, a, sw, f1, f0); c1_step<2>(c, a, sw, f0, f1); c1_step<3>(c, a, sw, f1, f0);
        STAMP(0, 2);
        epilogue(a, 0);
        STAMP(0, 3);
      }
      {
        Acc1 a = {};
        c1_step<4>(c, a, sw, f0, f1); c1_step<5>(c, a, sw, f1, f0); c1_step<6>(c, a, sw, f0, f1); c1_step<7>(c, a, sw, f1, f0);
        STAMP(0, 4);
        epilogue(a, 1);
        STAMP(0, 5);
      }
      tag = __builtin_amdgcn_readfirstlane(*(const unsigned*)(lds + L_TAG));
      WAIT_V(0);
      KEEP(r0);
      STAMP(0, 6);
      BAR();
    }
    using C2 = Cons<128, 128, 16, Ring2, false>;
    f32x4 acc[4][4] = {};
    C2::St st;
    C2::Fr fl;
    C2::run(lds, wp, wq, 0, l, acc, st, fl);
    STAMP(1, 2);
    {
      const unsigned yo = WS_Y + (unsigned)(cc * 2 + kh) * S * D * 2;
      const int chunk = (q >> 1) + 2 * (q & 1);
#pragma unroll
      for (int n = 0; n < 4; ++n) {
#pragma unroll
        for (int ks = 0; ks < 2; ++ks)
#pragma unroll
          for (int m = 0; m < 4; ++m) acc[m][n] = __builtin_amdgcn_mfma_f32_16x16x32_bf16(fl.pf[ks][m], fl.qf[ks][n], acc[m][n], 0, 0, 0);
#pragma unroll
        for (int mp = 0; mp < 2; ++mp) {
          const u32x2 a = {pack2(acc[2 * mp][n][0], acc[2 * mp][n][1]), pack2(acc[2 * mp][n][2], acc[2 * mp][n][3])};
          const u32x2 c = {pack2(acc[2 * mp + 1][n][0], acc[2 * mp + 1][n][1]), pack2(acc[2 * mp + 1][n][2], acc[2 * mp + 1][n][3])};
          const int row = wq * 64 + n * 16 + fr;
          __builtin_amdgcn_raw_buffer_store_b128(widen16(a, c), rs, yo + (unsigned)((tt * 128 + row) * D + et * 128 + wp * 64 + mp * 32 + chunk * 8) * 2, 0, 16);
        }
      }
    }
    STAMP(1, 3);
    WAIT_V(0);
    STAMP(1, 6);
  } else if (w < 6) {
    const int lw = w - 4;
    const int r8 = l >> 3, ch = l & 7;
    {
      L1Ctx c; c.lds = lds; c.w = lw;
      const u16* P = x + (size_t)s1 * 128 * D;
      const u16* Q = wdb + (size_t)(e1 & 7) * 64 * D;
#pragma unroll
      for (int i = 0; i < 4; ++i) {
        const int row = (lw * 4 + i) * 8 + r8;
        const size_t o = (size_t)row * D + ((ch ^ ((row >> 1) & 7)) * 8);
        c.xsrc[i] = P + o; c.wsrc[i] = Q + o;
      }
      l1_issue<0>(c); l1_issue<1>(c);
      WAIT_V(16); STAMPW(0, 1, 4); BAR();
      l1_issue<2>(c);
      WAIT_V(16); BAR();
      l1_issue<3>(c);
      WAIT_V(16); BAR();
      l1_issue<4>(c); l1_issue<5>(c);
      WAIT_V(16); BAR();
      l1_issue<6>(c); l1_issue<7>(c);
      WAIT_V(24); BAR();
      BAR();
      WAIT_V(16); BAR();
      WAIT_V(8); BAR();
      WAIT_V(0); BAR();
    }
    L2Ctx c2;
    c2.lds = lds; c2.w = lw;
    {
      const u16* P = (const u16*)(ws + WS_ABT) + (size_t)(cc * 512 + et * 128) * S + kh * 1024;
      const u16* Q = wsb + (size_t)tt * 128 * S + kh * 1024;
#pragma unroll
      for (int i = 0; i < 8; ++i) {
        const int row = (lw * 8 + i) * 8 + r8;
        const size_t o = (size_t)row * S + ((ch ^ ((row >> 1) & 7)) * 8);
        c2.psrc[i] = P + o; c2.qsrc[i] = Q + o;
      }
    }
    tag = __builtin_amdgcn_readfirstlane(*(const unsigned*)(lds + L_TAG));
    BAR();
    BAR();
    poll_words((const unsigned*)(ws + WS_F1) + g1 * 32 + fr, tag);
    STAMPW(1, 0, 4);
    l2_issueP<0>(c2); l2_issueP<1>(c2); l2_issueP<2>(c2);
    BAR();
    WAIT_V(16);
    STAMPW(1, 1, 4);
    BAR();
    l2_issueP<3>(c2); l2_issueP<4>(c2);
    l2_steps<0>(c2, std::make_integer_sequence<int, 15>{});
  } else if (w == 6) {
    const int r8 = l >> 3, ch = l & 7;
    const unsigned ep = __hip_atomic_load((gu32*)(ws + WS_EP), RLX_AGENT);
    const u16* Q = wsb + (size_t)tt * 128 * S + kh * 1024;
    const u16* qa = Q + (size_t)r8 * S + ((ch ^ (r8 >> 1)) * 8);
    const u16* qb = Q + (size_t)(8 + r8) * S + ((ch ^ (4 + (r8 >> 1))) * 8);
    auto issueQ = [&](int T) {
#pragma unroll
      for (int i = 0; i < 8; ++i) {
        if (QMODE == 0 || (QMODE == 1 && 2 * i < NPLAIN)) {
          GLDS16(qa + (size_t)i * 16 * S + T * 128, lds + L_QR + ((T + 3) % 5) * 16384 + (2 * i) * 1024, 0);
          GLDS16(qb + (size_t)i * 16 * S + T * 128, lds + L_QR + ((T + 3) % 5) * 16384 + (2 * i + 1) * 1024, 0);
        } else {
          GLDS16(qa + (size_t)i * 16 * S + T * 128, lds + L_QR + ((T + 3) % 5) * 16384 + (2 * i) * 1024, 2);
          GLDS16(qb + (size_t)i * 16 * S + T * 128, lds + L_QR + ((T + 3) % 5) * 16384 + (2 * i + 1) * 1024, 2);
        }
      }
    };
    BAR(); BAR();
    tag = __builtin_amdgcn_readfirstlane(ep * 1664525u + 1013904223u);
    *(unsigned*)(lds + L_TAG) = tag;
    WAIT_L(0);
    BAR(); BAR(); BAR();
    BAR();
    BAR(); issueQ(0);
    BAR(); issueQ(1);
    BAR(); issueQ(2);
    BAR(); BAR();
    BAR(); issueQ(3); issueQ(4);
    WAIT_V(32);
    BAR();
    const unsigned* fB = (const unsigned*)(ws + WS_F1) + 512 + g1 * 32 + fr;
    BAR(); BAR();
    const unsigned vB = __hip_atomic_load((gu32*)fB, RLX_AGENT);
    WAIT_V(17);
    BAR();
    if (!__all(vB == tag)) poll_words(fB, tag);
    WAIT_V(0);
    BAR();
#pragma unroll
    for (int T = 4; T < 15; ++T) BAR();
  } else {
    unsigned r[4] = {0, 0, 0, 0};
    constexpr int LPI = 64 / (2 * NPLAIN) > 0 ? 64 / (2 * NPLAIN) : 1;
    const int pr = j * NPLAIN + (l % NPLAIN);
    const int prow = (pr / (NPLAIN * 8)) * 128 + (pr % (NPLAIN * 8));
    const u16* wb = wsb + (size_t)((xcd & 3) * 512 + prow) * S + ((l / NPLAIN) & 1) * 1024 + (l / (2 * NPLAIN)) * 128;
#define TW(i) TOUCH(r[(i) & 1], wb + ((i) % (8 / LPI)) * (LPI * 128) + ((i) / (8 / LPI)) * 64)
    auto ffn_ptr = [&](int i) -> const char* {
      const int li = j * 192 + i * 64 + l;
      if (li < 4096) { const int mrow = (li & 2047) >> 2, c4 = li & 3; return (const char*)((li < 2048 ? w_gate : w_up) + (size_t)mrow * F + xcd * 224 + c4 * 64); }
      const int rr = li - 4096;
      return (const char*)(w_down + (size_t)(rr < F ? rr : 0) * D + xcd * 64);
    };
    if (CPF) {
      const unsigned off = (unsigned)(j * 16 + fr) * 128u;
      const char* cp = (const char*)code_lo + (off < code_len ? off : 0u);
      if (l < 16) TOUCH(r[3], cp);
    }
    const char* fp0 = ffn_ptr(0); const char* fp1 = ffn_ptr(1); const char* fp2 = ffn_ptr(2);
    const u16* wb2 = wsb + (size_t)((xcd & 3) * 512 + j * 16 + fr) * S + ((l >> 4) & 1) * 1024 + (l >> 5) * 128;
#define TF(i) TOUCH(r[(i) & 1], wb2 + ((i) & 3) * 256 + ((i) >> 2) * 64)
#define TFF(p) TOUCH(r[2], p)
    BAR(); BAR(); BAR(); TFF(fp0);
    tag = __builtin_amdgcn_readfirstlane(*(const unsigned*)(lds + L_TAG));
    BAR(); TFF(fp1); TF(0); BAR(); TFF(fp2); TF(1); BAR(); TF(2);
    BAR(); TF(3); BAR(); TF(4); BAR(); TF(5);
    BAR();
    if (l == 0) __hip_atomic_store((gu32*)(ws + WS_F1) + g1 * 32 + tt, tag, RLX_AGENT);
    TF(6);
    BAR(); TF(7);
    BAR();
    if (l == 0) __hip_atomic_store((gu32*)(ws + WS_F1) + 512 + g1 * 32 + tt, tag, RLX_AGENT);
#pragma unroll
    for (int T = 0; T < 16; ++T) {
      BAR();
    }
    WAIT_V(0);
    KEEP(r[0]); KEEP(r[1]); KEEP(r[2]); KEEP(r[3]);
  }
  const int t_own = tt * 128 + g1 * 8 + w;
  unsigned rf = 0;
  const u32x4 wr = *(const u32x4*)(w_r + l * 8);
  const u32x4 x_own = *(const u32x4*)(x + (size_t)t_own * D + l * 8);
  {
    BAR();
    if (tid == 0) __hip_atomic_store((gu32*)(ws + WS_F2) + tt * 32 + g1, tag, RLX_AGENT);
    STAMP(1, 4);
    if (w == 0) poll_words((const unsigned*)(ws + WS_F2) + tt * 32 + fr, tag);
    if (FMODE == 0) {
    if (w >= 5) {
      const int li = j * 192 + (w - 5) * 64 + l;
      const char* p2;
      if (li < 4096) { const int mrow = (li & 2047) >> 2, c4 = li & 3; p2 = (const char*)((li < 2048 ? w_gate : w_up) + (size_t)mrow * F + xcd * 224 + c4 * 64); }
      else { const int rr = li - 4096; p2 = (const char*)(w_down + (size_t)(rr < F ? rr : 0) * D + xcd * 64); }
      TOUCH(rf, p2);
    }
    }
    BAR();
  }
  STAMP(2, 0);
  unsigned* misc = (unsigned*)(lds + L_MISC);
  u32x4 own;
  {
    float h[8];
    load_h(x_own, rs, t_own, l, h);
    STAMP(2, 7);
    const u32x4 hn = layernorm8(h);
    float r = 0.f, r2 = 0.f;
#pragma unroll
    for (int e = 0; e < 4; ++e) { r += bflo(hn[e]) * bflo(wr[e]); r2 += bfhi(hn[e]) * bfhi(wr[e]); }
    r = wave_sum(r + r2);
    const unsigned rb = f2bf(r);
    const unsigned ord = (rb & 0x8000u) ? (~rb & 0xffffu) : (rb | 0x8000u);
    if (l == 0) { ((u16*)misc)[w] = (u16)ord; if (w == 0) misc[8] = 0; }
    WAIT_L(0);
    BAR();
    if (tid < 4) __hip_atomic_store((gu64*)(ws + WS_GR) + (tt * 16 + g1) * 4 + tid, ((unsigned long long)tag << 32) | misc[tid], RLX_AGENT);
    if (HNPUB) {
      __builtin_amdgcn_raw_buffer_store_b128((u32x4){hn[0], tag, hn[1], tag}, rs, WS_HN + (unsigned)t_own * 2048 + l * 32, 0, 16);
      __builtin_amdgcn_raw_buffer_store_b128((u32x4){hn[2], tag, hn[3], tag}, rs, WS_HN + (unsigned)t_own * 2048 + l * 32 + 16, 0, 16);
    }
#pragma unroll
    for (int e = 0; e < 4; ++e) own[e] = pack2(h[2 * e] + bflo(hn[e]), h[2 * e + 1] + bfhi(hn[e]));
  }
  KEEP(rf);
  {
    const u16* wsrc = down ? w_down + (size_t)(l >> 1) * D + db * 16 + (l & 1) * 8 : ((l & 1) ? w_up : w_gate) + (size_t)(l >> 1) * F + fb * 8;
    if (down) {
#pragma unroll
      for (int i = 0; i < 7; ++i) GLDS16(wsrc + (size_t)(w * 7 + i) * 32 * D, lds + L_W + (w * 7 + i) * 1024, 0);
    } else {
#pragma unroll
      for (int i = 0; i < 2; ++i) GLDS16(wsrc + (size_t)(w * 2 + i) * 32 * F, lds + L_W + (w * 2 + i) * 1024, 0);
    }
  }
  STAMP(2, 1);
  unsigned kk[4];
  {
    u32x4 a;
    for (int it = 0; it < SPIN_MAX; ++it) {
      asm volatile("" ::: "memory");
      a = __builtin_amdgcn_raw_buffer_load_b128(rs, WS_GR + (unsigned)tid * 16, 0, 16);
      if (__all(a[1] == tag && a[3] == tag)) break;
      __builtin_amdgcn_s_sleep(1);
    }
    __builtin_amdgcn_fence(__ATOMIC_ACQUIRE, "wavefront");
    const unsigned ib = 0xffffu - (unsigned)tid * 4;
    kk[0] = (a[0] << 16) | ib; kk[1] = (a[0] & 0xffff0000u) | (ib - 1); kk[2] = (a[2] << 16) | (ib - 2); kk[3] = (a[2] & 0xffff0000u) | (ib - 3);
  }
  STAMP(2, 2);
  {
    const unsigned m = wave_max_u32(umax(umax(kk[0], kk[1]), umax(kk[2], kk[3])));
    if (l == 0) misc[16 + w] = m;
  }
  WAIT_L(0);
  BAR();
  STAMP(2, 5);
  if (b == 0 && tid == 0) __hip_atomic_store((gu32*)(ws + WS_EP), tag, RLX_AGENT);
  {
    const u32x4 ma = *(const u32x4*)(misc + 16), mb2 = *(const u32x4*)(misc + 20);
    const unsigned lo = min(min(min(ma[0], ma[1]), min(ma[2], ma[3])), min(min(mb2[0], mb2[1]), min(mb2[2], mb2[3])));
    if (l < 16) misc[64 + w * 16 + l] = 0u;
    unsigned base = 0;
#pragma unroll
    for (int e = 0; e < 4; ++e) {
      const unsigned long long bm = __ballot(kk[e] >= lo);
      const unsigned pos = base + __builtin_amdgcn_mbcnt_hi((unsigned)(bm >> 32), __builtin_amdgcn_mbcnt_lo((unsigned)bm, 0u));
      if (kk[e] >= lo && pos < 16) misc[64 + w * 16 + pos] = kk[e];
      base += (unsigned)__builtin_popcountll(bm);
    }
    if (base > 16 && l == 0) misc[8] = 1u;
  }
  WAIT_L(0);
  BAR();
  STAMP(2, 6);
  unsigned key;
  bool sel_own;
  {
    const unsigned ovf = __builtin_amdgcn_readfirstlane(misc[8]);
    if (ovf == 0) {
      const unsigned cv = misc[64 + w * 16 + (l >> 2)];
      int rank = 0;
#pragma unroll
      for (int i = 0; i < 8; ++i) {
        const u32x4 c4 = *(const u32x4*)(misc + 64 + (l & 3) * 32 + i * 4);
#pragma unroll
        for (int e = 0; e < 4; ++e) rank += (c4[e] > cv) ? 1 : 0;
      }
      rank += (int)dpp_u<0xB1, 0xf>((unsigned)rank);
      rank += (int)dpp_u<0x4E, 0xf>((unsigned)rank);
      if (cv != 0u && rank < 8 && (l & 3) == 0) misc[32 + rank] = cv;
      WAIT_L(0);
      BAR();
      const unsigned sv = misc[32 + (l & 7)];
      key = (unsigned)__builtin_amdgcn_readlane((int)sv, w);
      sel_own = __any((int)(sv & 0xffffu) == 0xffff - t_own);
    } else {
      unsigned mine = 0;
#pragma unroll 1
      for (int it = 0; it < 8; ++it) {
        const unsigned m = wave_max_u32(umax(umax(kk[0], kk[1]), umax(kk[2], kk[3])));
#pragma unroll
        for (int e = 0; e < 4; ++e) kk[e] = (kk[e] == m) ? 0u : kk[e];
        if (l == it) mine = m;
      }
      if (l < 8) misc[192 + w * 8 + l] = mine;
      WAIT_L(0);
      BAR();
      unsigned cv = misc[192 + l];
      key = 0; sel_own = false;
#pragma unroll 1
      for (int it = 0; it < 8; ++it) {
        const unsigned m = wave_max_u32(cv);
        if (it == w) key = m;
        sel_own |= (int)(m & 0xffffu) == 0xffff - t_own;
        cv = (cv == m) ? 0u : cv;
      }
    }
  }
  STAMP(2, 3);
  const int t_sel = 0xffff - (int)(key & 0xffffu);
  if (HNPUB && !down) {
    u32x4 a, c;
    for (int it = 0; it < SPIN_MAX; ++it) {
      asm volatile("" ::: "memory");
      a = __builtin_amdgcn_raw_buffer_load_b128(rs, WS_HN + (unsigned)t_sel * 2048 + l * 32, 0, 16);
      c = __builtin_amdgcn_raw_buffer_load_b128(rs, WS_HN + (unsigned)t_sel * 2048 + l * 32 + 16, 0, 16);
      if (__all(a[1] == tag && a[3] == tag && c[1] == tag && c[3] == tag)) break;
      __builtin_amdgcn_s_sleep(1);
    }
    *(u32x4*)(lds + L_XS + w * XS_STRIDE + l * 16) = (u32x4){a[0], a[2], c[0], c[2]};
    if (!sel_own) *(u32x4*)(out + (size_t)t_own * D + l * 8) = own;
  } else {
    float h[8];
    const u32x4 xs = *(const u32x4*)(x + (size_t)t_sel * D + l * 8);
    load_h(xs, rs, t_sel, l, h);
    if (!sel_own) *(u32x4*)(out + (size_t)t_own * D + l * 8) = own;
    const u32x4 hn = layernorm8(h);
    if (!down) {
      *(u32x4*)(lds + L_XS + w * XS_STRIDE + l * 16) = hn;
    } else {
      if ((l >> 1) == db) {
        float2* hh = (float2*)(lds + L_HH) + w * 16 + (l & 1) * 8;
#pragma unroll
        for (int e = 0; e < 4; ++e) { hh[2 * e] = make_float2(h[2 * e], bflo(hn[e])); hh[2 * e + 1] = make_float2(h[2 * e + 1], bfhi(hn[e])); }
      }
      if (l == 0) misc[24 + w] = key;
    }
  }
  STAMP(2, 4);
  float* red = (float*)(lds + L_RED);
  if (!down) {
    WAIT_L(0);
    BAR();
    if (w == 0) {
      f32x4 a4[2] = {};
      const char* ap = lds + L_XS + (fr & 7) * XS_STRIDE + q * 16;
      const char* bp = lds + L_W + (q * 8 + (fr >> 2)) * 32 + (fr & 3) * 8;
#pragma unroll
      for (int ks = 0; ks < 16; ++ks) {
        const bf16x8 a = *(const bf16x8*)(ap + ks * 64);
        const v4i16_t g0 = __builtin_amdgcn_ds_read_tr16_b64_v4i16((LAS v4i16_t*)(bp + ks * 1024));
        const v4i16_t g1v = __builtin_amdgcn_ds_read_tr16_b64_v4i16((LAS v4i16_t*)(bp + ks * 1024 + 128));
        const bf16x8 bg = {g0[0], g0[1], g0[2], g0[3], g1v[0], g1v[1], g1v[2], g1v[3]};
        a4[ks & 1] = __builtin_amdgcn_mfma_f32_16x16x32_bf16(a, bg, a4[ks & 1], 0, 0, 0);
      }
      const f32x4 sum = a4[0] + a4[1];
      const bool hi = fr >= 8;
      unsigned actb[2];
#pragma unroll
      for (int k = 0; k < 2; ++k) {
        const float send = hi ? sum[k] : sum[2 + k];
        const float recv = dpp_f<0x128, 0xf>(send);
        const float g = rbf(hi ? recv : sum[k]), u = rbf(hi ? sum[2 + k] : recv);
        const float sg = rbf(g / (1.0f + expf(-g)));
        actb[k] = f2bf(sg * u);
      }
      const unsigned n0 = dpp_u<0xB1, 0xf>(actb[0]), n1 = dpp_u<0xB1, 0xf>(actb[1]);
      const unsigned val = (fr & 1) ? (n1 | (actb[1] << 16)) : (actb[0] | (n0 << 16));
      const int tok = q * 4 + (hi ? 2 : 0) + (fr & 1);
      if (q < 2) __hip_atomic_store((gu64*)(ws + WS_GA) + fb * 32 + tok * 4 + ((fr & 7) >> 1), ((unsigned long long)tag << 32) | val, RLX_AGENT);
    }
    STAMP(3, 0);
  } else {
    for (int it = 0; it < SPIN_MAX; ++it) {
      asm volatile("" ::: "memory");
      bool ok = true;
#pragma unroll
      for (int i = 0; i < 7; ++i) {
        const int rec = w * 28 + i * 4 + q;
        const u32x4 a = __builtin_amdgcn_raw_buffer_load_b128(rs, WS_GA + (unsigned)rec * 256 + fr * 16, 0, 16);
        ok &= (a[1] == tag) & (a[3] == tag);
        *(u32x2*)(lds + L_AS + (fr >> 1) * AS_STRIDE + rec * 16 + (fr & 1) * 8) = (u32x2){a[0], a[2]};
      }
      if (__all(ok)) break;
      __builtin_amdgcn_s_sleep(1);
    }
    STAMP(3, 1);
    WAIT_L(0);
    BAR();
    f32x4 a4 = {};
#pragma unroll
    for (int i = 0; i < 7; ++i) {
      const int ks = w * 7 + i;
      const bf16x8 a = *(const bf16x8*)(lds + L_AS + (fr & 7) * AS_STRIDE + ks * 64 + q * 16);
      const int r0 = ks * 32 + q * 8 + (fr >> 2);
      const int coff = (fr & 3) * 8;
      const v4i16_t g0 = __builtin_amdgcn_ds_read_tr16_b64_v4i16((LAS v4i16_t*)(lds + L_W + r0 * 32 + coff));
      const v4i16_t g1v = __builtin_amdgcn_ds_read_tr16_b64_v4i16((LAS v4i16_t*)(lds + L_W + (r0 + 4) * 32 + coff));
      const bf16x8 bg = {g0[0], g0[1], g0[2], g0[3], g1v[0], g1v[1], g1v[2], g1v[3]};
      a4 = __builtin_amdgcn_mfma_f32_16x16x32_bf16(a, bg, a4, 0, 0, 0);
    }
    if (q < 2) {
#pragma unroll
      for (int jj = 0; jj < 4; ++jj) red[w * 128 + (q * 4 + jj) * 16 + fr] = a4[jj];
    }
    WAIT_L(0);
    BAR();
    if (tid < 128) {
      float s = 0.f;
#pragma unroll
      for (int ww = 0; ww < 8; ++ww) s += red[ww * 128 + tid];
      const unsigned k2 = misc[24 + (tid >> 4)];
      const int t = 0xffff - (int)(k2 & 0xffffu);
      const unsigned ord = k2 >> 16;
      const unsigned rb = (ord & 0x8000u) ? (ord & 0x7fffu) : (~ord & 0xffffu);
      const float sig = rbf(1.0f / (1.0f + expf(-bf2f(rb))));
      const float upd = rbf(rbf(s) * sig);
      const float2 hh = ((const float2*)(lds + L_HH))[tid];
      out[(size_t)t * D + db * 16 + (tid & 15)] = (u16)f2bf(hh.x + rbf(hh.y + upd));
    }
    STAMP(3, 2);
  }
  STAMP_FLUSH;
  asm volatile(".Lfused_code_hi:");
}

extern "C" void kernel_launch(void* const* d_in, const int* in_sizes, int n_in, void* d_out, int out_size, void* d_ws,
                              size_t ws_size, hipStream_t stream) {
  fused<<<256, 512, 0, stream>>>((const u16*)d_in[0], (const u16*)d_in[1], (const u16*)d_in[2], (const u16*)d_in[3], (const u16*)d_in[4],
                                 (const u16*)d_in[6], (const u16*)d_in[7], (const u16*)d_in[8], (const u16*)d_in[9], (u16*)d_out, (char*)d_ws);
}
```

```cpp
#include <hip/hip_runtime.h>
#include <hip/hip_bf16.h>
#include <stdint.h>
#include <utility>

typedef unsigned short u16;
typedef __attribute__((ext_vector_type(8))) short bf16x8;
typedef __attribute__((ext_vector_type(4))) short v4i16_t;
typedef __attribute__((ext_vector_type(4))) float f32x4;
typedef __attribute__((ext_vector_type(4))) unsigned u32x4;
typedef __attribute__((ext_vector_type(2))) unsigned u32x2;
#define GAS __attribute__((address_space(1)))
#define LAS __attribute__((address_space(3)))
typedef GAS unsigned gu32;
typedef GAS unsigned long long gu64;
#define RLX_AGENT __ATOMIC_RELAXED, __HIP_MEMORY_SCOPE_AGENT

constexpr int S = 2048, D = 512, F = 1792;

__device__ __forceinline__ float bf2f(unsigned v) { return __uint_as_float(v << 16); }
__device__ __forceinline__ float bflo(unsigned v) { return __uint_as_float(v << 16); }
__device__ __forceinline__ float bfhi(unsigned v) { return __uint_as_float(v & 0xffff0000u); }
__device__ __forceinline__ unsigned f2bf(float f) { __bf16 b = (__bf16)f; return (unsigned)__builtin_bit_cast(u16, b); }
__device__ __forceinline__ float rbf(float f) { return bf2f(f2bf(f)); }
__device__ __forceinline__ unsigned pack2(float lo, float hi) { return f2bf(lo) | (f2bf(hi) << 16); }

#define WAIT_V(n) asm volatile("s_waitcnt vmcnt(%0)" ::"n"(n) : "memory")
#define WAIT_L(n) asm volatile("s_waitcnt lgkmcnt(%0)" ::"n"(n) : "memory")
#define BAR() __builtin_amdgcn_s_barrier()
#define GLDS16(src, dst, aux) __builtin_amdgcn_global_load_lds((const unsigned*)(src), (unsigned*)(dst), 16, 0, aux)
#define TOUCH(reg, ptr) asm volatile("global_load_dword %0, %1, off" : "+v"(reg) : "v"(ptr) : "memory")
#define TOUCH_SC1(reg, ptr) asm volatile("global_load_dword %0, %1, off sc1" : "+v"(reg) : "v"(ptr) : "memory")
#define TOUCH_NT(reg, ptr) asm volatile("global_load_dword %0, %1, off nt" : "+v"(reg) : "v"(ptr) : "memory")
#define KEEP(reg) asm volatile("" ::"v"(reg))

template <int CTRL, int RM> __device__ __forceinline__ float dpp_f(float v) {
  return __builtin_bit_cast(float, __builtin_amdgcn_update_dpp(0, __builtin_bit_cast(int, v), CTRL, RM, 0xf, true));
}
__device__ __forceinline__ float wave_sum(float v) {
  v += dpp_f<0x111, 0xf>(v); v += dpp_f<0x112, 0xf>(v); v += dpp_f<0x114, 0xf>(v); v += dpp_f<0x118, 0xf>(v);
  v += dpp_f<0x142, 0xa>(v); v += dpp_f<0x143, 0xc>(v);
  return __builtin_bit_cast(float, __builtin_amdgcn_readlane(__builtin_bit_cast(int, v), 63));
}
template <int CTRL, int RM> __device__ __forceinline__ unsigned dpp_u(unsigned v) {
  return (unsigned)__builtin_amdgcn_update_dpp(0, (int)v, CTRL, RM, 0xf, true);
}
__device__ __forceinline__ unsigned umax(unsigned a, unsigned b) { return a > b ? a : b; }
__device__ __forceinline__ unsigned wave_max_u32(unsigned v) {
  v = umax(v, dpp_u<0x111, 0xf>(v)); v = umax(v, dpp_u<0x112, 0xf>(v)); v = umax(v, dpp_u<0x114, 0xf>(v)); v = umax(v, dpp_u<0x118, 0xf>(v));
  v = umax(v, dpp_u<0x142, 0xa>(v)); v = umax(v, dpp_u<0x143, 0xc>(v));
  return (unsigned)__builtin_amdgcn_readlane((int)v, 63);
}

#define STAMP_DECL
#define STAMPW(k, i, wv)
#define STAMP_FLUSH
#define STAMP(k, i) STAMPW(k, i, 0)

constexpr unsigned WS_ABT = 0;
constexpr unsigned WS_Y = WS_ABT + 1024u * 2048 * 2;
constexpr unsigned WS_F1 = WS_Y + 4u * 2048 * 512 * 2;
constexpr unsigned WS_F2 = WS_F1 + 4096;
constexpr unsigned WS_GR = WS_F2 + 2048;
constexpr unsigned WS_GA = WS_GR + 1024 * 8;
constexpr unsigned WS_EP = WS_GA + 224 * 256;
constexpr unsigned WS_HN = WS_EP + 256;
constexpr unsigned WS_END = WS_HN + 2048u * 2048;

constexpr int SPIN_MAX = 1 << 18;
#define AUX1 0
#define CPF 1
#define FMODE 1
#define HNPUB 0
#define TLBW 1
#define TS 3
#define QMODE 0
#define NPLAIN 8
#define T1MODE 1
#define FFNSTEP 2

constexpr int L_QR = 81920;
constexpr int L_RST = 65536;
constexpr int L_TAG = 66560;
constexpr int L_W = 16384;
constexpr int L_T2 = 131072;
constexpr int XS_STRIDE = 1024 + 16;
constexpr int L_XS = 81920;
constexpr int L_RED = L_XS + 8 * XS_STRIDE;
constexpr int L_MISC = L_RED + 8 * 128 * 4;
constexpr int L_HH = L_MISC + 1024;
constexpr int AS_STRIDE = F * 2 + 16;
constexpr int L_AS = L_HH + 1024;
static_assert(L_AS + 8 * AS_STRIDE <= L_T2, "lds");

struct Ring2 {
  static constexpr int p(int T) { return (T % 5) * 16384; }
  static constexpr int q(int T) { return L_QR + ((T + 3) % 5) * 16384; }
};
constexpr int koff(int T) { return T < 8 ? T * 128 : (T - 8) * 128 + 64; }
constexpr int x1slot(int s) { return L_QR + (s % 5) * 16384; }

template <int BP, int BQ, int NKT, class RG, bool STATS>
struct Cons {
  static constexpr int TP = BP / 32, TQ = BQ / 32;
  struct Ctx { const char* lds; int poff[TP][2]; int qoff[TQ][2]; };
  struct Fr { bf16x8 pf[2][TP], qf[2][TQ]; };
  struct St { f32x4 s1[2], s2[2], sw[TQ]; };
  template <int T> static __device__ __forceinline__ void readf(const Ctx& c, Fr& f) {
#pragma unroll
    for (int ks = 0; ks < 2; ++ks) {
#pragma unroll
      for (int m = 0; m < TP; ++m) f.pf[ks][m] = *(const bf16x8*)(c.lds + RG::p(T) + c.poff[m][ks]);
#pragma unroll
      for (int n = 0; n < TQ; ++n) f.qf[ks][n] = *(const bf16x8*)(c.lds + RG::q(T) + c.qoff[n][ks]);
    }
  }
  template <int T> static __device__ __forceinline__ void step(const Ctx& c, f32x4 (&acc)[TP][TQ], Fr& cur, Fr& nxt, St& st) {
    if constexpr (T + 1 < NKT) {
      WAIT_L(0);
      BAR();
      __builtin_amdgcn_sched_barrier(0);
      readf<T + 1>(c, nxt);
    }
    if constexpr (T + 1 == NKT) return;
#pragma unroll
    for (int ks = 0; ks < 2; ++ks) {
#pragma unroll
      for (int m = 0; m < TP; ++m)
#pragma unroll
        for (int n = 0; n < TQ; ++n) acc[m][n] = __builtin_amdgcn_mfma_f32_16x16x32_bf16(cur.pf[ks][m], cur.qf[ks][n], acc[m][n], 0, 0, 0);
      if constexpr (STATS) {
        const bf16x8 ones = {0x3f80, 0x3f80, 0x3f80, 0x3f80, 0x3f80, 0x3f80, 0x3f80, 0x3f80};
#pragma unroll
        for (int m = 0; m < 2; ++m) {
          st.s1[m] = __builtin_amdgcn_mfma_f32_16x16x32_bf16(cur.pf[ks][m], ones, st.s1[m], 0, 0, 0);
          st.s2[m] = __builtin_amdgcn_mfma_f32_16x16x32_bf16(cur.pf[ks][m], cur.pf[ks][m], st.s2[m], 0, 0, 0);
        }
#pragma unroll
        for (int n = 0; n < TQ; ++n) st.sw[n] = __builtin_amdgcn_mfma_f32_16x16x32_bf16(ones, cur.qf[ks][n], st.sw[n], 0, 0, 0);
      }
    }
    if constexpr (T + 1 < NKT) {
#pragma unroll
      for (int i = 0; i < 2 * (TP + TQ); ++i) { __builtin_amdgcn_sched_group_barrier(0x100, 1, 0); __builtin_amdgcn_sched_group_barrier(0x8, 1, 0); }
    }
    __builtin_amdgcn_sched_barrier(0);
  }
  template <int... Ts>
  static __device__ __forceinline__ void steps(const Ctx& c, f32x4 (&acc)[TP][TQ], Fr& f0, Fr& f1, St& st, std::integer_sequence<int, Ts...>) {
    ((Ts & 1 ? step<Ts>(c, acc, f1, f0, st) : step<Ts>(c, acc, f0, f1, st)), ...);
  }
  static __device__ __forceinline__ void run(const char* lds, int wp, int wq, int rot, int l, f32x4 (&acc)[TP][TQ], St& st, Fr& f1) {
    Ctx c; c.lds = lds;
    const int fr = l & 15, q = l >> 4;
#pragma unroll
    for (int ks = 0; ks < 2; ++ks) {
#pragma unroll
      for (int m = 0; m < TP; ++m) { const int row = wp * (BP / 2) + ((m + rot) & (TP - 1)) * 16 + fr; c.poff[m][ks] = row * 128 + (((ks * 4 + q) ^ ((row >> 1) & 7)) * 16); }
#pragma unroll
      for (int n = 0; n < TQ; ++n) { const int row = wq * (BQ / 2) + n * 16 + fr; c.qoff[n][ks] = row * 128 + (((ks * 4 + q) ^ ((row >> 1) & 7)) * 16); }
    }
    Fr f0;
    BAR();
    readf<0>(c, f0);
    steps(c, acc, f0, f1, st, std::make_integer_sequence<int, NKT>{});
  }
};

struct L1Ctx { const u16* xsrc[4]; const u16* wsrc[4]; char* lds; int w; };
template <int s> __device__ __forceinline__ void l1_issue(const L1Ctx& c) {
#pragma unroll
  for (int h = 0; h < 2; ++h) {
#pragma unroll
    for (int i = 0; i < 4; ++i) GLDS16(c.xsrc[i] + (size_t)(s >> 2) * 64 * D + (s & 3) * 128 + h * 64, c.lds + x1slot(s) + h * 8192 + (c.w * 4 + i) * 1024, AUX1);
    if constexpr (s < 4) {
#pragma unroll
      for (int i = 0; i < 4; ++i) GLDS16(c.wsrc[i] + s * 128 + h * 64, c.lds + (2 * s + h) * 8192 + (c.w * 4 + i) * 1024, AUX1);
    }
  }
}
struct Fr1 { bf16x8 pf[4][2], qf[4][2]; };
struct C1Ctx { const char* lds; int poff[2][2]; int qoff[2][2]; };
template <int s> __device__ __forceinline__ void c1_read(const C1Ctx& c, Fr1& f) {
#pragma unroll
  for (int ks = 0; ks < 4; ++ks) {
#pragma unroll
    for (int m = 0; m < 2; ++m) f.pf[ks][m] = *(const bf16x8*)(c.lds + x1slot(s) + (ks >> 1) * 8192 + c.poff[m][ks & 1]);
#pragma unroll
    for (int n = 0; n < 2; ++n) f.qf[ks][n] = *(const bf16x8*)(c.lds + (2 * (s & 3) + (ks >> 1)) * 8192 + c.qoff[n][ks & 1]);
  }
}
struct Acc1 { f32x4 acc[2][2], s1, s2; };
template <int s> __device__ __forceinline__ void c1_step(const C1Ctx& c, Acc1& a, f32x4 (&sw)[2], Fr1& cur, Fr1& nxt) {
  if constexpr (s == 7) WAIT_V(0);
  WAIT_L(0);
  BAR();
  __builtin_amdgcn_sched_barrier(0);
  if constexpr (s + 1 < 8) c1_read<s + 1>(c, nxt);
  const bf16x8 ones = {0x3f80, 0x3f80, 0x3f80, 0x3f80, 0x3f80, 0x3f80, 0x3f80, 0x3f80};
#pragma unroll
  for (int ks = 0; ks < 4; ++ks) {
#pragma unroll
    for (int m = 0; m < 2; ++m)
#pragma unroll
      for (int n = 0; n < 2; ++n) a.acc[m][n] = __builtin_amdgcn_mfma_f32_16x16x32_bf16(cur.pf[ks][m], cur.qf[ks][n], a.acc[m][n], 0, 0, 0);
    a.s1 = __builtin_amdgcn_mfma_f32_16x16x32_bf16(cur.pf[ks][0], ones, a.s1, 0, 0, 0);
    a.s2 = __builtin_amdgcn_mfma_f32_16x16x32_bf16(cur.pf[ks][0], cur.pf[ks][0], a.s2, 0, 0, 0);
    if constexpr (s < 4) {
#pragma unroll
      for (int n = 0; n < 2; ++n) sw[n] = __builtin_amdgcn_mfma_f32_16x16x32_bf16(ones, cur.qf[ks][n], sw[n], 0, 0, 0);
    }
  }
  if constexpr (s + 1 < 8) {
#pragma unroll
    for (int i = 0; i < 16; ++i) { __builtin_amdgcn_sched_group_barrier(0x100, 1, 0); __builtin_amdgcn_sched_group_barrier(0x8, 1, 0); }
  }
  __builtin_amdgcn_sched_barrier(0);
}

struct L2Ctx { const u16* psrc[8]; const u16* qsrc[8]; char* lds; int w; };
template <int T> __device__ __forceinline__ void l2_issueP(const L2Ctx& c) {
#pragma unroll
  for (int i = 0; i < 8; ++i) GLDS16(c.psrc[i] + koff(T), c.lds + Ring2::p(T) + (c.w * 8 + i) * 1024, 16);
}
template <int T, int AUX> __device__ __forceinline__ void l2_issueQ(const L2Ctx& c) {
#pragma unroll
  for (int i = 0; i < 8; ++i) GLDS16(c.qsrc[i] + koff(T), c.lds + Ring2::q(T) + (c.w * 8 + i) * 1024, AUX);
}
constexpr int l2_cnt(int s) { return s < 5 ? 8 : 16; }
constexpr int l2_younger(int T) { int up = T + 4 < 15 ? T + 4 : 15; int n = 0; for (int s = T + 2; s <= up; ++s) n += l2_cnt(s); return n; }
template <int T, int AUX> __device__ __forceinline__ void l2_step(const L2Ctx& c) {
  WAIT_V(l2_younger(T));
  BAR();
  if constexpr (T + 5 < 16) { l2_issueP<T + 5>(c); l2_issueQ<T + 5, AUX>(c); }
}
template <int AUX, int... Ts> __device__ __forceinline__ void l2_steps(const L2Ctx& c, std::integer_sequence<int, Ts...>) { (l2_step<Ts, AUX>(c), ...); }

__device__ __forceinline__ u32x4 widen16(u32x2 a, u32x2 c) {
  const auto s0 = __builtin_amdgcn_permlane16_swap(a[0], c[0], false, false);
  const auto s1 = __builtin_amdgcn_permlane16_swap(a[1], c[1], false, false);
  return (u32x4){s0[0], s1[0], s0[1], s1[1]};
}

__device__ __forceinline__ void poll_words(const unsigned* p, unsigned tag) {
  for (int it = 0; it < SPIN_MAX; ++it) {
    const unsigned v = __hip_atomic_load((gu32*)p, RLX_AGENT);
    if (__all(v == tag)) break;
    __builtin_amdgcn_s_sleep(1);
  }
  __builtin_amdgcn_fence(__ATOMIC_ACQUIRE, "wavefront");
}

__device__ __forceinline__ unsigned add_pk(unsigned a, unsigned b) { return pack2(bflo(a) + bflo(b), bfhi(a) + bfhi(b)); }
__device__ __forceinline__ unsigned sub_pk(unsigned a, unsigned b) { return pack2(bflo(a) - bflo(b), bfhi(a) - bfhi(b)); }
template <class R>
__device__ __forceinline__ void load_h(const u32x4 xv, R rs, int t, int l, float (&h)[8]) {
  const unsigned o = (unsigned)(t * D + l * 8) * 2;
  const u32x4 y0 = __builtin_amdgcn_raw_buffer_load_b128(rs, WS_Y + o, 0, 16);
  const u32x4 y1 = __builtin_amdgcn_raw_buffer_load_b128(rs, WS_Y + 1u * S * D * 2 + o, 0, 16);
  const u32x4 y2 = __builtin_amdgcn_raw_buffer_load_b128(rs, WS_Y + 2u * S * D * 2 + o, 0, 16);
  const u32x4 y3 = __builtin_amdgcn_raw_buffer_load_b128(rs, WS_Y + 3u * S * D * 2 + o, 0, 16);
#pragma unroll
  for (int e = 0; e < 4; ++e) {
    const unsigned hv = add_pk(xv[e], sub_pk(add_pk(y0[e], y1[e]), add_pk(y2[e], y3[e])));
    h[2 * e] = bflo(hv); h[2 * e + 1] = bfhi(hv);
  }
}
__device__ __forceinline__ u32x4 layernorm8(const float (&h)[8]) {
  const float pv = __builtin_bit_cast(float, __builtin_amdgcn_readfirstlane(__builtin_bit_cast(int, h[0])));
  float s = 0.f, s2 = 0.f, v = 0.f, v2 = 0.f;
#pragma unroll
  for (int e = 0; e < 4; ++e) { const float a = h[2 * e] - pv, c = h[2 * e + 1] - pv; s += a; s2 += c; v += a * a; v2 += c * c; }
  const float md = wave_sum(s + s2) * (1.0f / D);
  v = fmaxf(wave_sum(v + v2) - md * md * (float)D, 0.f);
  const float mean = pv + md;
  const float mb = rbf(mean);
  const float sd = rbf(sqrtf(rbf(rbf(v * (1.0f / D)) + 1e-5f)));
  const float inv = 1.0f / sd;
  u32x4 o;
#pragma unroll
  for (int e = 0; e < 4; ++e) {
    const unsigned d = pack2(h[2 * e] - mb, h[2 * e + 1] - mb);
    const float a = bflo(d), c = bfhi(d);
    float qa = a * inv, qc = c * inv;
    qa = fmaf(fmaf(-qa, sd, a), inv, qa); qc = fmaf(fmaf(-qc, sd, c), inv, qc);
    o[e] = pack2(qa, qc);
  }
  return o;
}

__global__ __launch_bounds__(512) void fused(const u16* __restrict__ x, const u16* __restrict__ ws_re, const u16* __restrict__ ws_im,
                                             const u16* __restrict__ wd_re, const u16* __restrict__ wd_im, const u16* __restrict__ w_r,
                                             const u16* __restrict__ w_gate, const u16* __restrict__ w_up, const u16* __restrict__ w_down,
                                             u16* __restrict__ out, char* __restrict__ ws) {
  __shared__ __attribute__((aligned(1024))) char lds[163840];
  const int b = blockIdx.x, xcd = b & 7, j = b >> 3;
  const int cc = xcd >> 2, et = j & 3, tt = (xcd & 3) * 4 + ((j >> 2) & 3), kh = j >> 4;
  const int g1 = (cc * 4 + et) * 2 + kh;
  const int e1 = cc * 8 + et * 2 + (tt & 1), s1 = kh * 8 + (tt >> 1);
  const int tid = threadIdx.x, w = __builtin_amdgcn_readfirstlane(tid >> 6), l = tid & 63, fr = l & 15, q = l >> 4;
  const u16* wsb = cc ? ws_im : ws_re;
  const u16* wdb = cc ? wd_im : wd_re;
  const bool down = j >= 28;
  const int fb = xcd * 28 + j, db = xcd * 4 + (j - 28);
  const auto rs = __builtin_amdgcn_make_buffer_rsrc(ws, 0, WS_END, 0x00020000);
  unsigned tag;
  STAMP_DECL;
  STAMP(0, 0);
  unsigned long long code_lo; unsigned code_len;
  asm volatile("s_getpc_b64 %0\n.Lfused_code_lo:\n\ts_mov_b32 %1, .Lfused_code_hi-.Lfused_code_lo" : "=s"(code_lo), "=s"(code_len));

  if (w < 4) {
    const int wp = w >> 1, wq = w & 1;
    unsigned r0 = 0;
    {
      const int row = tid >> 3, ln = tid & 7;
      const int xs_tile = ((j >> 3) & 1) * 8 + (xcd & 3) * 2 + ((j >> 2) & 1);
      const u16* p = j < 16 ? x + (size_t)(xs_tile * 128 + (j & 3) * 32 + row) * D + ln * 64 : wdb + (size_t)((j - 16) * 32 + row) * D + ln * 64;
      if (T1MODE == 1) TOUCH(r0, p);
      if (T1MODE == 2) TOUCH_NT(r0, p);
    }
    {
      C1Ctx c; c.lds = lds;
#pragma unroll
      for (int ks = 0; ks < 2; ++ks) {
#pragma unroll
        for (int m = 0; m < 2; ++m) { const int row = wp * 32 + (m ^ wq) * 16 + fr; c.poff[m][ks] = row * 128 + (((ks * 4 + q) ^ ((row >> 1) & 7)) * 16); }
#pragma unroll
        for (int n = 0; n < 2; ++n) { const int row = wq * 32 + n * 16 + fr; c.qoff[n][ks] = row * 128 + (((ks * 4 + q) ^ ((row >> 1) & 7)) * 16); }
      }
      f32x4 sw[2] = {};
      Fr1 f0, f1;
      auto epilogue = [&](const Acc1& a, int pass) {
        float2* rst = (float2*)(lds + L_RST) + pass * 64;
        if ((fr >> 2) == q) {
          const int jj = fr & 3;
          const float s1v = jj == 0 ? a.s1[0] : jj == 1 ? a.s1[1] : jj == 2 ? a.s1[2] : a.s1[3];
          const float s2v = jj == 0 ? a.s2[0] : jj == 1 ? a.s2[1] : jj == 2 ? a.s2[2] : a.s2[3];
          const float mean = s1v * (1.0f / D);
          const float var = fmaxf(s2v * (1.0f / D) - mean * mean, 0.f);
          const float sd = rbf(sqrtf(rbf(rbf(var) + 1e-5f)));
          rst[wp * 32 + wq * 16 + fr] = make_float2(rbf(mean), 1.0f / sd);
        }
        WAIT_L(0);
        BAR();
        u32x2 pk[2][2];
#pragma unroll
        for (int m = 0; m < 2; ++m) {
          const int p0 = wp * 32 + (m ^ wq) * 16 + q * 4;
          const f32x4 sa = *(const f32x4*)(rst + p0), sb = *(const f32x4*)(rst + p0 + 2);
#pragma unroll
          for (int n = 0; n < 2; ++n) {
            const float swv = sw[n][0];
            const float v0 = (a.acc[m][n][0] - sa[0] * swv) * sa[1], v1 = (a.acc[m][n][1] - sa[2] * swv) * sa[3];
            const float v2 = (a.acc[m][n][2] - sb[0] * swv) * sb[1], v3 = (a.acc[m][n][3] - sb[2] * swv) * sb[3];
            pk[m][n] = (u32x2){pack2(v0, v1), pack2(v2, v3)};
          }
        }
        const int scol = s1 * 128 + pass * 64 + wp * 32 + ((q & 1) ^ wq) * 16 + (q >> 1) * 8;
#pragma unroll
        for (int n = 0; n < 2; ++n)
          __builtin_amdgcn_raw_buffer_store_b128(widen16(pk[0][n], pk[1][n]), rs, WS_ABT + (unsigned)((e1 * 64 + wq * 32 + n * 16 + fr) * S + scol) * 2, 0, 16);
      };
      BAR();
      c1_read<0>(c, f0);
      {
        Acc1 a = {};
        c1_step<0>(c, a, sw, f0, f1); c1_step<1>(c, a, sw, f1, f0); c1_step<2>(c, a, sw, f0, f1); c1_step<3>(c, a, sw, f1, f0);
        STAMP(0, 2);
        epilogue(a, 0);
        STAMP(0, 3);
      }
      {
        Acc1 a = {};
        c1_step<4>(c, a, sw, f0, f1); c1_step<5>(c, a, sw, f1, f0); c1_step<6>(c, a, sw, f0, f1); c1_step<7>(c, a, sw, f1, f0);
        STAMP(0, 4);
        epilogue(a, 1);
        STAMP(0, 5);
      }
      tag = __builtin_amdgcn_readfirstlane(*(const unsigned*)(lds + L_TAG));
      WAIT_V(0);
      KEEP(r0);
      STAMP(0, 6);
      BAR();
    }
    using C2 = Cons<128, 128, 16, Ring2, false>;
    f32x4 acc[4][4] = {};
    C2::St st;
    C2::Fr fl;
    C2::run(lds, wp, wq, 0, l, acc, st, fl);
    STAMP(1, 2);
    {
      const unsigned yo = WS_Y + (unsigned)(cc * 2 + kh) * S * D * 2;
      const int chunk = (q >> 1) + 2 * (q & 1);
#pragma unroll
      for (int n = 0; n < 4; ++n) {
#pragma unroll
        for (int ks = 0; ks < 2; ++ks)
#pragma unroll
          for (int m = 0; m < 4; ++m) acc[m][n] = __builtin_amdgcn_mfma_f32_16x16x32_bf16(fl.pf[ks][m], fl.qf[ks][n], acc[m][n], 0, 0, 0);
#pragma unroll
        for (int mp = 0; mp < 2; ++mp) {
          const u32x2 a = {pack2(acc[2 * mp][n][0], acc[2 * mp][n][1]), pack2(acc[2 * mp][n][2], acc[2 * mp][n][3])};
          const u32x2 c = {pack2(acc[2 * mp + 1][n][0], acc[2 * mp + 1][n][1]), pack2(acc[2 * mp + 1][n][2], acc[2 * mp + 1][n][3])};
          const int row = wq * 64 + n * 16 + fr;
          __builtin_amdgcn_raw_buffer_store_b128(widen16(a, c), rs, yo + (unsigned)((tt * 128 + row) * D + et * 128 + wp * 64 + mp * 32 + chunk * 8) * 2, 0, 16);
        }
      }
    }
    STAMP(1, 3);
    WAIT_V(0);
    STAMP(1, 6);
  } else if (w < 6) {
    const int lw = w - 4;
    const int r8 = l >> 3, ch = l & 7;
    {
      L1Ctx c; c.lds = lds; c.w = lw;
      const u16* P = x + (size_t)s1 * 128 * D;
      const u16* Q = wdb + (size_t)(e1 & 7) * 64 * D;
#pragma unroll
      for (int i = 0; i < 4; ++i) {
        const int row = (lw * 4 + i) * 8 + r8;
        const size_t o = (size_t)row * D + ((ch ^ ((row >> 1) & 7)) * 8);
        c.xsrc[i] = P + o; c.wsrc[i] = Q + o;
      }
      l1_issue<0>(c); l1_issue<1>(c);
      WAIT_V(16); STAMPW(0, 1, 4); BAR();
      l1_issue<2>(c);
      WAIT_V(16); BAR();
      l1_issue<3>(c);
      WAIT_V(16); BAR();
      l1_issue<4>(c); l1_issue<5>(c);
      WAIT_V(16); BAR();
      l1_issue<6>(c); l1_issue<7>(c);
      WAIT_V(24); BAR();
      BAR();
      WAIT_V(16); BAR();
      WAIT_V(8); BAR();
      WAIT_V(0); BAR();
    }
    L2Ctx c2;
    c2.lds = lds; c2.w = lw;
    {
      const u16* P = (const u16*)(ws + WS_ABT) + (size_t)(cc * 512 + et * 128) * S + kh * 1024;
      const u16* Q = wsb + (size_t)tt * 128 * S + kh * 1024;
#pragma unroll
      for (int i = 0; i < 8; ++i) {
        const int row = (lw * 8 + i) * 8 + r8;
        const size_t o = (size_t)row * S + ((ch ^ ((row >> 1) & 7)) * 8);
        c2.psrc[i] = P + o; c2.qsrc[i] = Q + o;
      }
    }
    tag = __builtin_amdgcn_readfirstlane(*(const unsigned*)(lds + L_TAG));
    BAR();
    BAR();
    poll_words((const unsigned*)(ws + WS_F1) + g1 * 32 + fr, tag);
    STAMPW(1, 0, 4);
    l2_issueP<0>(c2); l2_issueP<1>(c2); l2_issueP<2>(c2);
    BAR();
    WAIT_V(16);
    STAMPW(1, 1, 4);
    BAR();
    l2_issueP<3>(c2); l2_issueP<4>(c2);
    l2_steps<0>(c2, std::make_integer_sequence<int, 15>{});
  } else if (w == 6) {
    const int r8 = l >> 3, ch = l & 7;
    const unsigned ep = __hip_atomic_load((gu32*)(ws + WS_EP), RLX_AGENT);
    const u16* Q = wsb + (size_t)tt * 128 * S + kh * 1024;
    const u16* qa = Q + (size_t)r8 * S + ((ch ^ (r8 >> 1)) * 8);
    const u16* qb = Q + (size_t)(8 + r8) * S + ((ch ^ (4 + (r8 >> 1))) * 8);
    auto issueQ = [&](int T) {
#pragma unroll
      for (int i = 0; i < 8; ++i) {
        if (QMODE == 0 || (QMODE == 1 && 2 * i < NPLAIN)) {
          GLDS16(qa + (size_t)i * 16 * S + T * 128, lds + L_QR + ((T + 3) % 5) * 16384 + (2 * i) * 1024, 0);
          GLDS16(qb + (size_t)i * 16 * S + T * 128, lds + L_QR + ((T + 3) % 5) * 16384 + (2 * i + 1) * 1024, 0);
        } else {
          GLDS16(qa + (size_t)i * 16 * S + T * 128, lds + L_QR + ((T + 3) % 5) * 16384 + (2 * i) * 1024, 2);
          GLDS16(qb + (size_t)i * 16 * S + T * 128, lds + L_QR + ((T + 3) % 5) * 16384 + (2 * i + 1) * 1024, 2);
        }
      }
    };
    BAR(); BAR();
    tag = __builtin_amdgcn_readfirstlane(ep * 1664525u + 1013904223u);
    *(unsigned*)(lds + L_TAG) = tag;
    WAIT_L(0);
    BAR(); BAR(); BAR();
    BAR();
    BAR(); issueQ(0);
    BAR(); issueQ(1);
    BAR(); issueQ(2);
    BAR(); BAR();
    BAR(); issueQ(3); issueQ(4);
    WAIT_V(32);
    BAR();
    const unsigned* fB = (const unsigned*)(ws + WS_F1) + 512 + g1 * 32 + fr;
    BAR(); BAR();
    const unsigned vB = __hip_atomic_load((gu32*)fB, RLX_AGENT);
    WAIT_V(17);
    BAR();
    if (!__all(vB == tag)) poll_words(fB, tag);
    WAIT_V(0);
    BAR();
#pragma unroll
    for (int T = 4; T < 15; ++T) BAR();
  } else {
    unsigned r[4] = {0, 0, 0, 0};
    constexpr int LPI = 64 / (2 * NPLAIN) > 0 ? 64 / (2 * NPLAIN) : 1;
    const int pr = j * NPLAIN + (l % NPLAIN);
    const int prow = (pr / (NPLAIN * 8)) * 128 + (pr % (NPLAIN * 8));
    const u16* wb = wsb + (size_t)((xcd & 3) * 512 + prow) * S + ((l / NPLAIN) & 1) * 1024 + (l / (2 * NPLAIN)) * 128;
#define TW(i) TOUCH(r[(i) & 1], wb + ((i) % (8 / LPI)) * (LPI * 128) + ((i) / (8 / LPI)) * 64)
    auto ffn_ptr = [&](int i) -> const char* {
      const int li = j * 192 + i * 64 + l;
      if (li < 4096) { const int mrow = (li & 2047) >> 2, c4 = li & 3; return (const char*)((li < 2048 ? w_gate : w_up) + (size_t)mrow * F + xcd * 224 + c4 * 64); }
      const int rr = li - 4096;
      return (const char*)(w_down + (size_t)(rr < F ? rr : 0) * D + xcd * 64);
    };
    if (TLBW) {
      TOUCH_SC1(r[3], ws + (size_t)l * 65536);
      TOUCH_SC1(r[3], ws + WS_Y + (size_t)l * 65536);
      TOUCH_SC1(r[3], ws + WS_Y + (size_t)(64 + l) * 65536);
      TOUCH_SC1(r[3], (l < 32 ? (const char*)out + (size_t)l * 65536 : l < 34 ? (const char*)ws + WS_F1 + (size_t)(l - 32) * 65536
                       : (const char*)wsb + (size_t)(xcd & 3) * 2097152 + (size_t)(l - 34) * 65536));
    }
    if (CPF) {
      const unsigned off = (unsigned)(j * 16 + fr) * 128u;
      const char* cp = (const char*)code_lo + (off < code_len ? off : 0u);
      if (l < 16) TOUCH(r[3], cp);
    }
    const char* fp0 = ffn_ptr(0); const char* fp1 = ffn_ptr(1); const char* fp2 = ffn_ptr(2);
    const u16* wb2 = wsb + (size_t)((xcd & 3) * 512 + j * 16 + fr) * S + ((l >> 4) & 1) * 1024 + (l >> 5) * 128;
#define TF(i) TOUCH(r[(i) & 1], wb2 + ((i) & 3) * 256 + ((i) >> 2) * 64)
#define TFF(p) TOUCH(r[2], p)
    BAR(); BAR(); BAR(); TFF(fp0);
    tag = __builtin_amdgcn_readfirstlane(*(const unsigned*)(lds + L_TAG));
    BAR(); TFF(fp1); TF(0); BAR(); TFF(fp2); TF(1); BAR(); TF(2);
    BAR(); TF(3); BAR(); TF(4); BAR(); TF(5);
    BAR();
    if (l == 0) __hip_atomic_store((gu32*)(ws + WS_F1) + g1 * 32 + tt, tag, RLX_AGENT);
    TF(6);
    BAR(); TF(7);
    BAR();
    if (l == 0) __hip_atomic_store((gu32*)(ws + WS_F1) + 512 + g1 * 32 + tt, tag, RLX_AGENT);
#pragma unroll
    for (int T = 0; T < 16; ++T) {
      BAR();
    }
    WAIT_V(0);
    KEEP(r[0]); KEEP(r[1]); KEEP(r[2]); KEEP(r[3]);
  }
  const int t_own = tt * 128 + g1 * 8 + w;
  unsigned rf = 0;
  const u32x4 wr = *(const u32x4*)(w_r + l * 8);
  const u32x4 x_own = *(const u32x4*)(x + (size_t)t_own * D + l * 8);
  {
    BAR();
    if (tid == 0) __hip_atomic_store((gu32*)(ws + WS_F2) + tt * 32 + g1, tag, RLX_AGENT);
    STAMP(1, 4);
    if (w == 0) poll_words((const unsigned*)(ws + WS_F2) + tt * 32 + fr, tag);
    if (FMODE == 0) {
    if (w >= 5) {
      const int li = j * 192 + (w - 5) * 64 + l;
      const char* p2;
      if (li < 4096) { const int mrow = (li & 2047) >> 2, c4 = li & 3; p2 = (const char*)((li < 2048 ? w_gate : w_up) + (size_t)mrow * F + xcd * 224 + c4 * 64); }
      else { const int rr = li - 4096; p2 = (const char*)(w_down + (size_t)(rr < F ? rr : 0) * D + xcd * 64); }
      TOUCH(rf, p2);
    }
    }
    BAR();
  }
  STAMP(2, 0);
  unsigned* misc = (unsigned*)(lds + L_MISC);
  u32x4 own;
  {
    float h[8];
    load_h(x_own, rs, t_own, l, h);
    STAMP(2, 7);
    const u32x4 hn = layernorm8(h);
    float r = 0.f, r2 = 0.f;
#pragma unroll
    for (int e = 0; e < 4; ++e) { r += bflo(hn[e]) * bflo(wr[e]); r2 += bfhi(hn[e]) * bfhi(wr[e]); }
    r = wave_sum(r + r2);
    const unsigned rb = f2bf(r);
    const unsigned ord = (rb & 0x8000u) ? (~rb & 0xffffu) : (rb | 0x8000u);
    if (l == 0) { ((u16*)misc)[w] = (u16)ord; if (w == 0) misc[8] = 0; }
    WAIT_L(0);
    BAR();
    if (tid < 4) __hip_atomic_store((gu64*)(ws + WS_GR) + (tt * 16 + g1) * 4 + tid, ((unsigned long long)tag << 32) | misc[tid], RLX_AGENT);
    if (HNPUB) {
      __builtin_amdgcn_raw_buffer_store_b128((u32x4){hn[0], tag, hn[1], tag}, rs, WS_HN + (unsigned)t_own * 2048 + l * 32, 0, 16);
      __builtin_amdgcn_raw_buffer_store_b128((u32x4){hn[2], tag, hn[3], tag}, rs, WS_HN + (unsigned)t_own * 2048 + l * 32 + 16, 0, 16);
    }
#pragma unroll
    for (int e = 0; e < 4; ++e) own[e] = pack2(h[2 * e] + bflo(hn[e]), h[2 * e + 1] + bfhi(hn[e]));
  }
  KEEP(rf);
  {
    const u16* wsrc = down ? w_down + (size_t)(l >> 1) * D + db * 16 + (l & 1) * 8 : ((l & 1) ? w_up : w_gate) + (size_t)(l >> 1) * F + fb * 8;
    if (down) {
#pragma unroll
      for (int i = 0; i < 7; ++i) GLDS16(wsrc + (size_t)(w * 7 + i) * 32 * D, lds + L_W + (w * 7 + i) * 1024, 0);
    } else {
#pragma unroll
      for (int i = 0; i < 2; ++i) GLDS16(wsrc + (size_t)(w * 2 + i) * 32 * F, lds + L_W + (w * 2 + i) * 1024, 0);
    }
  }
  STAMP(2, 1);
  unsigned kk[4];
  {
    u32x4 a;
    for (int it = 0; it < SPIN_MAX; ++it) {
      asm volatile("" ::: "memory");
      a = __builtin_amdgcn_raw_buffer_load_b128(rs, WS_GR + (unsigned)tid * 16, 0, 16);
      if (__all(a[1] == tag && a[3] == tag)) break;
      __builtin_amdgcn_s_sleep(1);
    }
    __builtin_amdgcn_fence(__ATOMIC_ACQUIRE, "wavefront");
    const unsigned ib = 0xffffu - (unsigned)tid * 4;
    kk[0] = (a[0] << 16) | ib; kk[1] = (a[0] & 0xffff0000u) | (ib - 1); kk[2] = (a[2] << 16) | (ib - 2); kk[3] = (a[2] & 0xffff0000u) | (ib - 3);
  }
  STAMP(2, 2);
  {
    const unsigned m = wave_max_u32(umax(umax(kk[0], kk[1]), umax(kk[2], kk[3])));
    if (l == 0) misc[16 + w] = m;
  }
  WAIT_L(0);
  BAR();
  STAMP(2, 5);
  if (b == 0 && tid == 0) __hip_atomic_store((gu32*)(ws + WS_EP), tag, RLX_AGENT);
  {
    const u32x4 ma = *(const u32x4*)(misc + 16), mb2 = *(const u32x4*)(misc + 20);
    const unsigned lo = min(min(min(ma[0], ma[1]), min(ma[2], ma[3])), min(min(mb2[0], mb2[1]), min(mb2[2], mb2[3])));
    if (l < 16) misc[64 + w * 16 + l] = 0u;
    unsigned base = 0;
#pragma unroll
    for (int e = 0; e < 4; ++e) {
      const unsigned long long bm = __ballot(kk[e] >= lo);
      const unsigned pos = base + __builtin_amdgcn_mbcnt_hi((unsigned)(bm >> 32), __builtin_amdgcn_mbcnt_lo((unsigned)bm, 0u));
      if (kk[e] >= lo && pos < 16) misc[64 + w * 16 + pos] = kk[e];
      base += (unsigned)__builtin_popcountll(bm);
    }
    if (base > 16 && l == 0) misc[8] = 1u;
  }
  WAIT_L(0);
  BAR();
  STAMP(2, 6);
  unsigned key;
  bool sel_own;
  {
    const unsigned ovf = __builtin_amdgcn_readfirstlane(misc[8]);
    if (ovf == 0) {
      const unsigned cv = misc[64 + w * 16 + (l >> 2)];
      int rank = 0;
#pragma unroll
      for (int i = 0; i < 8; ++i) {
        const u32x4 c4 = *(const u32x4*)(misc + 64 + (l & 3) * 32 + i * 4);
#pragma unroll
        for (int e = 0; e < 4; ++e) rank += (c4[e] > cv) ? 1 : 0;
      }
      rank += (int)dpp_u<0xB1, 0xf>((unsigned)rank);
      rank += (int)dpp_u<0x4E, 0xf>((unsigned)rank);
      if (cv != 0u && rank < 8 && (l & 3) == 0) misc[32 + rank] = cv;
      WAIT_L(0);
      BAR();
      const unsigned sv = misc[32 + (l & 7)];
      key = (unsigned)__builtin_amdgcn_readlane((int)sv, w);
      sel_own = __any((int)(sv & 0xffffu) == 0xffff - t_own);
    } else {
      unsigned mine = 0;
#pragma unroll 1
      for (int it = 0; it < 8; ++it) {
        const unsigned m = wave_max_u32(umax(umax(kk[0], kk[1]), umax(kk[2], kk[3])));
#pragma unroll
        for (int e = 0; e < 4; ++e) kk[e] = (kk[e] == m) ? 0u : kk[e];
        if (l == it) mine = m;
      }
      if (l < 8) misc[192 + w * 8 + l] = mine;
      WAIT_L(0);
      BAR();
      unsigned cv = misc[192 + l];
      key = 0; sel_own = false;
#pragma unroll 1
      for (int it = 0; it < 8; ++it) {
        const unsigned m = wave_max_u32(cv);
        if (it == w) key = m;
        sel_own |= (int)(m & 0xffffu) == 0xffff - t_own;
        cv = (cv == m) ? 0u : cv;
      }
    }
  }
  STAMP(2, 3);
  const int t_sel = 0xffff - (int)(key & 0xffffu);
  if (HNPUB && !down) {
    u32x4 a, c;
    for (int it = 0; it < SPIN_MAX; ++it) {
      asm volatile("" ::: "memory");
      a = __builtin_amdgcn_raw_buffer_load_b128(rs, WS_HN + (unsigned)t_sel * 2048 + l * 32, 0, 16);
      c = __builtin_amdgcn_raw_buffer_load_b128(rs, WS_HN + (unsigned)t_sel * 2048 + l * 32 + 16, 0, 16);
      if (__all(a[1] == tag && a[3] == tag && c[1] == tag && c[3] == tag)) break;
      __builtin_amdgcn_s_sleep(1);
    }
    *(u32x4*)(lds + L_XS + w * XS_STRIDE + l * 16) = (u32x4){a[0], a[2], c[0], c[2]};
    if (!sel_own) *(u32x4*)(out + (size_t)t_own * D + l * 8) = own;
  } else {
    float h[8];
    const u32x4 xs = *(const u32x4*)(x + (size_t)t_sel * D + l * 8);
    load_h(xs, rs, t_sel, l, h);
    if (!sel_own) *(u32x4*)(out + (size_t)t_own * D + l * 8) = own;
    const u32x4 hn = layernorm8(h);
    if (!down) {
      *(u32x4*)(lds + L_XS + w * XS_STRIDE + l * 16) = hn;
    } else {
      if ((l >> 1) == db) {
        float2* hh = (float2*)(lds + L_HH) + w * 16 + (l & 1) * 8;
#pragma unroll
        for (int e = 0; e < 4; ++e) { hh[2 * e] = make_float2(h[2 * e], bflo(hn[e])); hh[2 * e + 1] = make_float2(h[2 * e + 1], bfhi(hn[e])); }
      }
      if (l == 0) misc[24 + w] = key;
    }
  }
  STAMP(2, 4);
  float* red = (float*)(lds + L_RED);
  if (!down) {
    WAIT_L(0);
    BAR();
    if (w == 0) {
      f32x4 a4[2] = {};
      const char* ap = lds + L_XS + (fr & 7) * XS_STRIDE + q * 16;
      const char* bp = lds + L_W + (q * 8 + (fr >> 2)) * 32 + (fr & 3) * 8;
#pragma unroll
      for (int ks = 0; ks < 16; ++ks) {
        const bf16x8 a = *(const bf16x8*)(ap + ks * 64);
        const v4i16_t g0 = __builtin_amdgcn_ds_read_tr16_b64_v4i16((LAS v4i16_t*)(bp + ks * 1024));
        const v4i16_t g1v = __builtin_amdgcn_ds_read_tr16_b64_v4i16((LAS v4i16_t*)(bp + ks * 1024 + 128));
        const bf16x8 bg = {g0[0], g0[1], g0[2], g0[3], g1v[0], g1v[1], g1v[2], g1v[3]};
        a4[ks & 1] = __builtin_amdgcn_mfma_f32_16x16x32_bf16(a, bg, a4[ks & 1], 0, 0, 0);
      }
      const f32x4 sum = a4[0] + a4[1];
      const bool hi = fr >= 8;
      unsigned actb[2];
#pragma unroll
      for (int k = 0; k < 2; ++k) {
        const float send = hi ? sum[k] : sum[2 + k];
        const float recv = dpp_f<0x128, 0xf>(send);
        const float g = rbf(hi ? recv : sum[k]), u = rbf(hi ? sum[2 + k] : recv);
        const float sg = rbf(g / (1.0f + expf(-g)));
        actb[k] = f2bf(sg * u);
      }
      const unsigned n0 = dpp_u<0xB1, 0xf>(actb[0]), n1 = dpp_u<0xB1, 0xf>(actb[1]);
      const unsigned val = (fr & 1) ? (n1 | (actb[1] << 16)) : (actb[0] | (n0 << 16));
      const int tok = q * 4 + (hi ? 2 : 0) + (fr & 1);
      if (q < 2) __hip_atomic_store((gu64*)(ws + WS_GA) + fb * 32 + tok * 4 + ((fr & 7) >> 1), ((unsigned long long)tag << 32) | val, RLX_AGENT);
    }
    STAMP(3, 0);
  } else {
    for (int it = 0; it < SPIN_MAX; ++it) {
      asm volatile("" ::: "memory");
      bool ok = true;
#pragma unroll
      for (int i = 0; i < 7; ++i) {
        const int rec = w * 28 + i * 4 + q;
        const u32x4 a = __builtin_amdgcn_raw_buffer_load_b128(rs, WS_GA + (unsigned)rec * 256 + fr * 16, 0, 16);
        ok &= (a[1] == tag) & (a[3] == tag);
        *(u32x2*)(lds + L_AS + (fr >> 1) * AS_STRIDE + rec * 16 + (fr & 1) * 8) = (u32x2){a[0], a[2]};
      }
      if (__all(ok)) break;
      __builtin_amdgcn_s_sleep(1);
    }
    STAMP(3, 1);
    WAIT_L(0);
    BAR();
    f32x4 a4 = {};
#pragma unroll
    for (int i = 0; i < 7; ++i) {
      const int ks = w * 7 + i;
      const bf16x8 a = *(const bf16x8*)(lds + L_AS + (fr & 7) * AS_STRIDE + ks * 64 + q * 16);
      const int r0 = ks * 32 + q * 8 + (fr >> 2);
      const int coff = (fr & 3) * 8;
      const v4i16_t g0 = __builtin_amdgcn_ds_read_tr16_b64_v4i16((LAS v4i16_t*)(lds + L_W + r0 * 32 + coff));
      const v4i16_t g1v = __builtin_amdgcn_ds_read_tr16_b64_v4i16((LAS v4i16_t*)(lds + L_W + (r0 + 4) * 32 + coff));
      const bf16x8 bg = {g0[0], g0[1], g0[2], g0[3], g1v[0], g1v[1], g1v[2], g1v[3]};
      a4 = __builtin_amdgcn_mfma_f32_16x16x32_bf16(a, bg, a4, 0, 0, 0);
    }
    if (q < 2) {
#pragma unroll
      for (int jj = 0; jj < 4; ++jj) red[w * 128 + (q * 4 + jj) * 16 + fr] = a4[jj];
    }
    WAIT_L(0);
    BAR();
    if (tid < 128) {
      float s = 0.f;
#pragma unroll
      for (int ww = 0; ww < 8; ++ww) s += red[ww * 128 + tid];
      const unsigned k2 = misc[24 + (tid >> 4)];
      const int t = 0xffff - (int)(k2 & 0xffffu);
      const unsigned ord = k2 >> 16;
      const unsigned rb = (ord & 0x8000u) ? (ord & 0x7fffu) : (~ord & 0xffffu);
      const float sig = rbf(1.0f / (1.0f + expf(-bf2f(rb))));
      const float upd = rbf(rbf(s) * sig);
      const float2 hh = ((const float2*)(lds + L_HH))[tid];
      out[(size_t)t * D + db * 16 + (tid & 15)] = (u16)f2bf(hh.x + rbf(hh.y + upd));
    }
    STAMP(3, 2);
  }
  STAMP_FLUSH;
  asm volatile(".Lfused_code_hi:");
}

extern "C" void kernel_launch(void* const* d_in, const int* in_sizes, int n_in, void* d_out, int out_size, void* d_ws,
                              size_t ws_size, hipStream_t stream) {
  fused<<<256, 512, 0, stream>>>((const u16*)d_in[0], (const u16*)d_in[1], (const u16*)d_in[2], (const u16*)d_in[3], (const u16*)d_in[4],
                                 (const u16*)d_in[6], (const u16*)d_in[7], (const u16*)d_in[8], (const u16*)d_in[9], (u16*)d_out, (char*)d_ws);
}
```

```cpp
#include <hip/hip_runtime.h>
#include <hip/hip_bf16.h>
#include <stdint.h>
#include <utility>

typedef unsigned short u16;
typedef __attribute__((ext_vector_type(8))) short bf16x8;
typedef __attribute__((ext_vector_type(4))) short v4i16_t;
typedef __attribute__((ext_vector_type(4))) float f32x4;
typedef __attribute__((ext_vector_type(4))) unsigned u32x4;
typedef __attribute__((ext_vector_type(2))) unsigned u32x2;
#define GAS __attribute__((address_space(1)))
#define LAS __attribute__((address_space(3)))
typedef GAS unsigned gu32;
typedef GAS unsigned long long gu64;
#define RLX_AGENT __ATOMIC_RELAXED, __HIP_MEMORY_SCOPE_AGENT

constexpr int S = 2048, D = 512, F = 1792;

__device__ __forceinline__ float bf2f(unsigned v) { return __uint_as_float(v << 16); }
__device__ __forceinline__ float bflo(unsigned v) { return __uint_as_float(v << 16); }
__device__ __forceinline__ float bfhi(unsigned v) { return __uint_as_float(v & 0xffff0000u); }
__device__ __forceinline__ unsigned f2bf(float f) { __bf16 b = (__bf16)f; return (unsigned)__builtin_bit_cast(u16, b); }
__device__ __forceinline__ float rbf(float f) { return bf2f(f2bf(f)); }
__device__ __forceinline__ unsigned pack2(float lo, float hi) { return f2bf(lo) | (f2bf(hi) << 16); }

#define WAIT_V(n) asm volatile("s_waitcnt vmcnt(%0)" ::"n"(n) : "memory")
#define WAIT_L(n) asm volatile("s_waitcnt lgkmcnt(%0)" ::"n"(n) : "memory")
#define BAR() __builtin_amdgcn_s_barrier()
#define GLDS16(src, dst, aux) __builtin_amdgcn_global_load_lds((const unsigned*)(src), (unsigned*)(dst), 16, 0, aux)
#define TOUCH(reg, ptr) asm volatile("global_load_dword %0, %1, off" : "+v"(reg) : "v"(ptr) : "memory")
#define TOUCH_SC1(reg, ptr) asm volatile("global_load_dword %0, %1, off sc1" : "+v"(reg) : "v"(ptr) : "memory")
#define TOUCH_NT(reg, ptr) asm volatile("global_load_dword %0, %1, off nt" : "+v"(reg) : "v"(ptr) : "memory")
#define KEEP(reg) asm volatile("" ::"v"(reg))

template <int CTRL, int RM> __device__ __forceinline__ float dpp_f(float v) {
  return __builtin_bit_cast(float, __builtin_amdgcn_update_dpp(0, __builtin_bit_cast(int, v), CTRL, RM, 0xf, true));
}
__device__ __forceinline__ float wave_sum(float v) {
  v += dpp_f<0x111, 0xf>(v); v += dpp_f<0x112, 0xf>(v); v += dpp_f<0x114, 0xf>(v); v += dpp_f<0x118, 0xf>(v);
  v += dpp_f<0x142, 0xa>(v); v += dpp_f<0x143, 0xc>(v);
  return __builtin_bit_cast(float, __builtin_amdgcn_readlane(__builtin_bit_cast(int, v), 63));
}
template <int CTRL, int RM> __device__ __forceinline__ unsigned dpp_u(unsigned v) {
  return (unsigned)__builtin_amdgcn_update_dpp(0, (int)v, CTRL, RM, 0xf, true);
}
__device__ __forceinline__ unsigned umax(unsigned a, unsigned b) { return a > b ? a : b; }
__device__ __forceinline__ unsigned wave_max_u32(unsigned v) {
  v = umax(v, dpp_u<0x111, 0xf>(v)); v = umax(v, dpp_u<0x112, 0xf>(v)); v = umax(v, dpp_u<0x114, 0xf>(v)); v = umax(v, dpp_u<0x118, 0xf>(v));
  v = umax(v, dpp_u<0x142, 0xa>(v)); v = umax(v, dpp_u<0x143, 0xc>(v));
  return (unsigned)__builtin_amdgcn_readlane((int)v, 63);
}

#define STAMP_DECL
#define STAMPW(k, i, wv)
#define STAMP_FLUSH
#define STAMP(k, i) STAMPW(k, i, 0)

constexpr unsigned WS_ABT = 0;
constexpr unsigned WS_Y = WS_ABT + 1024u * 2048 * 2;
constexpr unsigned WS_F1 = WS_Y + 4u * 2048 * 512 * 2;
constexpr unsigned WS_F2 = WS_F1 + 4096;
constexpr unsigned WS_GR = WS_F2 + 2048;
constexpr unsigned WS_GA = WS_GR + 1024 * 8;
constexpr unsigned WS_EP = WS_GA + 224 * 256;
constexpr unsigned WS_SEL = WS_EP + 256;
constexpr unsigned WS_HN = WS_SEL + 2048;
constexpr unsigned WS_END = WS_HN + 2048u * 2048;

constexpr int SPIN_MAX = 1 << 18;
#define AUX1 0
#define CPF 1
#define FMODE 1
#define HNPUB 2
#define WPOS 0
#define COOP 1
#define TLBW 0
#define TS 0
#define QMODE 2
#define NPLAIN 8
#define T1MODE 1
#define FFNSTEP 2

constexpr int L_QR = 81920;
constexpr int L_RST = 65536;
constexpr int L_TAG = 66560;
constexpr int L_W = 16384;
constexpr int L_T2 = 131072;
constexpr int XS_STRIDE = 1024 + 16;
constexpr int L_XS = 81920;
constexpr int L_RED = L_XS + 8 * XS_STRIDE;
constexpr int L_MISC = L_RED + 8 * 128 * 4;
constexpr int L_HH = L_MISC + 1024;
constexpr int AS_STRIDE = F * 2 + 16;
constexpr int L_AS = L_HH + 1024;
static_assert(L_AS + 8 * AS_STRIDE <= L_T2, "lds");

struct Ring2 {
  static constexpr int p(int T) { return (T % 5) * 16384; }
  static constexpr int q(int T) { return L_QR + ((T + 3) % 5) * 16384; }
};
constexpr int koff(int T) { return T < 8 ? T * 128 : (T - 8) * 128 + 64; }
constexpr int x1slot(int s) { return L_QR + (s % 5) * 16384; }

template <int BP, int BQ, int NKT, class RG, bool STATS>
struct Cons {
  static constexpr int TP = BP / 32, TQ = BQ / 32;
  struct Ctx { const char* lds; int poff[TP][2]; int qoff[TQ][2]; };
  struct Fr { bf16x8 pf[2][TP], qf[2][TQ]; };
  struct St { f32x4 s1[2], s2[2], sw[TQ]; };
  template <int T> static __device__ __forceinline__ void readf(const Ctx& c, Fr& f) {
#pragma unroll
    for (int ks = 0; ks < 2; ++ks) {
#pragma unroll
      for (int m = 0; m < TP; ++m) f.pf[ks][m] = *(const bf16x8*)(c.lds + RG::p(T) + c.poff[m][ks]);
#pragma unroll
      for (int n = 0; n < TQ; ++n) f.qf[ks][n] = *(const bf16x8*)(c.lds + RG::q(T) + c.qoff[n][ks]);
    }
  }
  template <int T> static __device__ __forceinline__ void step(const Ctx& c, f32x4 (&acc)[TP][TQ], Fr& cur, Fr& nxt, St& st) {
    if constexpr (T + 1 < NKT) {
      WAIT_L(0);
      BAR();
      __builtin_amdgcn_sched_barrier(0);
      readf<T + 1>(c, nxt);
    }
    if constexpr (T + 1 == NKT) return;
#pragma unroll
    for (int ks = 0; ks < 2; ++ks) {
#pragma unroll
      for (int m = 0; m < TP; ++m)
#pragma unroll
        for (int n = 0; n < TQ; ++n) acc[m][n] = __builtin_amdgcn_mfma_f32_16x16x32_bf16(cur.pf[ks][m], cur.qf[ks][n], acc[m][n], 0, 0, 0);
      if constexpr (STATS) {
        const bf16x8 ones = {0x3f80, 0x3f80, 0x3f80, 0x3f80, 0x3f80, 0x3f80, 0x3f80, 0x3f80};
#pragma unroll
        for (int m = 0; m < 2; ++m) {
          st.s1[m] = __builtin_amdgcn_mfma_f32_16x16x32_bf16(cur.pf[ks][m], ones, st.s1[m], 0, 0, 0);
          st.s2[m] = __builtin_amdgcn_mfma_f32_16x16x32_bf16(cur.pf[ks][m], cur.pf[ks][m], st.s2[m], 0, 0, 0);
        }
#pragma unroll
        for (int n = 0; n < TQ; ++n) st.sw[n] = __builtin_amdgcn_mfma_f32_16x16x32_bf16(ones, cur.qf[ks][n], st.sw[n], 0, 0, 0);
      }
    }
    if constexpr (T + 1 < NKT) {
#pragma unroll
      for (int i = 0; i < 2 * (TP + TQ); ++i) { __builtin_amdgcn_sched_group_barrier(0x100, 1, 0); __builtin_amdgcn_sched_group_barrier(0x8, 1, 0); }
    }
    __builtin_amdgcn_sched_barrier(0);
  }
  template <int... Ts>
  static __device__ __forceinline__ void steps(const Ctx& c, f32x4 (&acc)[TP][TQ], Fr& f0, Fr& f1, St& st, std::integer_sequence<int, Ts...>) {
    ((Ts & 1 ? step<Ts>(c, acc, f1, f0, st) : step<Ts>(c, acc, f0, f1, st)), ...);
  }
  static __device__ __forceinline__ void run(const char* lds, int wp, int wq, int rot, int l, f32x4 (&acc)[TP][TQ], St& st, Fr& f1) {
    Ctx c; c.lds = lds;
    const int fr = l & 15, q = l >> 4;
#pragma unroll
    for (int ks = 0; ks < 2; ++ks) {
#pragma unroll
      for (int m = 0; m < TP; ++m) { const int row = wp * (BP / 2) + ((m + rot) & (TP - 1)) * 16 + fr; c.poff[m][ks] = row * 128 + (((ks * 4 + q) ^ ((row >> 1) & 7)) * 16); }
#pragma unroll
      for (int n = 0; n < TQ; ++n) { const int row = wq * (BQ / 2) + n * 16 + fr; c.qoff[n][ks] = row * 128 + (((ks * 4 + q) ^ ((row >> 1) & 7)) * 16); }
    }
    Fr f0;
    BAR();
    readf<0>(c, f0);
    steps(c, acc, f0, f1, st, std::make_integer_sequence<int, NKT>{});
  }
};

struct L1Ctx { const u16* xsrc[4]; const u16* wsrc[4]; char* lds; int w; };
template <int s> __device__ __forceinline__ void l1_issue(const L1Ctx& c) {
#pragma unroll
  for (int h = 0; h < 2; ++h) {
#pragma unroll
    for (int i = 0; i < 4; ++i) GLDS16(c.xsrc[i] + (size_t)(s >> 2) * 64 * D + (s & 3) * 128 + h * 64, c.lds + x1slot(s) + h * 8192 + (c.w * 4 + i) * 1024, AUX1);
    if constexpr (s < 4) {
#pragma unroll
      for (int i = 0; i < 4; ++i) GLDS16(c.wsrc[i] + s * 128 + h * 64, c.lds + (2 * s + h) * 8192 + (c.w * 4 + i) * 1024, AUX1);
    }
  }
}
struct Fr1 { bf16x8 pf[4][2], qf[4][2]; };
struct C1Ctx { const char* lds; int poff[2][2]; int qoff[2][2]; };
template <int s> __device__ __forceinline__ void c1_read(const C1Ctx& c, Fr1& f) {
#pragma unroll
  for (int ks = 0; ks < 4; ++ks) {
#pragma unroll
    for (int m = 0; m < 2; ++m) f.pf[ks][m] = *(const bf16x8*)(c.lds + x1slot(s) + (ks >> 1) * 8192 + c.poff[m][ks & 1]);
#pragma unroll
    for (int n = 0; n < 2; ++n) f.qf[ks][n] = *(const bf16x8*)(c.lds + (2 * (s & 3) + (ks >> 1)) * 8192 + c.qoff[n][ks & 1]);
  }
}
struct Acc1 { f32x4 acc[2][2], s1, s2; };
template <int s> __device__ __forceinline__ void c1_step(const C1Ctx& c, Acc1& a, f32x4 (&sw)[2], Fr1& cur, Fr1& nxt) {
  if constexpr (s == 7) WAIT_V(0);
  WAIT_L(0);
  BAR();
  __builtin_amdgcn_sched_barrier(0);
  if constexpr (s + 1 < 8) c1_read<s + 1>(c, nxt);
  const bf16x8 ones = {0x3f80, 0x3f80, 0x3f80, 0x3f80, 0x3f80, 0x3f80, 0x3f80, 0x3f80};
#pragma unroll
  for (int ks = 0; ks < 4; ++ks) {
#pragma unroll
    for (int m = 0; m < 2; ++m)
#pragma unroll
      for (int n = 0; n < 2; ++n) a.acc[m][n] = __builtin_amdgcn_mfma_f32_16x16x32_bf16(cur.pf[ks][m], cur.qf[ks][n], a.acc[m][n], 0, 0, 0);
    a.s1 = __builtin_amdgcn_mfma_f32_16x16x32_bf16(cur.pf[ks][0], ones, a.s1, 0, 0, 0);
    a.s2 = __builtin_amdgcn_mfma_f32_16x16x32_bf16(cur.pf[ks][0], cur.pf[ks][0], a.s2, 0, 0, 0);
    if constexpr (s < 4) {
#pragma unroll
      for (int n = 0; n < 2; ++n) sw[n] = __builtin_amdgcn_mfma_f32_16x16x32_bf16(ones, cur.qf[ks][n], sw[n], 0, 0, 0);
    }
  }
  if constexpr (s + 1 < 8) {
#pragma unroll
    for (int i = 0; i < 16; ++i) { __builtin_amdgcn_sched_group_barrier(0x100, 1, 0); __builtin_amdgcn_sched_group_barrier(0x8, 1, 0); }
  }
  __builtin_amdgcn_sched_barrier(0);
}

struct L2Ctx { const u16* psrc[8]; const u16* qsrc[8]; char* lds; int w; };
template <int T> __device__ __forceinline__ void l2_issueP(const L2Ctx& c) {
#pragma unroll
  for (int i = 0; i < 8; ++i) GLDS16(c.psrc[i] + koff(T), c.lds + Ring2::p(T) + (c.w * 8 + i) * 1024, 16);
}
template <int T, int AUX> __device__ __forceinline__ void l2_issueQ(const L2Ctx& c) {
#pragma unroll
  for (int i = 0; i < 8; ++i) GLDS16(c.qsrc[i] + koff(T), c.lds + Ring2::q(T) + (c.w * 8 + i) * 1024, AUX);
}
constexpr int l2_cnt(int s) { return s < 5 ? 8 : 16; }
constexpr int l2_younger(int T) { int up = T + 4 < 15 ? T + 4 : 15; int n = 0; for (int s = T + 2; s <= up; ++s) n += l2_cnt(s); return n; }
template <int T, int AUX> __device__ __forceinline__ void l2_step(const L2Ctx& c) {
  WAIT_V(l2_younger(T));
  BAR();
  if constexpr (T + 5 < 16) { l2_issueP<T + 5>(c); l2_issueQ<T + 5, AUX>(c); }
}
template <int AUX, int... Ts> __device__ __forceinline__ void l2_steps(const L2Ctx& c, std::integer_sequence<int, Ts...>) { (l2_step<Ts, AUX>(c), ...); }

__device__ __forceinline__ u32x4 widen16(u32x2 a, u32x2 c) {
  const auto s0 = __builtin_amdgcn_permlane16_swap(a[0], c[0], false, false);
  const auto s1 = __builtin_amdgcn_permlane16_swap(a[1], c[1], false, false);
  return (u32x4){s0[0], s1[0], s0[1], s1[1]};
}

__device__ __forceinline__ void glds16_asm(const void* gsrc, unsigned lds_dst) {
  unsigned keep;
  asm volatile("s_mov_b32 %0, m0\n\ts_mov_b32 m0, %2\n\ts_nop 0\n\tglobal_load_lds_dwordx4 %1, off\n\ts_mov_b32 m0, %0" : "=&s"(keep) : "v"(gsrc), "s"(lds_dst) : "memory");
}

__device__ __forceinline__ void poll_words(const unsigned* p, unsigned tag) {
  for (int it = 0; it < SPIN_MAX; ++it) {
    const unsigned v = __hip_atomic_load((gu32*)p, RLX_AGENT);
    if (__all(v == tag)) break;
    __builtin_amdgcn_s_sleep(1);
  }
  __builtin_amdgcn_fence(__ATOMIC_ACQUIRE, "wavefront");
}

__device__ __forceinline__ unsigned add_pk(unsigned a, unsigned b) { return pack2(bflo(a) + bflo(b), bfhi(a) + bfhi(b)); }
__device__ __forceinline__ unsigned sub_pk(unsigned a, unsigned b) { return pack2(bflo(a) - bflo(b), bfhi(a) - bfhi(b)); }
template <class R>
__device__ __forceinline__ void load_h(const u32x4 xv, R rs, int t, int l, float (&h)[8]) {
  const unsigned o = (unsigned)(t * D + l * 8) * 2;
  const u32x4 y0 = __builtin_amdgcn_raw_buffer_load_b128(rs, WS_Y + o, 0, 16);
  const u32x4 y1 = __builtin_amdgcn_raw_buffer_load_b128(rs, WS_Y + 1u * S * D * 2 + o, 0, 16);
  const u32x4 y2 = __builtin_amdgcn_raw_buffer_load_b128(rs, WS_Y + 2u * S * D * 2 + o, 0, 16);
  const u32x4 y3 = __builtin_amdgcn_raw_buffer_load_b128(rs, WS_Y + 3u * S * D * 2 + o, 0, 16);
#pragma unroll
  for (int e = 0; e < 4; ++e) {
    const unsigned yv = pack2((bflo(y0[e]) + bflo(y1[e])) - (bflo(y2[e]) + bflo(y3[e])), (bfhi(y0[e]) + bfhi(y1[e])) - (bfhi(y2[e]) + bfhi(y3[e])));
    const unsigned hv = add_pk(xv[e], yv);
    h[2 * e] = bflo(hv); h[2 * e + 1] = bfhi(hv);
  }
}
__device__ __forceinline__ u32x4 layernorm8(const float (&h)[8]) {
  const float pv = __builtin_bit_cast(float, __builtin_amdgcn_readfirstlane(__builtin_bit_cast(int, h[0])));
  float s = 0.f, s2 = 0.f, v = 0.f, v2 = 0.f;
#pragma unroll
  for (int e = 0; e < 4; ++e) { const float a = h[2 * e] - pv, c = h[2 * e + 1] - pv; s += a; s2 += c; v += a * a; v2 += c * c; }
  const float md = wave_sum(s + s2) * (1.0f / D);
  v = fmaxf(wave_sum(v + v2) - md * md * (float)D, 0.f);
  const float mean = pv + md;
  const float mb = rbf(mean);
  const float sd = rbf(sqrtf(rbf(rbf(v * (1.0f / D)) + 1e-5f)));
  const float inv = 1.0f / sd;
  u32x4 o;
#pragma unroll
  for (int e = 0; e < 4; ++e) {
    const unsigned d = pack2(h[2 * e] - mb, h[2 * e + 1] - mb);
    const float a = bflo(d), c = bfhi(d);
    float qa = a * inv, qc = c * inv;
    qa = fmaf(fmaf(-qa, sd, a), inv, qa); qc = fmaf(fmaf(-qc, sd, c), inv, qc);
    o[e] = pack2(qa, qc);
  }
  return o;
}

__global__ __launch_bounds__(512) void fused(const u16* __restrict__ x, const u16* __restrict__ ws_re, const u16* __restrict__ ws_im,
                                             const u16* __restrict__ wd_re, const u16* __restrict__ wd_im, const u16* __restrict__ w_r,
                                             const u16* __restrict__ w_gate, const u16* __restrict__ w_up, const u16* __restrict__ w_down,
                                             u16* __restrict__ out, char* __restrict__ ws) {
  __shared__ __attribute__((aligned(1024))) char lds[163840];
  const int b = blockIdx.x, xcd = b & 7, j = b >> 3;
  const int cc = xcd >> 2, et = j & 3, tt = (xcd & 3) * 4 + ((j >> 2) & 3), kh = j >> 4;
  const int g1 = (cc * 4 + et) * 2 + kh;
  const int e1 = cc * 8 + et * 2 + (tt & 1), s1 = kh * 8 + (tt >> 1);
  const int tid = threadIdx.x, w = __builtin_amdgcn_readfirstlane(tid >> 6), l = tid & 63, fr = l & 15, q = l >> 4;
  const u16* wsb = cc ? ws_im : ws_re;
  const u16* wdb = cc ? wd_im : wd_re;
  const bool down = j >= 28;
  const int fb = xcd * 28 + j, db = xcd * 4 + (j - 28);
  const auto rs = __builtin_amdgcn_make_buffer_rsrc(ws, 0, WS_END, 0x00020000);
  unsigned tag;
  STAMP_DECL;
  STAMP(0, 0);
  unsigned long long code_lo; unsigned code_len;
  asm volatile("s_getpc_b64 %0\n.Lfused_code_lo:\n\ts_mov_b32 %1, .Lfused_code_hi-.Lfused_code_lo" : "=s"(code_lo), "=s"(code_len));

  const unsigned lbase = (unsigned)(uintptr_t)(LAS char*)lds;
  {
    const int row = w * 8 + (l >> 3);
    const size_t o = (size_t)row * D + (((l & 7) ^ ((row >> 1) & 7)) * 8);
    const u16* xp = x + (size_t)s1 * 128 * D + o;
    const u16* wp_ = wdb + (size_t)(e1 & 7) * 64 * D + o;
#pragma unroll
    for (int k = 0; k < 4; ++k) {
      glds16_asm(xp + k * 64, lbase + x1slot(k >> 1) + (k & 1) * 8192 + w * 1024);
      glds16_asm(wp_ + k * 64, lbase + k * 8192 + w * 1024);
    }
  }
  if (w < 4) {
    const int wp = w >> 1, wq = w & 1;
    unsigned r0 = 0;
    {
      const int row = tid >> 3, ln = tid & 7;
      const int xs_tile = ((j >> 3) & 1) * 8 + (xcd & 3) * 2 + ((j >> 2) & 1);
      const u16* p = j < 16 ? x + (size_t)(xs_tile * 128 + (j & 3) * 32 + row) * D + ln * 64 : wdb + (size_t)((j - 16) * 32 + row) * D + ln * 64;
      if (T1MODE == 1) TOUCH(r0, p);
      if (T1MODE == 2) TOUCH_NT(r0, p);
    }
    {
      C1Ctx c; c.lds = lds;
#pragma unroll
      for (int ks = 0; ks < 2; ++ks) {
#pragma unroll
        for (int m = 0; m < 2; ++m) { const int row = wp * 32 + (m ^ wq) * 16 + fr; c.poff[m][ks] = row * 128 + (((ks * 4 + q) ^ ((row >> 1) & 7)) * 16); }
#pragma unroll
        for (int n = 0; n < 2; ++n) { const int row = wq * 32 + n * 16 + fr; c.qoff[n][ks] = row * 128 + (((ks * 4 + q) ^ ((row >> 1) & 7)) * 16); }
      }
      f32x4 sw[2] = {};
      Fr1 f0, f1;
      auto epilogue = [&](const Acc1& a, int pass) {
        float2* rst = (float2*)(lds + L_RST) + pass * 64;
        if ((fr >> 2) == q) {
          const int jj = fr & 3;
          const float s1v = jj == 0 ? a.s1[0] : jj == 1 ? a.s1[1] : jj == 2 ? a.s1[2] : a.s1[3];
          const float s2v = jj == 0 ? a.s2[0] : jj == 1 ? a.s2[1] : jj == 2 ? a.s2[2] : a.s2[3];
          const float mean = s1v * (1.0f / D);
          const float var = fmaxf(s2v * (1.0f / D) - mean * mean, 0.f);
          const float sd = rbf(sqrtf(rbf(rbf(var) + 1e-5f)));
          rst[wp * 32 + wq * 16 + fr] = make_float2(rbf(mean), 1.0f / sd);
        }
        WAIT_L(0);
        BAR();
        u32x2 pk[2][2];
#pragma unroll
        for (int m = 0; m < 2; ++m) {
          const int p0 = wp * 32 + (m ^ wq) * 16 + q * 4;
          const f32x4 sa = *(const f32x4*)(rst + p0), sb = *(const f32x4*)(rst + p0 + 2);
#pragma unroll
          for (int n = 0; n < 2; ++n) {
            const float swv = sw[n][0];
            const float v0 = (a.acc[m][n][0] - sa[0] * swv) * sa[1], v1 = (a.acc[m][n][1] - sa[2] * swv) * sa[3];
            const float v2 = (a.acc[m][n][2] - sb[0] * swv) * sb[1], v3 = (a.acc[m][n][3] - sb[2] * swv) * sb[3];
            pk[m][n] = (u32x2){pack2(v0, v1), pack2(v2, v3)};
          }
        }
        const int scol = s1 * 128 + pass * 64 + wp * 32 + ((q & 1) ^ wq) * 16 + (q >> 1) * 8;
#pragma unroll
        for (int n = 0; n < 2; ++n)
          __builtin_amdgcn_raw_buffer_store_b128(widen16(pk[0][n], pk[1][n]), rs, WS_ABT + (unsigned)((e1 * 64 + wq * 32 + n * 16 + fr) * S + scol) * 2, 0, 16);
      };
      if (COOP) WAIT_V(5);
      BAR();
      c1_read<0>(c, f0);
      if (COOP) WAIT_V(1);
      {
        Acc1 a = {};
        c1_step<0>(c, a, sw, f0, f1); c1_step<1>(c, a, sw, f1, f0); c1_step<2>(c, a, sw, f0, f1); c1_step<3>(c, a, sw, f1, f0);
        STAMP(0, 2);
        epilogue(a, 0);
        STAMP(0, 3);
      }
      {
        Acc1 a = {};
        c1_step<4>(c, a, sw, f0, f1); c1_step<5>(c, a, sw, f1, f0); c1_step<6>(c, a, sw, f0, f1); c1_step<7>(c, a, sw, f1, f0);
        STAMP(0, 4);
        epilogue(a, 1);
        STAMP(0, 5);
      }
      tag = __builtin_amdgcn_readfirstlane(*(const unsigned*)(lds + L_TAG));
      WAIT_V(0);
      KEEP(r0);
      STAMP(0, 6);
      BAR();
    }
    using C2 = Cons<128, 128, 16, Ring2, false>;
    f32x4 acc[4][4] = {};
    C2::St st;
    C2::Fr fl;
    C2::run(lds, wp, wq, 0, l, acc, st, fl);
    STAMP(1, 2);
    {
      const unsigned yo = WS_Y + (unsigned)(cc * 2 + kh) * S * D * 2;
      const int chunk = (q >> 1) + 2 * (q & 1);
#pragma unroll
      for (int n = 0; n < 4; ++n) {
#pragma unroll
        for (int ks = 0; ks < 2; ++ks)
#pragma unroll
          for (int m = 0; m < 4; ++m) acc[m][n] = __builtin_amdgcn_mfma_f32_16x16x32_bf16(fl.pf[ks][m], fl.qf[ks][n], acc[m][n], 0, 0, 0);
#pragma unroll
        for (int mp = 0; mp < 2; ++mp) {
          const u32x2 a = {pack2(acc[2 * mp][n][0], acc[2 * mp][n][1]), pack2(acc[2 * mp][n][2], acc[2 * mp][n][3])};
          const u32x2 c = {pack2(acc[2 * mp + 1][n][0], acc[2 * mp + 1][n][1]), pack2(acc[2 * mp + 1][n][2], acc[2 * mp + 1][n][3])};
          const int row = wq * 64 + n * 16 + fr;
          __builtin_amdgcn_raw_buffer_store_b128(widen16(a, c), rs, yo + (unsigned)((tt * 128 + row) * D + et * 128 + wp * 64 + mp * 32 + chunk * 8) * 2, 0, 16);
        }
      }
    }
    STAMP(1, 3);
    WAIT_V(0);
    STAMP(1, 6);
  } else if (w < 6) {
    const int lw = w - 4;
    const int r8 = l >> 3, ch = l & 7;
    {
      L1Ctx c; c.lds = lds; c.w = lw;
      const u16* P = x + (size_t)s1 * 128 * D;
      const u16* Q = wdb + (size_t)(e1 & 7) * 64 * D;
#pragma unroll
      for (int i = 0; i < 4; ++i) {
        const int row = (lw * 4 + i) * 8 + r8;
        const size_t o = (size_t)row * D + ((ch ^ ((row >> 1) & 7)) * 8);
        c.xsrc[i] = P + o; c.wsrc[i] = Q + o;
      }
      l1_issue<2>(c);
      WAIT_V(20); STAMPW(0, 1, 4); BAR();
      WAIT_V(16); BAR();
      l1_issue<3>(c);
      WAIT_V(16); BAR();
      l1_issue<4>(c); l1_issue<5>(c);
      WAIT_V(16); BAR();
      l1_issue<6>(c); l1_issue<7>(c);
      WAIT_V(24); BAR();
      BAR();
      WAIT_V(16); BAR();
      WAIT_V(8); BAR();
      WAIT_V(0); BAR();
    }
    L2Ctx c2;
    c2.lds = lds; c2.w = lw;
    {
      const u16* P = (const u16*)(ws + WS_ABT) + (size_t)(cc * 512 + et * 128) * S + kh * 1024;
      const u16* Q = wsb + (size_t)tt * 128 * S + kh * 1024;
#pragma unroll
      for (int i = 0; i < 8; ++i) {
        const int row = (lw * 8 + i) * 8 + r8;
        const size_t o = (size_t)row * S + ((ch ^ ((row >> 1) & 7)) * 8);
        c2.psrc[i] = P + o; c2.qsrc[i] = Q + o;
      }
    }
    tag = __builtin_amdgcn_readfirstlane(*(const unsigned*)(lds + L_TAG));
    BAR();
    BAR();
    poll_words((const unsigned*)(ws + WS_F1) + g1 * 32 + fr, tag);
    STAMPW(1, 0, 4);
    l2_issueP<0>(c2); l2_issueP<1>(c2); l2_issueP<2>(c2);
    BAR();
    WAIT_V(16);
    STAMPW(1, 1, 4);
    BAR();
    l2_issueP<3>(c2); l2_issueP<4>(c2);
    l2_steps<2>(c2, std::make_integer_sequence<int, 15>{});
  } else if (w == 6) {
    const int r8 = l >> 3, ch = l & 7;
    const unsigned ep = __hip_atomic_load((gu32*)(ws + WS_EP), RLX_AGENT);
    const u16* Q = wsb + (size_t)tt * 128 * S + kh * 1024;
    const u16* qa = Q + (size_t)r8 * S + ((ch ^ (r8 >> 1)) * 8);
    const u16* qb = Q + (size_t)(8 + r8) * S + ((ch ^ (4 + (r8 >> 1))) * 8);
    auto issueQ = [&](int T) {
#pragma unroll
      for (int i = 0; i < 8; ++i) {
        if (QMODE == 0 || (QMODE == 1 && 2 * i < NPLAIN)) {
          GLDS16(qa + (size_t)i * 16 * S + T * 128, lds + L_QR + ((T + 3) % 5) * 16384 + (2 * i) * 1024, 0);
          GLDS16(qb + (size_t)i * 16 * S + T * 128, lds + L_QR + ((T + 3) % 5) * 16384 + (2 * i + 1) * 1024, 0);
        } else {
          GLDS16(qa + (size_t)i * 16 * S + T * 128, lds + L_QR + ((T + 3) % 5) * 16384 + (2 * i) * 1024, 2);
          GLDS16(qb + (size_t)i * 16 * S + T * 128, lds + L_QR + ((T + 3) % 5) * 16384 + (2 * i + 1) * 1024, 2);
        }
      }
    };
    if (COOP) WAIT_V(5);
    BAR();
    if (COOP) WAIT_V(1);
    BAR();
    tag = __builtin_amdgcn_readfirstlane(ep * 1664525u + 1013904223u);
    *(unsigned*)(lds + L_TAG) = tag;
    WAIT_L(0);
    BAR(); BAR(); BAR();
    BAR();
    BAR(); issueQ(0);
    BAR(); issueQ(1);
    BAR(); issueQ(2);
    BAR(); BAR();
    BAR(); issueQ(3); issueQ(4);
    WAIT_V(32);
    BAR();
    const unsigned* fB = (const unsigned*)(ws + WS_F1) + 512 + g1 * 32 + fr;
    BAR(); BAR();
    const unsigned vB = __hip_atomic_load((gu32*)fB, RLX_AGENT);
    WAIT_V(17);
    BAR();
    if (!__all(vB == tag)) poll_words(fB, tag);
    WAIT_V(0);
    BAR();
#pragma unroll
    for (int T = 4; T < 15; ++T) BAR();
  } else {
    unsigned r[4] = {0, 0, 0, 0};
    constexpr int LPI = 64 / (2 * NPLAIN) > 0 ? 64 / (2 * NPLAIN) : 1;
    const int pr = j * NPLAIN + (l % NPLAIN);
    const int prow = (pr / (NPLAIN * 8)) * 128 + (pr % (NPLAIN * 8));
    const u16* wb = wsb + (size_t)((xcd & 3) * 512 + prow) * S + ((l / NPLAIN) & 1) * 1024 + (l / (2 * NPLAIN)) * 128;
#define TW(i) TOUCH(r[(i) & 1], wb + ((i) % (8 / LPI)) * (LPI * 128) + ((i) / (8 / LPI)) * 64)
    auto ffn_ptr = [&](int i) -> const char* {
      const int li = j * 192 + i * 64 + l;
      if (li < 4096) { const int mrow = (li & 2047) >> 2, c4 = li & 3; return (const char*)((li < 2048 ? w_gate : w_up) + (size_t)mrow * F + xcd * 224 + c4 * 64); }
      const int rr = li - 4096;
      return (const char*)(w_down + (size_t)(rr < F ? rr : 0) * D + xcd * 64);
    };
    if (TLBW) {
      TOUCH_SC1(r[3], ws + (size_t)l * 65536);
      TOUCH_SC1(r[3], ws + WS_Y + (size_t)l * 65536);
      TOUCH_SC1(r[3], ws + WS_Y + (size_t)(64 + l) * 65536);
      TOUCH_SC1(r[3], (l < 32 ? (const char*)out + (size_t)l * 65536 : l < 34 ? (const char*)ws + WS_F1 + (size_t)(l - 32) * 65536
                       : (const char*)wsb + (size_t)(xcd & 3) * 2097152 + (size_t)(l - 34) * 65536));
    }
    if (CPF) {
      const unsigned off = (unsigned)(j * 16 + fr) * 128u;
      const char* cp = (const char*)code_lo + (off < code_len ? off : 0u);
      if (l < 16) TOUCH(r[3], cp);
    }
    const char* fp0 = ffn_ptr(0); const char* fp1 = ffn_ptr(1); const char* fp2 = ffn_ptr(2);
    const u16* wb2 = wsb + (size_t)((xcd & 3) * 512 + j * 16 + fr) * S + ((l >> 4) & 1) * 1024 + (l >> 5) * 128;
#define TF(i) TOUCH(r[(i) & 1], wb2 + ((i) & 3) * 256 + ((i) >> 2) * 64)
#define TFF(p) TOUCH(r[2], p)
    if (COOP) WAIT_V(4 + CPF);
    BAR();
    if (COOP) WAIT_V(CPF);
    BAR(); BAR(); TFF(fp0);
    tag = __builtin_amdgcn_readfirstlane(*(const unsigned*)(lds + L_TAG));
    BAR(); TFF(fp1); BAR(); TFF(fp2); BAR();
    BAR(); BAR(); BAR();
    BAR();
    if (l == 0) __hip_atomic_store((gu32*)(ws + WS_F1) + g1 * 32 + tt, tag, RLX_AGENT);
    BAR();
    BAR();
    if (l == 0) __hip_atomic_store((gu32*)(ws + WS_F1) + 512 + g1 * 32 + tt, tag, RLX_AGENT);
#pragma unroll
    for (int T = 0; T < 16; ++T) {
      BAR();
    }
    WAIT_V(0);
    KEEP(r[0]); KEEP(r[1]); KEEP(r[2]); KEEP(r[3]);
  }
  const int t_own = tt * 128 + g1 * 8 + w;
  unsigned rf = 0;
  const u32x4 wr = *(const u32x4*)(w_r + l * 8);
  const u32x4 x_own = *(const u32x4*)(x + (size_t)t_own * D + l * 8);
  {
    BAR();
    if (tid == 0) __hip_atomic_store((gu32*)(ws + WS_F2) + tt * 32 + g1, tag, RLX_AGENT);
    STAMP(1, 4);
    if (w == 0) poll_words((const unsigned*)(ws + WS_F2) + tt * 32 + fr, tag);
    if (FMODE == 0) {
    if (w >= 5) {
      const int li = j * 192 + (w - 5) * 64 + l;
      const char* p2;
      if (li < 4096) { const int mrow = (li & 2047) >> 2, c4 = li & 3; p2 = (const char*)((li < 2048 ? w_gate : w_up) + (size_t)mrow * F + xcd * 224 + c4 * 64); }
      else { const int rr = li - 4096; p2 = (const char*)(w_down + (size_t)(rr < F ? rr : 0) * D + xcd * 64); }
      TOUCH(rf, p2);
    }
    }
    BAR();
  }
  STAMP(2, 0);
  unsigned* misc = (unsigned*)(lds + L_MISC);
  u32x4 own;
  {
    float h[8];
    load_h(x_own, rs, t_own, l, h);
    STAMP(2, 7);
    const u32x4 hn = layernorm8(h);
    float r = 0.f, r2 = 0.f;
#pragma unroll
    for (int e = 0; e < 4; ++e) { r += bflo(hn[e]) * bflo(wr[e]); r2 += bfhi(hn[e]) * bfhi(wr[e]); }
    r = wave_sum(r + r2);
    const unsigned rb = f2bf(r);
    const unsigned ord = (rb & 0x8000u) ? (~rb & 0xffffu) : (rb | 0x8000u);
    if (l == 0) { ((u16*)misc)[w] = (u16)ord; if (w == 0) misc[8] = 0; }
    WAIT_L(0);
    BAR();
    if (tid < 4) __hip_atomic_store((gu64*)(ws + WS_GR) + (tt * 16 + g1) * 4 + tid, ((unsigned long long)tag << 32) | misc[tid], RLX_AGENT);
    bool pub = HNPUB == 1;
    if (HNPUB == 2) {
      const unsigned ov = ((const u16*)misc)[l & 7];
      pub = !__any(ov > ord || (ov == ord && (l & 7) < w));
    }
    if (pub) {
      __builtin_amdgcn_raw_buffer_store_b128((u32x4){hn[0], tag, hn[1], tag}, rs, WS_HN + (unsigned)t_own * 2048 + l * 32, 0, 16);
      __builtin_amdgcn_raw_buffer_store_b128((u32x4){hn[2], tag, hn[3], tag}, rs, WS_HN + (unsigned)t_own * 2048 + l * 32 + 16, 0, 16);
    }
#pragma unroll
    for (int e = 0; e < 4; ++e) own[e] = pack2(h[2 * e] + bflo(hn[e]), h[2 * e + 1] + bfhi(hn[e]));
  }
  KEEP(rf);
  {
    const u16* wsrc = down ? w_down + (size_t)(l >> 1) * D + db * 16 + (l & 1) * 8 : ((l & 1) ? w_up : w_gate) + (size_t)(l >> 1) * F + fb * 8;
    if (down) {
#pragma unroll
      for (int i = 0; i < 7; ++i) glds16_asm(wsrc + (size_t)(w * 7 + i) * 32 * D, lbase + L_W + (w * 7 + i) * 1024);
    } else {
#pragma unroll
      for (int i = 0; i < 2; ++i) glds16_asm(wsrc + (size_t)(w * 2 + i) * 32 * F, lbase + L_W + (w * 2 + i) * 1024);
    }
  }
  STAMP(2, 1);
  unsigned key;
  bool sel_own;
  bool fast = HNPUB == 1;
  if (!down) {
  unsigned kk[4];
  {
    u32x4 a;
    for (int it = 0; it < SPIN_MAX; ++it) {
      asm volatile("" ::: "memory");
      a = __builtin_amdgcn_raw_buffer_load_b128(rs, WS_GR + (unsigned)tid * 16, 0, 16);
      if (__all(a[1] == tag && a[3] == tag)) break;
      __builtin_amdgcn_s_sleep(1);
    }
    __builtin_amdgcn_fence(__ATOMIC_ACQUIRE, "wavefront");
    const unsigned ib = 0xffffu - (unsigned)tid * 4;
    kk[0] = (a[0] << 16) | ib; kk[1] = (a[0] & 0xffff0000u) | (ib - 1); kk[2] = (a[2] << 16) | (ib - 2); kk[3] = (a[2] & 0xffff0000u) | (ib - 3);
  }
  STAMP(2, 2);
  {
    const unsigned m = wave_max_u32(umax(umax(kk[0], kk[1]), umax(kk[2], kk[3])));
    if (l == 0) misc[16 + w] = m;
  }
  WAIT_L(0);
  BAR();
  STAMP(2, 5);
  if (b == 0 && tid == 0) __hip_atomic_store((gu32*)(ws + WS_EP), tag, RLX_AGENT);
  {
    const u32x4 ma = *(const u32x4*)(misc + 16), mb2 = *(const u32x4*)(misc + 20);
    const unsigned lo = min(min(min(ma[0], ma[1]), min(ma[2], ma[3])), min(min(mb2[0], mb2[1]), min(mb2[2], mb2[3])));
    if (l < 16) misc[64 + w * 16 + l] = 0u;
    unsigned base = 0;
#pragma unroll
    for (int e = 0; e < 4; ++e) {
      const unsigned long long bm = __ballot(kk[e] >= lo);
      const unsigned pos = base + __builtin_amdgcn_mbcnt_hi((unsigned)(bm >> 32), __builtin_amdgcn_mbcnt_lo((unsigned)bm, 0u));
      if (kk[e] >= lo && pos < 16) misc[64 + w * 16 + pos] = kk[e];
      base += (unsigned)__builtin_popcountll(bm);
    }
    if (base > 16 && l == 0) misc[8] = 1u;
  }
  WAIT_L(0);
  BAR();
  STAMP(2, 6);
  {
    const unsigned ovf = __builtin_amdgcn_readfirstlane(misc[8]);
    if (ovf == 0) {
      const unsigned cv = misc[64 + w * 16 + (l >> 2)];
      int rank = 0;
#pragma unroll
      for (int i = 0; i < 8; ++i) {
        const u32x4 c4 = *(const u32x4*)(misc + 64 + (l & 3) * 32 + i * 4);
#pragma unroll
        for (int e = 0; e < 4; ++e) rank += (c4[e] > cv) ? 1 : 0;
      }
      rank += (int)dpp_u<0xB1, 0xf>((unsigned)rank);
      rank += (int)dpp_u<0x4E, 0xf>((unsigned)rank);
      if (cv != 0u && rank < 8 && (l & 3) == 0) misc[32 + rank] = cv;
      WAIT_L(0);
      BAR();
      const unsigned sv = misc[32 + (l & 7)];
      if (b < 8 && tid < 8) __hip_atomic_store((gu64*)(ws + WS_SEL) + b * 32 + tid, ((unsigned long long)tag << 32) | sv, RLX_AGENT);
      key = (unsigned)__builtin_amdgcn_readlane((int)sv, w);
      sel_own = __any((int)(sv & 0xffffu) == 0xffff - t_own);
      fast = !__any(sv > key && ((0xffffu - (sv & 0xffffu)) >> 3) == ((0xffffu - (key & 0xffffu)) >> 3));
    } else {
      unsigned mine = 0;
#pragma unroll 1
      for (int it = 0; it < 8; ++it) {
        const unsigned m = wave_max_u32(umax(umax(kk[0], kk[1]), umax(kk[2], kk[3])));
#pragma unroll
        for (int e = 0; e < 4; ++e) kk[e] = (kk[e] == m) ? 0u : kk[e];
        if (l == it) mine = m;
      }
      if (l < 8) misc[192 + w * 8 + l] = mine;
      WAIT_L(0);
      BAR();
      unsigned cv = misc[192 + l];
      key = 0; sel_own = false;
#pragma unroll 1
      for (int it = 0; it < 8; ++it) {
        const unsigned m = wave_max_u32(cv);
        if (it == w) key = m;
        sel_own |= (int)(m & 0xffffu) == 0xffff - t_own;
        cv = (cv == m) ? 0u : cv;
      }
      if (b < 8 && l == 0) __hip_atomic_store((gu64*)(ws + WS_SEL) + b * 32 + w, ((unsigned long long)tag << 32) | key, RLX_AGENT);
    }
  }
  } else {
    if (w == 0) {
      unsigned long long g;
      for (int it = 0; it < SPIN_MAX; ++it) {
        g = __hip_atomic_load((gu64*)(ws + WS_SEL) + xcd * 32 + (l & 7), RLX_AGENT);
        if (__all((unsigned)(g >> 32) == tag)) break;
        __builtin_amdgcn_s_sleep(1);
      }
      if (l < 8) misc[32 + l] = (unsigned)g;
      WAIT_L(0);
    }
    BAR();
    const unsigned sv = misc[32 + (l & 7)];
    key = (unsigned)__builtin_amdgcn_readlane((int)sv, w);
    sel_own = __any((int)(sv & 0xffffu) == 0xffff - t_own);
  }
  STAMP(2, 3);
  const int t_sel = 0xffff - (int)(key & 0xffffu);
  if (HNPUB && !down && fast) {
    u32x4 a, c;
    for (int it = 0; it < SPIN_MAX; ++it) {
      asm volatile("" ::: "memory");
      a = __builtin_amdgcn_raw_buffer_load_b128(rs, WS_HN + (unsigned)t_sel * 2048 + l * 32, 0, 16);
      c = __builtin_amdgcn_raw_buffer_load_b128(rs, WS_HN + (unsigned)t_sel * 2048 + l * 32 + 16, 0, 16);
      if (__all(a[1] == tag && a[3] == tag && c[1] == tag && c[3] == tag)) break;
      __builtin_amdgcn_s_sleep(1);
    }
    *(u32x4*)(lds + L_XS + w * XS_STRIDE + l * 16) = (u32x4){a[0], a[2], c[0], c[2]};
    if (!sel_own) *(u32x4*)(out + (size_t)t_own * D + l * 8) = own;
  } else {
    float h[8];
    const u32x4 xs = *(const u32x4*)(x + (size_t)t_sel * D + l * 8);
    load_h(xs, rs, t_sel, l, h);
    if (!sel_own) *(u32x4*)(out + (size_t)t_own * D + l * 8) = own;
    const u32x4 hn = layernorm8(h);
    if (!down) {
      *(u32x4*)(lds + L_XS + w * XS_STRIDE + l * 16) = hn;
    } else {
      if ((l >> 1) == db) {
        float2* hh = (float2*)(lds + L_HH) + w * 16 + (l & 1) * 8;
#pragma unroll
        for (int e = 0; e < 4; ++e) { hh[2 * e] = make_float2(h[2 * e], bflo(hn[e])); hh[2 * e + 1] = make_float2(h[2 * e + 1], bfhi(hn[e])); }
      }
      if (l == 0) misc[24 + w] = key;
    }
  }
  STAMP(2, 4);
  float* red = (float*)(lds + L_RED);
  if (!down) {
    WAIT_L(0);
    BAR();
    if (w == 0) {
      f32x4 a4[2] = {};
      const char* ap = lds + L_XS + (fr & 7) * XS_STRIDE + q * 16;
      const char* bp = lds + L_W + (q * 8 + (fr >> 2)) * 32 + (fr & 3) * 8;
#pragma unroll
      for (int ks = 0; ks < 16; ++ks) {
        const bf16x8 a = *(const bf16x8*)(ap + ks * 64);
        const v4i16_t g0 = __builtin_amdgcn_ds_read_tr16_b64_v4i16((LAS v4i16_t*)(bp + ks * 1024));
        const v4i16_t g1v = __builtin_amdgcn_ds_read_tr16_b64_v4i16((LAS v4i16_t*)(bp + ks * 1024 + 128));
        const bf16x8 bg = {g0[0], g0[1], g0[2], g0[3], g1v[0], g1v[1], g1v[2], g1v[3]};
        a4[ks & 1] = __builtin_amdgcn_mfma_f32_16x16x32_bf16(a, bg, a4[ks & 1], 0, 0, 0);
      }
      const f32x4 sum = a4[0] + a4[1];
      const bool hi = fr >= 8;
      unsigned actb[2];
#pragma unroll
      for (int k = 0; k < 2; ++k) {
        const float send = hi ? sum[k] : sum[2 + k];
        const float recv = dpp_f<0x128, 0xf>(send);
        const float g = rbf(hi ? recv : sum[k]), u = rbf(hi ? sum[2 + k] : recv);
        const float sg = rbf(g / (1.0f + expf(-g)));
        actb[k] = f2bf(sg * u);
      }
      const unsigned n0 = dpp_u<0xB1, 0xf>(actb[0]), n1 = dpp_u<0xB1, 0xf>(actb[1]);
      const unsigned val = (fr & 1) ? (n1 | (actb[1] << 16)) : (actb[0] | (n0 << 16));
      const int tok = q * 4 + (hi ? 2 : 0) + (fr & 1);
      if (q < 2) __hip_atomic_store((gu64*)(ws + WS_GA) + fb * 32 + tok * 4 + ((fr & 7) >> 1), ((unsigned long long)tag << 32) | val, RLX_AGENT);
    }
    STAMP(3, 0);
  } else {
    for (int it = 0; it < SPIN_MAX; ++it) {
      asm volatile("" ::: "memory");
      bool ok = true;
#pragma unroll
      for (int i = 0; i < 7; ++i) {
        const int rec = w * 28 + i * 4 + q;
        const u32x4 a = __builtin_amdgcn_raw_buffer_load_b128(rs, WS_GA + (unsigned)rec * 256 + fr * 16, 0, 16);
        ok &= (a[1] == tag) & (a[3] == tag);
        *(u32x2*)(lds + L_AS + (fr >> 1) * AS_STRIDE + rec * 16 + (fr & 1) * 8) = (u32x2){a[0], a[2]};
      }
      if (__all(ok)) break;
      __builtin_amdgcn_s_sleep(1);
    }
    STAMP(3, 1);
    WAIT_L(0);
    f32x4 a4 = {};
#pragma unroll
    for (int i = 0; i < 7; ++i) {
      const int ks = w * 7 + i;
      const bf16x8 a = *(const bf16x8*)(lds + L_AS + (fr & 7) * AS_STRIDE + ks * 64 + q * 16);
      const int r0 = ks * 32 + q * 8 + (fr >> 2);
      const int coff = (fr & 3) * 8;
      const v4i16_t g0 = __builtin_amdgcn_ds_read_tr16_b64_v4i16((LAS v4i16_t*)(lds + L_W + r0 * 32 + coff));
      const v4i16_t g1v = __builtin_amdgcn_ds_read_tr16_b64_v4i16((LAS v4i16_t*)(lds + L_W + (r0 + 4) * 32 + coff));
      const bf16x8 bg = {g0[0], g0[1], g0[2], g0[3], g1v[0], g1v[1], g1v[2], g1v[3]};
      a4 = __builtin_amdgcn_mfma_f32_16x16x32_bf16(a, bg, a4, 0, 0, 0);
    }
    if (q < 2) {
#pragma unroll
      for (int jj = 0; jj < 4; ++jj) red[w * 128 + (q * 4 + jj) * 16 + fr] = a4[jj];
    }
    WAIT_L(0);
    BAR();
    if (tid < 128) {
      float s = 0.f;
#pragma unroll
      for (int ww = 0; ww < 8; ++ww) s += red[ww * 128 + tid];
      const unsigned k2 = misc[24 + (tid >> 4)];
      const int t = 0xffff - (int)(k2 & 0xffffu);
      const unsigned ord = k2 >> 16;
      const unsigned rb = (ord & 0x8000u) ? (ord & 0x7fffu) : (~ord & 0xffffu);
      const float sig = rbf(1.0f / (1.0f + expf(-bf2f(rb))));
      const float upd = rbf(rbf(s) * sig);
      const float2 hh = ((const float2*)(lds + L_HH))[tid];
      out[(size_t)t * D + db * 16 + (tid & 15)] = (u16)f2bf(hh.x + rbf(hh.y + upd));
    }
    STAMP(3, 2);
  }
  STAMP_FLUSH;
  asm volatile(".Lfused_code_hi:");
}

extern "C" void kernel_launch(void* const* d_in, const int* in_sizes, int n_in, void* d_out, int out_size, void* d_ws,
                              size_t ws_size, hipStream_t stream) {
  fused<<<256, 512, 0, stream>>>((const u16*)d_in[0], (const u16*)d_in[1], (const u16*)d_in[2], (const u16*)d_in[3], (const u16*)d_in[4],
                                 (const u16*)d_in[6], (const u16*)d_in[7], (const u16*)d_in[8], (const u16*)d_in[9], (u16*)d_out, (char*)d_ws);
}
```

```cpp
#define PAD 26
#include <hip/hip_runtime.h>
#include <hip/hip_bf16.h>
#include <stdint.h>
#include <utility>

typedef unsigned short u16;
typedef __attribute__((ext_vector_type(8))) short bf16x8;
typedef __attribute__((ext_vector_type(4))) short v4i16_t;
typedef __attribute__((ext_vector_type(4))) float f32x4;
typedef __attribute__((ext_vector_type(4))) unsigned u32x4;
typedef __attribute__((ext_vector_type(2))) unsigned u32x2;
#define GAS __attribute__((address_space(1)))
#define LAS __attribute__((address_space(3)))
typedef GAS unsigned gu32;
typedef GAS unsigned long long gu64;
#define RLX_AGENT __ATOMIC_RELAXED, __HIP_MEMORY_SCOPE_AGENT

constexpr int S = 2048, D = 512, F = 1792;

__device__ __forceinline__ float bf2f(unsigned v) { return __uint_as_float(v << 16); }
__device__ __forceinline__ float bflo(unsigned v) { return __uint_as_float(v << 16); }
__device__ __forceinline__ float bfhi(unsigned v) { return __uint_as_float(v & 0xffff0000u); }
__device__ __forceinline__ unsigned f2bf(float f) { __bf16 b = (__bf16)f; return (unsigned)__builtin_bit_cast(u16, b); }
__device__ __forceinline__ float rbf(float f) { return bf2f(f2bf(f)); }
__device__ __forceinline__ unsigned pack2(float lo, float hi) { return f2bf(lo) | (f2bf(hi) << 16); }

#define WAIT_V(n) asm volatile("s_waitcnt vmcnt(%0)" ::"n"(n) : "memory")
#define WAIT_L(n) asm volatile("s_waitcnt lgkmcnt(%0)" ::"n"(n) : "memory")
#define BAR() __builtin_amdgcn_s_barrier()
#define GLDS16(src, dst, aux) __builtin_amdgcn_global_load_lds((const unsigned*)(src), (unsigned*)(dst), 16, 0, aux)
#define TOUCH(reg, ptr) asm volatile("global_load_dword %0, %1, off" : "+v"(reg) : "v"(ptr) : "memory")
#define TOUCH_SC1(reg, ptr) asm volatile("global_load_dword %0, %1, off sc1" : "+v"(reg) : "v"(ptr) : "memory")
#define TOUCH_NT(reg, ptr) asm volatile("global_load_dword %0, %1, off nt" : "+v"(reg) : "v"(ptr) : "memory")
#define KEEP(reg) asm volatile("" ::"v"(reg))

template <int CTRL, int RM> __device__ __forceinline__ float dpp_f(float v) {
  return __builtin_bit_cast(float, __builtin_amdgcn_update_dpp(0, __builtin_bit_cast(int, v), CTRL, RM, 0xf, true));
}
__device__ __forceinline__ float wave_sum(float v) {
  v += dpp_f<0x111, 0xf>(v); v += dpp_f<0x112, 0xf>(v); v += dpp_f<0x114, 0xf>(v); v += dpp_f<0x118, 0xf>(v);
  v += dpp_f<0x142, 0xa>(v); v += dpp_f<0x143, 0xc>(v);
  return __builtin_bit_cast(float, __builtin_amdgcn_readlane(__builtin_bit_cast(int, v), 63));
}
template <int CTRL, int RM> __device__ __forceinline__ unsigned dpp_u(unsigned v) {
  return (unsigned)__builtin_amdgcn_update_dpp(0, (int)v, CTRL, RM, 0xf, true);
}
__device__ __forceinline__ unsigned umax(unsigned a, unsigned b) { return a > b ? a : b; }
__device__ __forceinline__ unsigned wave_max_u32(unsigned v) {
  v = umax(v, dpp_u<0x111, 0xf>(v)); v = umax(v, dpp_u<0x112, 0xf>(v)); v = umax(v, dpp_u<0x114, 0xf>(v)); v = umax(v, dpp_u<0x118, 0xf>(v));
  v = umax(v, dpp_u<0x142, 0xa>(v)); v = umax(v, dpp_u<0x143, 0xc>(v));
  return (unsigned)__builtin_amdgcn_readlane((int)v, 63);
}

#define STAMP_DECL
#define STAMPW(k, i, wv)
#define STAMP_FLUSH
#define STAMP(k, i) STAMPW(k, i, 0)

constexpr unsigned WS_ABT = 0;
constexpr unsigned WS_Y = WS_ABT + 1024u * 2048 * 2;
constexpr unsigned WS_F1 = WS_Y + 4u * 2048 * 512 * 2;
constexpr unsigned WS_F2 = WS_F1 + 4096;
constexpr unsigned WS_GR = WS_F2 + 2048;
constexpr unsigned WS_GA = WS_GR + 1024 * 8;
constexpr unsigned WS_EP = WS_GA + 224 * 256;
constexpr unsigned WS_SEL = WS_EP + 256;
constexpr unsigned WS_HN = WS_SEL + 2048;
constexpr unsigned WS_END = WS_HN + 2048u * 2048;

constexpr int SPIN_MAX = 1 << 18;
#define AUX1 0
#define CPF 1
#define FMODE 1
#define HNPUB 2
#define WPOS 0
#define COOP 1
#define TLBW 0
#define TS 0
#define QMODE 0
#define NPLAIN 8
#define T1MODE 1
#define FFNSTEP 2

constexpr int L_QR = 81920;
constexpr int L_RST = 65536;
constexpr int L_TAG = 66560;
constexpr int L_W = 16384;
constexpr int L_T2 = 131072;
constexpr int XS_STRIDE = 1024 + 16;
constexpr int L_XS = 81920;
constexpr int L_RED = L_XS + 8 * XS_STRIDE;
constexpr int L_MISC = L_RED + 8 * 128 * 4;
constexpr int L_HH = L_MISC + 1024;
constexpr int AS_STRIDE = F * 2 + 16;
constexpr int L_AS = L_HH + 1024;
static_assert(L_AS + 8 * AS_STRIDE <= L_T2, "lds");

struct Ring2 {
  static constexpr int p(int T) { return (T % 5) * 16384; }
  static constexpr int q(int T) { return L_QR + ((T + 3) % 5) * 16384; }
};
constexpr int koff(int T) { return T < 8 ? T * 128 : (T - 8) * 128 + 64; }
constexpr int x1slot(int s) { return L_QR + (s % 5) * 16384; }

template <int BP, int BQ, int NKT, class RG, bool STATS>
struct Cons {
  static constexpr int TP = BP / 32, TQ = BQ / 32;
  struct Ctx { const char* lds; int poff[TP][2]; int qoff[TQ][2]; };
  struct Fr { bf16x8 pf[2][TP], qf[2][TQ]; };
  struct St { f32x4 s1[2], s2[2], sw[TQ]; };
  template <int T> static __device__ __forceinline__ void readf(const Ctx& c, Fr& f) {
#pragma unroll
    for (int ks = 0; ks < 2; ++ks) {
#pragma unroll
      for (int m = 0; m < TP; ++m) f.pf[ks][m] = *(const bf16x8*)(c.lds + RG::p(T) + c.poff[m][ks]);
#pragma unroll
      for (int n = 0; n < TQ; ++n) f.qf[ks][n] = *(const bf16x8*)(c.lds + RG::q(T) + c.qoff[n][ks]);
    }
  }
  template <int T> static __device__ __forceinline__ void step(const Ctx& c, f32x4 (&acc)[TP][TQ], Fr& cur, Fr& nxt, St& st) {
    if constexpr (T + 1 < NKT) {
      WAIT_L(0);
      BAR();
      __builtin_amdgcn_sched_barrier(0);
      readf<T + 1>(c, nxt);
    }
    if constexpr (T + 1 == NKT) return;
#pragma unroll
    for (int ks = 0; ks < 2; ++ks) {
#pragma unroll
      for (int m = 0; m < TP; ++m)
#pragma unroll
        for (int n = 0; n < TQ; ++n) acc[m][n] = __builtin_amdgcn_mfma_f32_16x16x32_bf16(cur.pf[ks][m], cur.qf[ks][n], acc[m][n], 0, 0, 0);
      if constexpr (STATS) {
        const bf16x8 ones = {0x3f80, 0x3f80, 0x3f80, 0x3f80, 0x3f80, 0x3f80, 0x3f80, 0x3f80};
#pragma unroll
        for (int m = 0; m < 2; ++m) {
          st.s1[m] = __builtin_amdgcn_mfma_f32_16x16x32_bf16(cur.pf[ks][m], ones, st.s1[m], 0, 0, 0);
          st.s2[m] = __builtin_amdgcn_mfma_f32_16x16x32_bf16(cur.pf[ks][m], cur.pf[ks][m], st.s2[m], 0, 0, 0);
        }
#pragma unroll
        for (int n = 0; n < TQ; ++n) st.sw[n] = __builtin_amdgcn_mfma_f32_16x16x32_bf16(ones, cur.qf[ks][n], st.sw[n], 0, 0, 0);
      }
    }
    if constexpr (T + 1 < NKT) {
#pragma unroll
      for (int i = 0; i < 2 * (TP + TQ); ++i) { __builtin_amdgcn_sched_group_barrier(0x100, 1, 0); __builtin_amdgcn_sched_group_barrier(0x8, 1, 0); }
    }
    __builtin_amdgcn_sched_barrier(0);
  }
  template <int... Ts>
  static __device__ __forceinline__ void steps(const Ctx& c, f32x4 (&acc)[TP][TQ], Fr& f0, Fr& f1, St& st, std::integer_sequence<int, Ts...>) {
    ((Ts & 1 ? step<Ts>(c, acc, f1, f0, st) : step<Ts>(c, acc, f0, f1, st)), ...);
  }
  static __device__ __forceinline__ void run(const char* lds, int wp, int wq, int rot, int l, f32x4 (&acc)[TP][TQ], St& st, Fr& f1) {
    Ctx c; c.lds = lds;
    const int fr = l & 15, q = l >> 4;
#pragma unroll
    for (int ks = 0; ks < 2; ++ks) {
#pragma unroll
      for (int m = 0; m < TP; ++m) { const int row = wp * (BP / 2) + ((m + rot) & (TP - 1)) * 16 + fr; c.poff[m][ks] = row * 128 + (((ks * 4 + q) ^ ((row >> 1) & 7)) * 16); }
#pragma unroll
      for (int n = 0; n < TQ; ++n) { const int row = wq * (BQ / 2) + n * 16 + fr; c.qoff[n][ks] = row * 128 + (((ks * 4 + q) ^ ((row >> 1) & 7)) * 16); }
    }
    Fr f0;
    BAR();
    readf<0>(c, f0);
    steps(c, acc, f0, f1, st, std::make_integer_sequence<int, NKT>{});
  }
};

struct L1Ctx { const u16* xsrc[4]; const u16* wsrc[4]; char* lds; int w; };
template <int s> __device__ __forceinline__ void l1_issue(const L1Ctx& c) {
#pragma unroll
  for (int h = 0; h < 2; ++h) {
#pragma unroll
    for (int i = 0; i < 4; ++i) GLDS16(c.xsrc[i] + (size_t)(s >> 2) * 64 * D + (s & 3) * 128 + h * 64, c.lds + x1slot(s) + h * 8192 + (c.w * 4 + i) * 1024, AUX1);
    if constexpr (s < 4) {
#pragma unroll
      for (int i = 0; i < 4; ++i) GLDS16(c.wsrc[i] + s * 128 + h * 64, c.lds + (2 * s + h) * 8192 + (c.w * 4 + i) * 1024, AUX1);
    }
  }
}
struct Fr1 { bf16x8 pf[4][2], qf[4][2]; };
struct C1Ctx { const char* lds; int poff[2][2]; int qoff[2][2]; };
template <int s> __device__ __forceinline__ void c1_read(const C1Ctx& c, Fr1& f) {
#pragma unroll
  for (int ks = 0; ks < 4; ++ks) {
#pragma unroll
    for (int m = 0; m < 2; ++m) f.pf[ks][m] = *(const bf16x8*)(c.lds + x1slot(s) + (ks >> 1) * 8192 + c.poff[m][ks & 1]);
#pragma unroll
    for (int n = 0; n < 2; ++n) f.qf[ks][n] = *(const bf16x8*)(c.lds + (2 * (s & 3) + (ks >> 1)) * 8192 + c.qoff[n][ks & 1]);
  }
}
struct Acc1 { f32x4 acc[2][2], s1, s2; };
template <int s> __device__ __forceinline__ void c1_step(const C1Ctx& c, Acc1& a, f32x4 (&sw)[2], Fr1& cur, Fr1& nxt) {
  if constexpr (s == 7) WAIT_V(0);
  WAIT_L(0);
  BAR();
  __builtin_amdgcn_sched_barrier(0);
  if constexpr (s + 1 < 8) c1_read<s + 1>(c, nxt);
  const bf16x8 ones = {0x3f80, 0x3f80, 0x3f80, 0x3f80, 0x3f80, 0x3f80, 0x3f80, 0x3f80};
#pragma unroll
  for (int ks = 0; ks < 4; ++ks) {
#pragma unroll
    for (int m = 0; m < 2; ++m)
#pragma unroll
      for (int n = 0; n < 2; ++n) a.acc[m][n] = __builtin_amdgcn_mfma_f32_16x16x32_bf16(cur.pf[ks][m], cur.qf[ks][n], a.acc[m][n], 0, 0, 0);
    a.s1 = __builtin_amdgcn_mfma_f32_16x16x32_bf16(cur.pf[ks][0], ones, a.s1, 0, 0, 0);
    a.s2 = __builtin_amdgcn_mfma_f32_16x16x32_bf16(cur.pf[ks][0], cur.pf[ks][0], a.s2, 0, 0, 0);
    if constexpr (s < 4) {
#pragma unroll
      for (int n = 0; n < 2; ++n) sw[n] = __builtin_amdgcn_mfma_f32_16x16x32_bf16(ones, cur.qf[ks][n], sw[n], 0, 0, 0);
    }
  }
  if constexpr (s + 1 < 8) {
#pragma unroll
    for (int i = 0; i < 16; ++i) { __builtin_amdgcn_sched_group_barrier(0x100, 1, 0); __builtin_amdgcn_sched_group_barrier(0x8, 1, 0); }
  }
  __builtin_amdgcn_sched_barrier(0);
}

struct L2Ctx { const u16* psrc[8]; const u16* qsrc[8]; char* lds; int w; };
template <int T> __device__ __forceinline__ void l2_issueP(const L2Ctx& c) {
#pragma unroll
  for (int i = 0; i < 8; ++i) GLDS16(c.psrc[i] + koff(T), c.lds + Ring2::p(T) + (c.w * 8 + i) * 1024, 16);
}
template <int T, int AUX> __device__ __forceinline__ void l2_issueQ(const L2Ctx& c) {
#pragma unroll
  for (int i = 0; i < 8; ++i) GLDS16(c.qsrc[i] + koff(T), c.lds + Ring2::q(T) + (c.w * 8 + i) * 1024, AUX);
}
constexpr int l2_cnt(int s) { return s < 5 ? 8 : 16; }
constexpr int l2_younger(int T) { int up = T + 4 < 15 ? T + 4 : 15; int n = 0; for (int s = T + 2; s <= up; ++s) n += l2_cnt(s); return n; }
template <int T, int AUX> __device__ __forceinline__ void l2_step(const L2Ctx& c) {
  WAIT_V(l2_younger(T));
  BAR();
  if constexpr (T + 5 < 16) { l2_issueP<T + 5>(c); l2_issueQ<T + 5, AUX>(c); }
}
template <int AUX, int... Ts> __device__ __forceinline__ void l2_steps(const L2Ctx& c, std::integer_sequence<int, Ts...>) { (l2_step<Ts, AUX>(c), ...); }

__device__ __forceinline__ u32x4 widen16(u32x2 a, u32x2 c) {
  const auto s0 = __builtin_amdgcn_permlane16_swap(a[0], c[0], false, false);
  const auto s1 = __builtin_amdgcn_permlane16_swap(a[1], c[1], false, false);
  return (u32x4){s0[0], s1[0], s0[1], s1[1]};
}

__device__ __forceinline__ void glds16_asm(const void* gsrc, unsigned lds_dst) {
  unsigned keep;
  asm volatile("s_mov_b32 %0, m0\n\ts_mov_b32 m0, %2\n\ts_nop 0\n\tglobal_load_lds_dwordx4 %1, off\n\ts_mov_b32 m0, %0" : "=&s"(keep) : "v"(gsrc), "s"(lds_dst) : "memory");
}

__device__ __forceinline__ void poll_words(const unsigned* p, unsigned tag) {
  for (int it = 0; it < SPIN_MAX; ++it) {
    const unsigned v = __hip_atomic_load((gu32*)p, RLX_AGENT);
    if (__all(v == tag)) break;
    __builtin_amdgcn_s_sleep(1);
  }
  __builtin_amdgcn_fence(__ATOMIC_ACQUIRE, "wavefront");
}

__device__ __forceinline__ unsigned add_pk(unsigned a, unsigned b) { return pack2(bflo(a) + bflo(b), bfhi(a) + bfhi(b)); }
__device__ __forceinline__ unsigned sub_pk(unsigned a, unsigned b) { return pack2(bflo(a) - bflo(b), bfhi(a) - bfhi(b)); }
template <class R>
__device__ __forceinline__ void load_h(const u32x4 xv, R rs, int t, int l, float (&h)[8]) {
  const unsigned o = (unsigned)(t * D + l * 8) * 2;
  const u32x4 y0 = __builtin_amdgcn_raw_buffer_load_b128(rs, WS_Y + o, 0, 16);
  const u32x4 y1 = __builtin_amdgcn_raw_buffer_load_b128(rs, WS_Y + 1u * S * D * 2 + o, 0, 16);
  const u32x4 y2 = __builtin_amdgcn_raw_buffer_load_b128(rs, WS_Y + 2u * S * D * 2 + o, 0, 16);
  const u32x4 y3 = __builtin_amdgcn_raw_buffer_load_b128(rs, WS_Y + 3u * S * D * 2 + o, 0, 16);
#pragma unroll
  for (int e = 0; e < 4; ++e) {
    const unsigned yv = pack2((bflo(y0[e]) + bflo(y1[e])) - (bflo(y2[e]) + bflo(y3[e])), (bfhi(y0[e]) + bfhi(y1[e])) - (bfhi(y2[e]) + bfhi(y3[e])));
    const unsigned hv = add_pk(xv[e], yv);
    h[2 * e] = bflo(hv); h[2 * e + 1] = bfhi(hv);
  }
}
__device__ __forceinline__ u32x4 layernorm8(const float (&h)[8]) {
  const float pv = __builtin_bit_cast(float, __builtin_amdgcn_readfirstlane(__builtin_bit_cast(int, h[0])));
  float s = 0.f, s2 = 0.f, v = 0.f, v2 = 0.f;
#pragma unroll
  for (int e = 0; e < 4; ++e) { const float a = h[2 * e] - pv, c = h[2 * e + 1] - pv; s += a; s2 += c; v += a * a; v2 += c * c; }
  const float md = wave_sum(s + s2) * (1.0f / D);
  v = fmaxf(wave_sum(v + v2) - md * md * (float)D, 0.f);
  const float mean = pv + md;
  const float mb = rbf(mean);
  const float sd = rbf(sqrtf(rbf(rbf(v * (1.0f / D)) + 1e-5f)));
  const float inv = 1.0f / sd;
  u32x4 o;
#pragma unroll
  for (int e = 0; e < 4; ++e) {
    const unsigned d = pack2(h[2 * e] - mb, h[2 * e + 1] - mb);
    const float a = bflo(d), c = bfhi(d);
    float qa = a * inv, qc = c * inv;
    qa = fmaf(fmaf(-qa, sd, a), inv, qa); qc = fmaf(fmaf(-qc, sd, c), inv, qc);
    o[e] = pack2(qa, qc);
  }
  return o;
}

__global__ __launch_bounds__(512) void fused(const u16* __restrict__ x, const u16* __restrict__ ws_re, const u16* __restrict__ ws_im,
                                             const u16* __restrict__ wd_re, const u16* __restrict__ wd_im, const u16* __restrict__ w_r,
                                             const u16* __restrict__ w_gate, const u16* __restrict__ w_up, const u16* __restrict__ w_down,
                                             u16* __restrict__ out, char* __restrict__ ws) {
  __shared__ __attribute__((aligned(1024))) char lds[163840];
  const int b = blockIdx.x, xcd = b & 7, j = b >> 3;
  const int cc = xcd >> 2, et = j & 3, tt = (xcd & 3) * 4 + ((j >> 2) & 3), kh = j >> 4;
  const int g1 = (cc * 4 + et) * 2 + kh;
  const int e1 = cc * 8 + et * 2 + (tt & 1), s1 = kh * 8 + (tt >> 1);
  const int tid = threadIdx.x, w = __builtin_amdgcn_readfirstlane(tid >> 6), l = tid & 63, fr = l & 15, q = l >> 4;
  const u16* wsb = cc ? ws_im : ws_re;
  const u16* wdb = cc ? wd_im : wd_re;
  const bool down = j >= 28;
  const int fb = xcd * 28 + j, db = xcd * 4 + (j - 28);
  const auto rs = __builtin_amdgcn_make_buffer_rsrc(ws, 0, WS_END, 0x00020000);
  unsigned tag;
  const unsigned long long pad_t0 = __builtin_amdgcn_s_memrealtime();
  STAMP_DECL;
  STAMP(0, 0);
  unsigned long long code_lo; unsigned code_len;
  asm volatile("s_getpc_b64 %0\n.Lfused_code_lo:\n\ts_mov_b32 %1, .Lfused_code_hi-.Lfused_code_lo" : "=s"(code_lo), "=s"(code_len));

  const unsigned lbase = (unsigned)(uintptr_t)(LAS char*)lds;
  {
    const int row = w * 8 + (l >> 3);
    const size_t o = (size_t)row * D + (((l & 7) ^ ((row >> 1) & 7)) * 8);
    const u16* xp = x + (size_t)s1 * 128 * D + o;
    const u16* wp_ = wdb + (size_t)(e1 & 7) * 64 * D + o;
#pragma unroll
    for (int k = 0; k < 4; ++k) {
      glds16_asm(xp + k * 64, lbase + x1slot(k >> 1) + (k & 1) * 8192 + w * 1024);
      glds16_asm(wp_ + k * 64, lbase + k * 8192 + w * 1024);
    }
  }
  if (w < 4) {
    const int wp = w >> 1, wq = w & 1;
    unsigned r0 = 0;
    {
      const int row = tid >> 3, ln = tid & 7;
      const int xs_tile = ((j >> 3) & 1) * 8 + (xcd & 3) * 2 + ((j >> 2) & 1);
      const u16* p = j < 16 ? x + (size_t)(xs_tile * 128 + (j & 3) * 32 + row) * D + ln * 64 : wdb + (size_t)((j - 16) * 32 + row) * D + ln * 64;
      if (T1MODE == 1) TOUCH(r0, p);
      if (T1MODE == 2) TOUCH_NT(r0, p);
    }
    {
      C1Ctx c; c.lds = lds;
#pragma unroll
      for (int ks = 0; ks < 2; ++ks) {
#pragma unroll
        for (int m = 0; m < 2; ++m) { const int row = wp * 32 + (m ^ wq) * 16 + fr; c.poff[m][ks] = row * 128 + (((ks * 4 + q) ^ ((row >> 1) & 7)) * 16); }
#pragma unroll
        for (int n = 0; n < 2; ++n) { const int row = wq * 32 + n * 16 + fr; c.qoff[n][ks] = row * 128 + (((ks * 4 + q) ^ ((row >> 1) & 7)) * 16); }
      }
      f32x4 sw[2] = {};
      Fr1 f0, f1;
      auto epilogue = [&](const Acc1& a, int pass) {
        float2* rst = (float2*)(lds + L_RST) + pass * 64;
        if ((fr >> 2) == q) {
          const int jj = fr & 3;
          const float s1v = jj == 0 ? a.s1[0] : jj == 1 ? a.s1[1] : jj == 2 ? a.s1[2] : a.s1[3];
          const float s2v = jj == 0 ? a.s2[0] : jj == 1 ? a.s2[1] : jj == 2 ? a.s2[2] : a.s2[3];
          const float mean = s1v * (1.0f / D);
          const float var = fmaxf(s2v * (1.0f / D) - mean * mean, 0.f);
          const float sd = rbf(sqrtf(rbf(rbf(var) + 1e-5f)));
          rst[wp * 32 + wq * 16 + fr] = make_float2(rbf(mean), 1.0f / sd);
        }
        WAIT_L(0);
        BAR();
        u32x2 pk[2][2];
#pragma unroll
        for (int m = 0; m < 2; ++m) {
          const int p0 = wp * 32 + (m ^ wq) * 16 + q * 4;
          const f32x4 sa = *(const f32x4*)(rst + p0), sb = *(const f32x4*)(rst + p0 + 2);
#pragma unroll
          for (int n = 0; n < 2; ++n) {
            const float swv = sw[n][0];
            const float v0 = (a.acc[m][n][0] - sa[0] * swv) * sa[1], v1 = (a.acc[m][n][1] - sa[2] * swv) * sa[3];
            const float v2 = (a.acc[m][n][2] - sb[0] * swv) * sb[1], v3 = (a.acc[m][n][3] - sb[2] * swv) * sb[3];
            pk[m][n] = (u32x2){pack2(v0, v1), pack2(v2, v3)};
          }
        }
        const int scol = s1 * 128 + pass * 64 + wp * 32 + ((q & 1) ^ wq) * 16 + (q >> 1) * 8;
#pragma unroll
        for (int n = 0; n < 2; ++n)
          __builtin_amdgcn_raw_buffer_store_b128(widen16(pk[0][n], pk[1][n]), rs, WS_ABT + (unsigned)((e1 * 64 + wq * 32 + n * 16 + fr) * S + scol) * 2, 0, 16);
      };
      if (COOP) WAIT_V(5);
      BAR();
      c1_read<0>(c, f0);
      if (COOP) WAIT_V(1);
      {
        Acc1 a = {};
        c1_step<0>(c, a, sw, f0, f1); c1_step<1>(c, a, sw, f1, f0); c1_step<2>(c, a, sw, f0, f1); c1_step<3>(c, a, sw, f1, f0);
        STAMP(0, 2);
        epilogue(a, 0);
        STAMP(0, 3);
      }
      {
        Acc1 a = {};
        c1_step<4>(c, a, sw, f0, f1); c1_step<5>(c, a, sw, f1, f0); c1_step<6>(c, a, sw, f0, f1); c1_step<7>(c, a, sw, f1, f0);
        STAMP(0, 4);
        epilogue(a, 1);
        STAMP(0, 5);
      }
      tag = __builtin_amdgcn_readfirstlane(*(const unsigned*)(lds + L_TAG));
      WAIT_V(0);
      KEEP(r0);
      STAMP(0, 6);
      BAR();
    }
    using C2 = Cons<128, 128, 16, Ring2, false>;
    f32x4 acc[4][4] = {};
    C2::St st;
    C2::Fr fl;
    C2::run(lds, wp, wq, 0, l, acc, st, fl);
    STAMP(1, 2);
    {
      const unsigned yo = WS_Y + (unsigned)(cc * 2 + kh) * S * D * 2;
      const int chunk = (q >> 1) + 2 * (q & 1);
#pragma unroll
      for (int n = 0; n < 4; ++n) {
#pragma unroll
        for (int ks = 0; ks < 2; ++ks)
#pragma unroll
          for (int m = 0; m < 4; ++m) acc[m][n] = __builtin_amdgcn_mfma_f32_16x16x32_bf16(fl.pf[ks][m], fl.qf[ks][n], acc[m][n], 0, 0, 0);
#pragma unroll
        for (int mp = 0; mp < 2; ++mp) {
          const u32x2 a = {pack2(acc[2 * mp][n][0], acc[2 * mp][n][1]), pack2(acc[2 * mp][n][2], acc[2 * mp][n][3])};
          const u32x2 c = {pack2(acc[2 * mp + 1][n][0], acc[2 * mp + 1][n][1]), pack2(acc[2 * mp + 1][n][2], acc[2 * mp + 1][n][3])};
          const int row = wq * 64 + n * 16 + fr;
          __builtin_amdgcn_raw_buffer_store_b128(widen16(a, c), rs, yo + (unsigned)((tt * 128 + row) * D + et * 128 + wp * 64 + mp * 32 + chunk * 8) * 2, 0, 16);
        }
      }
    }
    STAMP(1, 3);
    WAIT_V(0);
    STAMP(1, 6);
  } else if (w < 6) {
    const int lw = w - 4;
    const int r8 = l >> 3, ch = l & 7;
    {
      L1Ctx c; c.lds = lds; c.w = lw;
      const u16* P = x + (size_t)s1 * 128 * D;
      const u16* Q = wdb + (size_t)(e1 & 7) * 64 * D;
#pragma unroll
      for (int i = 0; i < 4; ++i) {
        const int row = (lw * 4 + i) * 8 + r8;
        const size_t o = (size_t)row * D + ((ch ^ ((row >> 1) & 7)) * 8);
        c.xsrc[i] = P + o; c.wsrc[i] = Q + o;
      }
      l1_issue<2>(c);
      WAIT_V(20); STAMPW(0, 1, 4); BAR();
      WAIT_V(16); BAR();
      l1_issue<3>(c);
      WAIT_V(16); BAR();
      l1_issue<4>(c); l1_issue<5>(c);
      WAIT_V(16); BAR();
      l1_issue<6>(c); l1_issue<7>(c);
      WAIT_V(24); BAR();
      BAR();
      WAIT_V(16); BAR();
      WAIT_V(8); BAR();
      WAIT_V(0); BAR();
    }
    L2Ctx c2;
    c2.lds = lds; c2.w = lw;
    {
      const u16* P = (const u16*)(ws + WS_ABT) + (size_t)(cc * 512 + et * 128) * S + kh * 1024;
      const u16* Q = wsb + (size_t)tt * 128 * S + kh * 1024;
#pragma unroll
      for (int i = 0; i < 8; ++i) {
        const int row = (lw * 8 + i) * 8 + r8;
        const size_t o = (size_t)row * S + ((ch ^ ((row >> 1) & 7)) * 8);
        c2.psrc[i] = P + o; c2.qsrc[i] = Q + o;
      }
    }
    tag = __builtin_amdgcn_readfirstlane(*(const unsigned*)(lds + L_TAG));
    BAR();
    BAR();
    poll_words((const unsigned*)(ws + WS_F1) + g1 * 32 + fr, tag);
    STAMPW(1, 0, 4);
    l2_issueP<0>(c2); l2_issueP<1>(c2); l2_issueP<2>(c2);
    BAR();
    WAIT_V(16);
    STAMPW(1, 1, 4);
    BAR();
    l2_issueP<3>(c2); l2_issueP<4>(c2);
    l2_steps<0>(c2, std::make_integer_sequence<int, 15>{});
  } else if (w == 6) {
    const int r8 = l >> 3, ch = l & 7;
    const unsigned ep = __hip_atomic_load((gu32*)(ws + WS_EP), RLX_AGENT);
    const u16* Q = wsb + (size_t)tt * 128 * S + kh * 1024;
    const u16* qa = Q + (size_t)r8 * S + ((ch ^ (r8 >> 1)) * 8);
    const u16* qb = Q + (size_t)(8 + r8) * S + ((ch ^ (4 + (r8 >> 1))) * 8);
    auto issueQ = [&](int T) {
#pragma unroll
      for (int i = 0; i < 8; ++i) {
        if (QMODE == 0 || (QMODE == 1 && 2 * i < NPLAIN)) {
          GLDS16(qa + (size_t)i * 16 * S + T * 128, lds + L_QR + ((T + 3) % 5) * 16384 + (2 * i) * 1024, 0);
          GLDS16(qb + (size_t)i * 16 * S + T * 128, lds + L_QR + ((T + 3) % 5) * 16384 + (2 * i + 1) * 1024, 0);
        } else {
          GLDS16(qa + (size_t)i * 16 * S + T * 128, lds + L_QR + ((T + 3) % 5) * 16384 + (2 * i) * 1024, 2);
          GLDS16(qb + (size_t)i * 16 * S + T * 128, lds + L_QR + ((T + 3) % 5) * 16384 + (2 * i + 1) * 1024, 2);
        }
      }
    };
    if (COOP) WAIT_V(5);
    BAR();
    if (COOP) WAIT_V(1);
    BAR();
    tag = __builtin_amdgcn_readfirstlane(ep * 1664525u + 1013904223u);
    *(unsigned*)(lds + L_TAG) = tag;
    WAIT_L(0);
    BAR(); BAR(); BAR();
    BAR();
    BAR(); issueQ(0);
    BAR(); issueQ(1);
    BAR(); issueQ(2);
    BAR(); BAR();
    BAR(); issueQ(3); issueQ(4);
    WAIT_V(32);
    BAR();
    const unsigned* fB = (const unsigned*)(ws + WS_F1) + 512 + g1 * 32 + fr;
    BAR(); BAR();
    const unsigned vB = __hip_atomic_load((gu32*)fB, RLX_AGENT);
    WAIT_V(17);
    BAR();
    if (!__all(vB == tag)) poll_words(fB, tag);
    WAIT_V(0);
    BAR();
#pragma unroll
    for (int T = 4; T < 15; ++T) BAR();
  } else {
    unsigned r[4] = {0, 0, 0, 0};
    constexpr int LPI = 64 / (2 * NPLAIN) > 0 ? 64 / (2 * NPLAIN) : 1;
    const int pr = j * NPLAIN + (l % NPLAIN);
    const int prow = (pr / (NPLAIN * 8)) * 128 + (pr % (NPLAIN * 8));
    const u16* wb = wsb + (size_t)((xcd & 3) * 512 + prow) * S + ((l / NPLAIN) & 1) * 1024 + (l / (2 * NPLAIN)) * 128;
#define TW(i) TOUCH(r[(i) & 1], wb + ((i) % (8 / LPI)) * (LPI * 128) + ((i) / (8 / LPI)) * 64)
    auto ffn_ptr = [&](int i) -> const char* {
      const int li = j * 192 + i * 64 + l;
      if (li < 4096) { const int mrow = (li & 2047) >> 2, c4 = li & 3; return (const char*)((li < 2048 ? w_gate : w_up) + (size_t)mrow * F + xcd * 224 + c4 * 64); }
      const int rr = li - 4096;
      return (const char*)(w_down + (size_t)(rr < F ? rr : 0) * D + xcd * 64);
    };
    if (TLBW) {
      TOUCH_SC1(r[3], ws + (size_t)l * 65536);
      TOUCH_SC1(r[3], ws + WS_Y + (size_t)l * 65536);
      TOUCH_SC1(r[3], ws + WS_Y + (size_t)(64 + l) * 65536);
      TOUCH_SC1(r[3], (l < 32 ? (const char*)out + (size_t)l * 65536 : l < 34 ? (const char*)ws + WS_F1 + (size_t)(l - 32) * 65536
                       : (const char*)wsb + (size_t)(xcd & 3) * 2097152 + (size_t)(l - 34) * 65536));
    }
    if (CPF) {
      const unsigned off = (unsigned)(j * 16 + fr) * 128u;
      const char* cp = (const char*)code_lo + (off < code_len ? off : 0u);
      if (l < 16) TOUCH(r[3], cp);
    }
    const char* fp0 = ffn_ptr(0); const char* fp1 = ffn_ptr(1); const char* fp2 = ffn_ptr(2);
    const u16* wb2 = wsb + (size_t)((xcd & 3) * 512 + j * 16 + fr) * S + ((l >> 4) & 1) * 1024 + (l >> 5) * 128;
#define TF(i) TOUCH(r[(i) & 1], wb2 + ((i) & 3) * 256 + ((i) >> 2) * 64)
#define TFF(p) TOUCH(r[2], p)
    if (COOP) WAIT_V(4 + CPF);
    BAR();
    if (COOP) WAIT_V(CPF);
    BAR(); BAR(); TFF(fp0);
    tag = __builtin_amdgcn_readfirstlane(*(const unsigned*)(lds + L_TAG));
    BAR(); TFF(fp1); BAR(); TFF(fp2); BAR();
    BAR(); BAR(); BAR();
    BAR();
    if (l == 0) __hip_atomic_store((gu32*)(ws + WS_F1) + g1 * 32 + tt, tag, RLX_AGENT);
    BAR();
    BAR();
    if (l == 0) __hip_atomic_store((gu32*)(ws + WS_F1) + 512 + g1 * 32 + tt, tag, RLX_AGENT);
#pragma unroll
    for (int T = 0; T < 16; ++T) {
      BAR();
    }
    WAIT_V(0);
    KEEP(r[0]); KEEP(r[1]); KEEP(r[2]); KEEP(r[3]);
  }
  const int t_own = tt * 128 + g1 * 8 + w;
  unsigned rf = 0;
  const u32x4 wr = *(const u32x4*)(w_r + l * 8);
  const u32x4 x_own = *(const u32x4*)(x + (size_t)t_own * D + l * 8);
  {
    BAR();
    if (tid == 0) __hip_atomic_store((gu32*)(ws + WS_F2) + tt * 32 + g1, tag, RLX_AGENT);
    STAMP(1, 4);
    if (w == 0) poll_words((const unsigned*)(ws + WS_F2) + tt * 32 + fr, tag);
    if (FMODE == 0) {
    if (w >= 5) {
      const int li = j * 192 + (w - 5) * 64 + l;
      const char* p2;
      if (li < 4096) { const int mrow = (li & 2047) >> 2, c4 = li & 3; p2 = (const char*)((li < 2048 ? w_gate : w_up) + (size_t)mrow * F + xcd * 224 + c4 * 64); }
      else { const int rr = li - 4096; p2 = (const char*)(w_down + (size_t)(rr < F ? rr : 0) * D + xcd * 64); }
      TOUCH(rf, p2);
    }
    }
    BAR();
  }
  STAMP(2, 0);
  unsigned* misc = (unsigned*)(lds + L_MISC);
  u32x4 own;
  {
    float h[8];
    load_h(x_own, rs, t_own, l, h);
    STAMP(2, 7);
    const u32x4 hn = layernorm8(h);
    float r = 0.f, r2 = 0.f;
#pragma unroll
    for (int e = 0; e < 4; ++e) { r += bflo(hn[e]) * bflo(wr[e]); r2 += bfhi(hn[e]) * bfhi(wr[e]); }
    r = wave_sum(r + r2);
    const unsigned rb = f2bf(r);
    const unsigned ord = (rb & 0x8000u) ? (~rb & 0xffffu) : (rb | 0x8000u);
    if (l == 0) { ((u16*)misc)[w] = (u16)ord; if (w == 0) misc[8] = 0; }
    WAIT_L(0);
    BAR();
    if (tid < 4) __hip_atomic_store((gu64*)(ws + WS_GR) + (tt * 16 + g1) * 4 + tid, ((unsigned long long)tag << 32) | misc[tid], RLX_AGENT);
    bool pub = HNPUB == 1;
    if (HNPUB == 2) {
      const unsigned ov = ((const u16*)misc)[l & 7];
      pub = !__any(ov > ord || (ov == ord && (l & 7) < w));
    }
    if (pub) {
      __builtin_amdgcn_raw_buffer_store_b128((u32x4){hn[0], tag, hn[1], tag}, rs, WS_HN + (unsigned)t_own * 2048 + l * 32, 0, 16);
      __builtin_amdgcn_raw_buffer_store_b128((u32x4){hn[2], tag, hn[3], tag}, rs, WS_HN + (unsigned)t_own * 2048 + l * 32 + 16, 0, 16);
    }
#pragma unroll
    for (int e = 0; e < 4; ++e) own[e] = pack2(h[2 * e] + bflo(hn[e]), h[2 * e + 1] + bfhi(hn[e]));
  }
  KEEP(rf);
  {
    const u16* wsrc = down ? w_down + (size_t)(l >> 1) * D + db * 16 + (l & 1) * 8 : ((l & 1) ? w_up : w_gate) + (size_t)(l >> 1) * F + fb * 8;
    if (down) {
#pragma unroll
      for (int i = 0; i < 7; ++i) glds16_asm(wsrc + (size_t)(w * 7 + i) * 32 * D, lbase + L_W + (w * 7 + i) * 1024);
    } else {
#pragma unroll
      for (int i = 0; i < 2; ++i) glds16_asm(wsrc + (size_t)(w * 2 + i) * 32 * F, lbase + L_W + (w * 2 + i) * 1024);
    }
  }
  STAMP(2, 1);
  unsigned key;
  bool sel_own;
  bool fast = HNPUB == 1;
  if (!down) {
  unsigned kk[4];
  {
    u32x4 a;
    for (int it = 0; it < SPIN_MAX; ++it) {
      asm volatile("" ::: "memory");
      a = __builtin_amdgcn_raw_buffer_load_b128(rs, WS_GR + (unsigned)tid * 16, 0, 16);
      if (__all(a[1] == tag && a[3] == tag)) break;
      __builtin_amdgcn_s_sleep(1);
    }
    __builtin_amdgcn_fence(__ATOMIC_ACQUIRE, "wavefront");
    const unsigned ib = 0xffffu - (unsigned)tid * 4;
    kk[0] = (a[0] << 16) | ib; kk[1] = (a[0] & 0xffff0000u) | (ib - 1); kk[2] = (a[2] << 16) | (ib - 2); kk[3] = (a[2] & 0xffff0000u) | (ib - 3);
  }
  STAMP(2, 2);
  {
    const unsigned m = wave_max_u32(umax(umax(kk[0], kk[1]), umax(kk[2], kk[3])));
    if (l == 0) misc[16 + w] = m;
  }
  WAIT_L(0);
  BAR();
  STAMP(2, 5);
  if (b == 0 && tid == 0) __hip_atomic_store((gu32*)(ws + WS_EP), tag, RLX_AGENT);
  {
    const u32x4 ma = *(const u32x4*)(misc + 16), mb2 = *(const u32x4*)(misc + 20);
    const unsigned lo = min(min(min(ma[0], ma[1]), min(ma[2], ma[3])), min(min(mb2[0], mb2[1]), min(mb2[2], mb2[3])));
    if (l < 16) misc[64 + w * 16 + l] = 0u;
    unsigned base = 0;
#pragma unroll
    for (int e = 0; e < 4; ++e) {
      const unsigned long long bm = __ballot(kk[e] >= lo);
      const unsigned pos = base + __builtin_amdgcn_mbcnt_hi((unsigned)(bm >> 32), __builtin_amdgcn_mbcnt_lo((unsigned)bm, 0u));
      if (kk[e] >= lo && pos < 16) misc[64 + w * 16 + pos] = kk[e];
      base += (unsigned)__builtin_popcountll(bm);
    }
    if (base > 16 && l == 0) misc[8] = 1u;
  }
  WAIT_L(0);
  BAR();
  STAMP(2, 6);
  {
    const unsigned ovf = __builtin_amdgcn_readfirstlane(misc[8]);
    if (ovf == 0) {
      const unsigned cv = misc[64 + w * 16 + (l >> 2)];
      int rank = 0;
#pragma unroll
      for (int i = 0; i < 8; ++i) {
        const u32x4 c4 = *(const u32x4*)(misc + 64 + (l & 3) * 32 + i * 4);
#pragma unroll
        for (int e = 0; e < 4; ++e) rank += (c4[e] > cv) ? 1 : 0;
      }
      rank += (int)dpp_u<0xB1, 0xf>((unsigned)rank);
      rank += (int)dpp_u<0x4E, 0xf>((unsigned)rank);
      if (cv != 0u && rank < 8 && (l & 3) == 0) misc[32 + rank] = cv;
      WAIT_L(0);
      BAR();
      const unsigned sv = misc[32 + (l & 7)];
      if (b < 8 && tid < 8) __hip_atomic_store((gu64*)(ws + WS_SEL) + b * 32 + tid, ((unsigned long long)tag << 32) | sv, RLX_AGENT);
      key = (unsigned)__builtin_amdgcn_readlane((int)sv, w);
      sel_own = __any((int)(sv & 0xffffu) == 0xffff - t_own);
      fast = !__any(sv > key && ((0xffffu - (sv & 0xffffu)) >> 3) == ((0xffffu - (key & 0xffffu)) >> 3));
    } else {
      unsigned mine = 0;
#pragma unroll 1
      for (int it = 0; it < 8; ++it) {
        const unsigned m = wave_max_u32(umax(umax(kk[0], kk[1]), umax(kk[2], kk[3])));
#pragma unroll
        for (int e = 0; e < 4; ++e) kk[e] = (kk[e] == m) ? 0u : kk[e];
        if (l == it) mine = m;
      }
      if (l < 8) misc[192 + w * 8 + l] = mine;
      WAIT_L(0);
      BAR();
      unsigned cv = misc[192 + l];
      key = 0; sel_own = false;
#pragma unroll 1
      for (int it = 0; it < 8; ++it) {
        const unsigned m = wave_max_u32(cv);
        if (it == w) key = m;
        sel_own |= (int)(m & 0xffffu) == 0xffff - t_own;
        cv = (cv == m) ? 0u : cv;
      }
      if (b < 8 && l == 0) __hip_atomic_store((gu64*)(ws + WS_SEL) + b * 32 + w, ((unsigned long long)tag << 32) | key, RLX_AGENT);
    }
  }
  } else {
    if (w == 0) {
      unsigned long long g;
      for (int it = 0; it < SPIN_MAX; ++it) {
        g = __hip_atomic_load((gu64*)(ws + WS_SEL) + xcd * 32 + (l & 7), RLX_AGENT);
        if (__all((unsigned)(g >> 32) == tag)) break;
        __builtin_amdgcn_s_sleep(1);
      }
      if (l < 8) misc[32 + l] = (unsigned)g;
      WAIT_L(0);
    }
    BAR();
    const unsigned sv = misc[32 + (l & 7)];
    key = (unsigned)__builtin_amdgcn_readlane((int)sv, w);
    sel_own = __any((int)(sv & 0xffffu) == 0xffff - t_own);
    fast = !__any(sv > key && ((0xffffu - (sv & 0xffffu)) >> 3) == ((0xffffu - (key & 0xffffu)) >> 3));
  }
  STAMP(2, 3);
  const int t_sel = 0xffff - (int)(key & 0xffffu);
  bf16x8 bw[16];
  if (!down && w == 0) {
    const char* bp = lds + L_W + (q * 8 + (fr >> 2)) * 32 + (fr & 3) * 8;
#pragma unroll
    for (int ks = 0; ks < 16; ++ks) {
      const v4i16_t g0 = __builtin_amdgcn_ds_read_tr16_b64_v4i16((LAS v4i16_t*)(bp + ks * 1024));
      const v4i16_t g1v = __builtin_amdgcn_ds_read_tr16_b64_v4i16((LAS v4i16_t*)(bp + ks * 1024 + 128));
      bw[ks] = (bf16x8){g0[0], g0[1], g0[2], g0[3], g1v[0], g1v[1], g1v[2], g1v[3]};
    }
  }
  {
    float h[8];
    u32x4 hn;
    if (down || !fast) {
      const u32x4 xs = *(const u32x4*)(x + (size_t)t_sel * D + l * 8);
      load_h(xs, rs, t_sel, l, h);
    }
    if (fast) {
      u32x4 a, c;
      for (int it = 0; it < SPIN_MAX; ++it) {
        asm volatile("" ::: "memory");
        a = __builtin_amdgcn_raw_buffer_load_b128(rs, WS_HN + (unsigned)t_sel * 2048 + l * 32, 0, 16);
        c = __builtin_amdgcn_raw_buffer_load_b128(rs, WS_HN + (unsigned)t_sel * 2048 + l * 32 + 16, 0, 16);
        if (__all(a[1] == tag && a[3] == tag && c[1] == tag && c[3] == tag)) break;
        __builtin_amdgcn_s_sleep(1);
      }
      hn = (u32x4){a[0], a[2], c[0], c[2]};
    } else {
      hn = layernorm8(h);
    }
    if (!down) {
      *(u32x4*)(lds + L_XS + w * XS_STRIDE + l * 16) = hn;
    } else {
      if ((l >> 1) == db) {
        float2* hh = (float2*)(lds + L_HH) + w * 16 + (l & 1) * 8;
#pragma unroll
        for (int e = 0; e < 4; ++e) { hh[2 * e] = make_float2(h[2 * e], bflo(hn[e])); hh[2 * e + 1] = make_float2(h[2 * e + 1], bfhi(hn[e])); }
      }
      if (l == 0) {
        const unsigned ord = key >> 16;
        const unsigned rb = (ord & 0x8000u) ? (ord & 0x7fffu) : (~ord & 0xffffu);
        misc[24 + w] = key;
        misc[40 + w] = __float_as_uint(rbf(1.0f / (1.0f + expf(-bf2f(rb)))));
      }
    }
    if (!sel_own) *(u32x4*)(out + (size_t)t_own * D + l * 8) = own;
  }
  STAMP(2, 4);
  float* red = (float*)(lds + L_RED);
  if (!down) {
    WAIT_L(0);
    BAR();
    if (w == 0) {
      f32x4 a4[2] = {};
      const char* ap = lds + L_XS + (fr & 7) * XS_STRIDE + q * 16;
#pragma unroll
      for (int ks = 0; ks < 16; ++ks) {
        const bf16x8 a = *(const bf16x8*)(ap + ks * 64);
        a4[ks & 1] = __builtin_amdgcn_mfma_f32_16x16x32_bf16(a, bw[ks], a4[ks & 1], 0, 0, 0);
      }
      const f32x4 sum = a4[0] + a4[1];
      const bool hi = fr >= 8;
      unsigned actb[2];
#pragma unroll
      for (int k = 0; k < 2; ++k) {
        const float send = hi ? sum[k] : sum[2 + k];
        const float recv = dpp_f<0x128, 0xf>(send);
        const float g = rbf(hi ? recv : sum[k]), u = rbf(hi ? sum[2 + k] : recv);
        const float sg = rbf(g / (1.0f + expf(-g)));
        actb[k] = f2bf(sg * u);
      }
      const unsigned n0 = dpp_u<0xB1, 0xf>(actb[0]), n1 = dpp_u<0xB1, 0xf>(actb[1]);
      const unsigned val = (fr & 1) ? (n1 | (actb[1] << 16)) : (actb[0] | (n0 << 16));
      const int tok = q * 4 + (hi ? 2 : 0) + (fr & 1);
      if (q < 2) __hip_atomic_store((gu64*)(ws + WS_GA) + fb * 32 + tok * 4 + ((fr & 7) >> 1), ((unsigned long long)tag << 32) | val, RLX_AGENT);
    }
    STAMP(3, 0);
  } else {
    for (int it = 0; it < SPIN_MAX; ++it) {
      asm volatile("" ::: "memory");
      bool ok = true;
#pragma unroll
      for (int i = 0; i < 7; ++i) {
        const int rec = w * 28 + i * 4 + q;
        const u32x4 a = __builtin_amdgcn_raw_buffer_load_b128(rs, WS_GA + (unsigned)rec * 256 + fr * 16, 0, 16);
        ok &= (a[1] == tag) & (a[3] == tag);
        *(u32x2*)(lds + L_AS + (fr >> 1) * AS_STRIDE + rec * 16 + (fr & 1) * 8) = (u32x2){a[0], a[2]};
      }
      if (__all(ok)) break;
      __builtin_amdgcn_s_sleep(1);
    }
    STAMP(3, 1);
    WAIT_L(0);
    f32x4 a4 = {};
#pragma unroll
    for (int i = 0; i < 7; ++i) {
      const int ks = w * 7 + i;
      const bf16x8 a = *(const bf16x8*)(lds + L_AS + (fr & 7) * AS_STRIDE + ks * 64 + q * 16);
      const int r0 = ks * 32 + q * 8 + (fr >> 2);
      const int coff = (fr & 3) * 8;
      const v4i16_t g0 = __builtin_amdgcn_ds_read_tr16_b64_v4i16((LAS v4i16_t*)(lds + L_W + r0 * 32 + coff));
      const v4i16_t g1v = __builtin_amdgcn_ds_read_tr16_b64_v4i16((LAS v4i16_t*)(lds + L_W + (r0 + 4) * 32 + coff));
      const bf16x8 bg = {g0[0], g0[1], g0[2], g0[3], g1v[0], g1v[1], g1v[2], g1v[3]};
      a4 = __builtin_amdgcn_mfma_f32_16x16x32_bf16(a, bg, a4, 0, 0, 0);
    }
    if (q < 2) {
#pragma unroll
      for (int jj = 0; jj < 4; ++jj) red[w * 128 + (q * 4 + jj) * 16 + fr] = a4[jj];
    }
    WAIT_L(0);
    BAR();
    if (tid < 128) {
      float s = 0.f;
#pragma unroll
      for (int ww = 0; ww < 8; ++ww) s += red[ww * 128 + tid];
      const unsigned k2 = misc[24 + (tid >> 4)];
      const int t = 0xffff - (int)(k2 & 0xffffu);
      const float sig = __uint_as_float(misc[40 + (tid >> 4)]);
      const float upd = rbf(rbf(s) * sig);
      const float2 hh = ((const float2*)(lds + L_HH))[tid];
      out[(size_t)t * D + db * 16 + (tid & 15)] = (u16)f2bf(hh.x + rbf(hh.y + upd));
    }
    STAMP(3, 2);
  }
  STAMP_FLUSH;
  if (b == 0) { while (__builtin_amdgcn_s_memrealtime() - pad_t0 < PAD * 100ull) __builtin_amdgcn_s_sleep(1); }
  asm volatile(".Lfused_code_hi:");
}

extern "C" void kernel_launch(void* const* d_in, const int* in_sizes, int n_in, void* d_out, int out_size, void* d_ws,
                              size_t ws_size, hipStream_t stream) {
  fused<<<256, 512, 0, stream>>>((const u16*)d_in[0], (const u16*)d_in[1], (const u16*)d_in[2], (const u16*)d_in[3], (const u16*)d_in[4],
                                 (const u16*)d_in[6], (const u16*)d_in[7], (const u16*)d_in[8], (const u16*)d_in[9], (u16*)d_out, (char*)d_ws);
}
```

```cpp
#define WARM 30
#include <hip/hip_runtime.h>
#include <hip/hip_bf16.h>
#include <stdint.h>
#include <utility>

typedef unsigned short u16;
typedef __attribute__((ext_vector_type(8))) short bf16x8;
typedef __attribute__((ext_vector_type(4))) short v4i16_t;
typedef __attribute__((ext_vector_type(4))) float f32x4;
typedef __attribute__((ext_vector_type(4))) unsigned u32x4;
typedef __attribute__((ext_vector_type(2))) unsigned u32x2;
#define GAS __attribute__((address_space(1)))
#define LAS __attribute__((address_space(3)))
typedef GAS unsigned gu32;
typedef GAS unsigned long long gu64;
#define RLX_AGENT __ATOMIC_RELAXED, __HIP_MEMORY_SCOPE_AGENT

constexpr int S = 2048, D = 512, F = 1792;

__device__ __forceinline__ float bf2f(unsigned v) { return __uint_as_float(v << 16); }
__device__ __forceinline__ float bflo(unsigned v) { return __uint_as_float(v << 16); }
__device__ __forceinline__ float bfhi(unsigned v) { return __uint_as_float(v & 0xffff0000u); }
__device__ __forceinline__ unsigned f2bf(float f) { __bf16 b = (__bf16)f; return (unsigned)__builtin_bit_cast(u16, b); }
__device__ __forceinline__ float rbf(float f) { return bf2f(f2bf(f)); }
__device__ __forceinline__ unsigned pack2(float lo, float hi) { return f2bf(lo) | (f2bf(hi) << 16); }

#define WAIT_V(n) asm volatile("s_waitcnt vmcnt(%0)" ::"n"(n) : "memory")
#define WAIT_L(n) asm volatile("s_waitcnt lgkmcnt(%0)" ::"n"(n) : "memory")
#define BAR() __builtin_amdgcn_s_barrier()
#define GLDS16(src, dst, aux) __builtin_amdgcn_global_load_lds((const unsigned*)(src), (unsigned*)(dst), 16, 0, aux)
#define TOUCH(reg, ptr) asm volatile("global_load_dword %0, %1, off" : "+v"(reg) : "v"(ptr) : "memory")
#define TOUCH_SC1(reg, ptr) asm volatile("global_load_dword %0, %1, off sc1" : "+v"(reg) : "v"(ptr) : "memory")
#define TOUCH_NT(reg, ptr) asm volatile("global_load_dword %0, %1, off nt" : "+v"(reg) : "v"(ptr) : "memory")
#define KEEP(reg) asm volatile("" ::"v"(reg))

template <int CTRL, int RM> __device__ __forceinline__ float dpp_f(float v) {
  return __builtin_bit_cast(float, __builtin_amdgcn_update_dpp(0, __builtin_bit_cast(int, v), CTRL, RM, 0xf, true));
}
__device__ __forceinline__ float wave_sum(float v) {
  v += dpp_f<0x111, 0xf>(v); v += dpp_f<0x112, 0xf>(v); v += dpp_f<0x114, 0xf>(v); v += dpp_f<0x118, 0xf>(v);
  v += dpp_f<0x142, 0xa>(v); v += dpp_f<0x143, 0xc>(v);
  return __builtin_bit_cast(float, __builtin_amdgcn_readlane(__builtin_bit_cast(int, v), 63));
}
template <int CTRL, int RM> __device__ __forceinline__ unsigned dpp_u(unsigned v) {
  return (unsigned)__builtin_amdgcn_update_dpp(0, (int)v, CTRL, RM, 0xf, true);
}
__device__ __forceinline__ unsigned umax(unsigned a, unsigned b) { return a > b ? a : b; }
__device__ __forceinline__ unsigned wave_max_u32(unsigned v) {
  v = umax(v, dpp_u<0x111, 0xf>(v)); v = umax(v, dpp_u<0x112, 0xf>(v)); v = umax(v, dpp_u<0x114, 0xf>(v)); v = umax(v, dpp_u<0x118, 0xf>(v));
  v = umax(v, dpp_u<0x142, 0xa>(v)); v = umax(v, dpp_u<0x143, 0xc>(v));
  return (unsigned)__builtin_amdgcn_readlane((int)v, 63);
}

#define STAMP_DECL
#define STAMPW(k, i, wv)
#define STAMP_FLUSH
#define STAMP(k, i) STAMPW(k, i, 0)

constexpr unsigned WS_ABT = 0;
constexpr unsigned WS_Y = WS_ABT + 1024u * 2048 * 2;
constexpr unsigned WS_F1 = WS_Y + 4u * 2048 * 512 * 2;
constexpr unsigned WS_F2 = WS_F1 + 4096;
constexpr unsigned WS_GR = WS_F2 + 2048;
constexpr unsigned WS_GA = WS_GR + 1024 * 8;
constexpr unsigned WS_EP = WS_GA + 224 * 256;
constexpr unsigned WS_SEL = WS_EP + 256;
constexpr unsigned WS_HN = WS_SEL + 2048;
constexpr unsigned WS_END = WS_HN + 2048u * 2048;

constexpr int SPIN_MAX = 1 << 18;
#define AUX1 0
#define CPF 1
#define FMODE 1
#define HNPUB 2
#define WPOS 0
#define COOP 1
#define TLBW 0
#define TS 0
#define QMODE 0
#define NPLAIN 8
#define T1MODE 1
#define FFNSTEP 2

constexpr int L_QR = 81920;
constexpr int L_RST = 65536;
constexpr int L_TAG = 66560;
constexpr int L_W = 16384;
constexpr int L_T2 = 131072;
constexpr int XS_STRIDE = 1024 + 16;
constexpr int L_XS = 81920;
constexpr int L_RED = L_XS + 8 * XS_STRIDE;
constexpr int L_MISC = L_RED + 8 * 128 * 4;
constexpr int L_HH = L_MISC + 1024;
constexpr int AS_STRIDE = F * 2 + 16;
constexpr int L_AS = L_HH + 1024;
static_assert(L_AS + 8 * AS_STRIDE <= L_T2, "lds");

struct Ring2 {
  static constexpr int p(int T) { return (T % 5) * 16384; }
  static constexpr int q(int T) { return L_QR + ((T + 3) % 5) * 16384; }
};
constexpr int koff(int T) { return T < 8 ? T * 128 : (T - 8) * 128 + 64; }
constexpr int x1slot(int s) { return L_QR + (s % 5) * 16384; }

template <int BP, int BQ, int NKT, class RG, bool STATS>
struct Cons {
  static constexpr int TP = BP / 32, TQ = BQ / 32;
  struct Ctx { const char* lds; int poff[TP][2]; int qoff[TQ][2]; };
  struct Fr { bf16x8 pf[2][TP], qf[2][TQ]; };
  struct St { f32x4 s1[2], s2[2], sw[TQ]; };
  template <int T> static __device__ __forceinline__ void readf(const Ctx& c, Fr& f) {
#pragma unroll
    for (int ks = 0; ks < 2; ++ks) {
#pragma unroll
      for (int m = 0; m < TP; ++m) f.pf[ks][m] = *(const bf16x8*)(c.lds + RG::p(T) + c.poff[m][ks]);
#pragma unroll
      for (int n = 0; n < TQ; ++n) f.qf[ks][n] = *(const bf16x8*)(c.lds + RG::q(T) + c.qoff[n][ks]);
    }
  }
  template <int T> static __device__ __forceinline__ void step(const Ctx& c, f32x4 (&acc)[TP][TQ], Fr& cur, Fr& nxt, St& st) {
    if constexpr (T + 1 < NKT) {
      WAIT_L(0);
      BAR();
      __builtin_amdgcn_sched_barrier(0);
      readf<T + 1>(c, nxt);
    }
    if constexpr (T + 1 == NKT) return;
#pragma unroll
    for (int ks = 0; ks < 2; ++ks) {
#pragma unroll
      for (int m = 0; m < TP; ++m)
#pragma unroll
        for (int n = 0; n < TQ; ++n) acc[m][n] = __builtin_amdgcn_mfma_f32_16x16x32_bf16(cur.pf[ks][m], cur.qf[ks][n], acc[m][n], 0, 0, 0);
      if constexpr (STATS) {
        const bf16x8 ones = {0x3f80, 0x3f80, 0x3f80, 0x3f80, 0x3f80, 0x3f80, 0x3f80, 0x3f80};
#pragma unroll
        for (int m = 0; m < 2; ++m) {
          st.s1[m] = __builtin_amdgcn_mfma_f32_16x16x32_bf16(cur.pf[ks][m], ones, st.s1[m], 0, 0, 0);
          st.s2[m] = __builtin_amdgcn_mfma_f32_16x16x32_bf16(cur.pf[ks][m], cur.pf[ks][m], st.s2[m], 0, 0, 0);
        }
#pragma unroll
        for (int n = 0; n < TQ; ++n) st.sw[n] = __builtin_amdgcn_mfma_f32_16x16x32_bf16(ones, cur.qf[ks][n], st.sw[n], 0, 0, 0);
      }
    }
    if constexpr (T + 1 < NKT) {
#pragma unroll
      for (int i = 0; i < 2 * (TP + TQ); ++i) { __builtin_amdgcn_sched_group_barrier(0x100, 1, 0); __builtin_amdgcn_sched_group_barrier(0x8, 1, 0); }
    }
    __builtin_amdgcn_sched_barrier(0);
  }
  template <int... Ts>
  static __device__ __forceinline__ void steps(const Ctx& c, f32x4 (&acc)[TP][TQ], Fr& f0, Fr& f1, St& st, std::integer_sequence<int, Ts...>) {
    ((Ts & 1 ? step<Ts>(c, acc, f1, f0, st) : step<Ts>(c, acc, f0, f1, st)), ...);
  }
  static __device__ __forceinline__ void run(const char* lds, int wp, int wq, int rot, int l, f32x4 (&acc)[TP][TQ], St& st, Fr& f1) {
    Ctx c; c.lds = lds;
    const int fr = l & 15, q = l >> 4;
#pragma unroll
    for (int ks = 0; ks < 2; ++ks) {
#pragma unroll
      for (int m = 0; m < TP; ++m) { const int row = wp * (BP / 2) + ((m + rot) & (TP - 1)) * 16 + fr; c.poff[m][ks] = row * 128 + (((ks * 4 + q) ^ ((row >> 1) & 7)) * 16); }
#pragma unroll
      for (int n = 0; n < TQ; ++n) { const int row = wq * (BQ / 2) + n * 16 + fr; c.qoff[n][ks] = row * 128 + (((ks * 4 + q) ^ ((row >> 1) & 7)) * 16); }
    }
    Fr f0;
    BAR();
    readf<0>(c, f0);
    steps(c, acc, f0, f1, st, std::make_integer_sequence<int, NKT>{});
  }
};

struct L1Ctx { const u16* xsrc[4]; const u16* wsrc[4]; char* lds; int w; };
template <int s> __device__ __forceinline__ void l1_issue(const L1Ctx& c) {
#pragma unroll
  for (int h = 0; h < 2; ++h) {
#pragma unroll
    for (int i = 0; i < 4; ++i) GLDS16(c.xsrc[i] + (size_t)(s >> 2) * 64 * D + (s & 3) * 128 + h * 64, c.lds + x1slot(s) + h * 8192 + (c.w * 4 + i) * 1024, AUX1);
    if constexpr (s < 4) {
#pragma unroll
      for (int i = 0; i < 4; ++i) GLDS16(c.wsrc[i] + s * 128 + h * 64, c.lds + (2 * s + h) * 8192 + (c.w * 4 + i) * 1024, AUX1);
    }
  }
}
struct Fr1 { bf16x8 pf[4][2], qf[4][2]; };
struct C1Ctx { const char* lds; int poff[2][2]; int qoff[2][2]; };
template <int s> __device__ __forceinline__ void c1_read(const C1Ctx& c, Fr1& f) {
#pragma unroll
  for (int ks = 0; ks < 4; ++ks) {
#pragma unroll
    for (int m = 0; m < 2; ++m) f.pf[ks][m] = *(const bf16x8*)(c.lds + x1slot(s) + (ks >> 1) * 8192 + c.poff[m][ks & 1]);
#pragma unroll
    for (int n = 0; n < 2; ++n) f.qf[ks][n] = *(const bf16x8*)(c.lds + (2 * (s & 3) + (ks >> 1)) * 8192 + c.qoff[n][ks & 1]);
  }
}
struct Acc1 { f32x4 acc[2][2], s1, s2; };
template <int s> __device__ __forceinline__ void c1_step(const C1Ctx& c, Acc1& a, f32x4 (&sw)[2], Fr1& cur, Fr1& nxt) {
  if constexpr (s == 7) WAIT_V(0);
  WAIT_L(0);
  BAR();
  __builtin_amdgcn_sched_barrier(0);
  if constexpr (s + 1 < 8) c1_read<s + 1>(c, nxt);
  const bf16x8 ones = {0x3f80, 0x3f80, 0x3f80, 0x3f80, 0x3f80, 0x3f80, 0x3f80, 0x3f80};
#pragma unroll
  for (int ks = 0; ks < 4; ++ks) {
#pragma unroll
    for (int m = 0; m < 2; ++m)
#pragma unroll
      for (int n = 0; n < 2; ++n) a.acc[m][n] = __builtin_amdgcn_mfma_f32_16x16x32_bf16(cur.pf[ks][m], cur.qf[ks][n], a.acc[m][n], 0, 0, 0);
    a.s1 = __builtin_amdgcn_mfma_f32_16x16x32_bf16(cur.pf[ks][0], ones, a.s1, 0, 0, 0);
    a.s2 = __builtin_amdgcn_mfma_f32_16x16x32_bf16(cur.pf[ks][0], cur.pf[ks][0], a.s2, 0, 0, 0);
    if constexpr (s < 4) {
#pragma unroll
      for (int n = 0; n < 2; ++n) sw[n] = __builtin_amdgcn_mfma_f32_16x16x32_bf16(ones, cur.qf[ks][n], sw[n], 0, 0, 0);
    }
  }
  if constexpr (s + 1 < 8) {
#pragma unroll
    for (int i = 0; i < 16; ++i) { __builtin_amdgcn_sched_group_barrier(0x100, 1, 0); __builtin_amdgcn_sched_group_barrier(0x8, 1, 0); }
  }
  __builtin_amdgcn_sched_barrier(0);
}

struct L2Ctx { const u16* psrc[8]; const u16* qsrc[8]; char* lds; int w; };
template <int T> __device__ __forceinline__ void l2_issueP(const L2Ctx& c) {
#pragma unroll
  for (int i = 0; i < 8; ++i) GLDS16(c.psrc[i] + koff(T), c.lds + Ring2::p(T) + (c.w * 8 + i) * 1024, 16);
}
template <int T, int AUX> __device__ __forceinline__ void l2_issueQ(const L2Ctx& c) {
#pragma unroll
  for (int i = 0; i < 8; ++i) GLDS16(c.qsrc[i] + koff(T), c.lds + Ring2::q(T) + (c.w * 8 + i) * 1024, AUX);
}
constexpr int l2_cnt(int s) { return s < 5 ? 8 : 16; }
constexpr int l2_younger(int T) { int up = T + 4 < 15 ? T + 4 : 15; int n = 0; for (int s = T + 2; s <= up; ++s) n += l2_cnt(s); return n; }
template <int T, int AUX> __device__ __forceinline__ void l2_step(const L2Ctx& c) {
  WAIT_V(l2_younger(T));
  BAR();
  if constexpr (T + 5 < 16) { l2_issueP<T + 5>(c); l2_issueQ<T + 5, AUX>(c); }
}
template <int AUX, int... Ts> __device__ __forceinline__ void l2_steps(const L2Ctx& c, std::integer_sequence<int, Ts...>) { (l2_step<Ts, AUX>(c), ...); }

__device__ __forceinline__ u32x4 widen16(u32x2 a, u32x2 c) {
  const auto s0 = __builtin_amdgcn_permlane16_swap(a[0], c[0], false, false);
  const auto s1 = __builtin_amdgcn_permlane16_swap(a[1], c[1], false, false);
  return (u32x4){s0[0], s1[0], s0[1], s1[1]};
}

__device__ __forceinline__ void glds16_asm(const void* gsrc, unsigned lds_dst) {
  unsigned keep;
  asm volatile("s_mov_b32 %0, m0\n\ts_mov_b32 m0, %2\n\ts_nop 0\n\tglobal_load_lds_dwordx4 %1, off\n\ts_mov_b32 m0, %0" : "=&s"(keep) : "v"(gsrc), "s"(lds_dst) : "memory");
}

__device__ __forceinline__ void poll_words(const unsigned* p, unsigned tag) {
  for (int it = 0; it < SPIN_MAX; ++it) {
    const unsigned v = __hip_atomic_load((gu32*)p, RLX_AGENT);
    if (__all(v == tag)) break;
    __builtin_amdgcn_s_sleep(1);
  }
  __builtin_amdgcn_fence(__ATOMIC_ACQUIRE, "wavefront");
}

__device__ __forceinline__ unsigned add_pk(unsigned a, unsigned b) { return pack2(bflo(a) + bflo(b), bfhi(a) + bfhi(b)); }
__device__ __forceinline__ unsigned sub_pk(unsigned a, unsigned b) { return pack2(bflo(a) - bflo(b), bfhi(a) - bfhi(b)); }
template <class R>
__device__ __forceinline__ void load_h(const u32x4 xv, R rs, int t, int l, float (&h)[8]) {
  const unsigned o = (unsigned)(t * D + l * 8) * 2;
  const u32x4 y0 = __builtin_amdgcn_raw_buffer_load_b128(rs, WS_Y + o, 0, 16);
  const u32x4 y1 = __builtin_amdgcn_raw_buffer_load_b128(rs, WS_Y + 1u * S * D * 2 + o, 0, 16);
  const u32x4 y2 = __builtin_amdgcn_raw_buffer_load_b128(rs, WS_Y + 2u * S * D * 2 + o, 0, 16);
  const u32x4 y3 = __builtin_amdgcn_raw_buffer_load_b128(rs, WS_Y + 3u * S * D * 2 + o, 0, 16);
#pragma unroll
  for (int e = 0; e < 4; ++e) {
    const unsigned yv = pack2((bflo(y0[e]) + bflo(y1[e])) - (bflo(y2[e]) + bflo(y3[e])), (bfhi(y0[e]) + bfhi(y1[e])) - (bfhi(y2[e]) + bfhi(y3[e])));
    const unsigned hv = add_pk(xv[e], yv);
    h[2 * e] = bflo(hv); h[2 * e + 1] = bfhi(hv);
  }
}
__device__ __forceinline__ u32x4 layernorm8(const float (&h)[8]) {
  const float pv = __builtin_bit_cast(float, __builtin_amdgcn_readfirstlane(__builtin_bit_cast(int, h[0])));
  float s = 0.f, s2 = 0.f, v = 0.f, v2 = 0.f;
#pragma unroll
  for (int e = 0; e < 4; ++e) { const float a = h[2 * e] - pv, c = h[2 * e + 1] - pv; s += a; s2 += c; v += a * a; v2 += c * c; }
  const float md = wave_sum(s + s2) * (1.0f / D);
  v = fmaxf(wave_sum(v + v2) - md * md * (float)D, 0.f);
  const float mean = pv + md;
  const float mb = rbf(mean);
  const float sd = rbf(sqrtf(rbf(rbf(v * (1.0f / D)) + 1e-5f)));
  const float inv = 1.0f / sd;
  u32x4 o;
#pragma unroll
  for (int e = 0; e < 4; ++e) {
    const unsigned d = pack2(h[2 * e] - mb, h[2 * e + 1] - mb);
    const float a = bflo(d), c = bfhi(d);
    float qa = a * inv, qc = c * inv;
    qa = fmaf(fmaf(-qa, sd, a), inv, qa); qc = fmaf(fmaf(-qc, sd, c), inv, qc);
    o[e] = pack2(qa, qc);
  }
  return o;
}

__global__ __launch_bounds__(512) void fused(const u16* __restrict__ x, const u16* __restrict__ ws_re, const u16* __restrict__ ws_im,
                                             const u16* __restrict__ wd_re, const u16* __restrict__ wd_im, const u16* __restrict__ w_r,
                                             const u16* __restrict__ w_gate, const u16* __restrict__ w_up, const u16* __restrict__ w_down,
                                             u16* __restrict__ out, char* __restrict__ ws) {
  __shared__ __attribute__((aligned(1024))) char lds[163840];
  const int b = blockIdx.x, xcd = b & 7, j = b >> 3;
  const int cc = xcd >> 2, et = j & 3, tt = (xcd & 3) * 4 + ((j >> 2) & 3), kh = j >> 4;
  const int g1 = (cc * 4 + et) * 2 + kh;
  const int e1 = cc * 8 + et * 2 + (tt & 1), s1 = kh * 8 + (tt >> 1);
  const int tid = threadIdx.x, w = __builtin_amdgcn_readfirstlane(tid >> 6), l = tid & 63, fr = l & 15, q = l >> 4;
  const u16* wsb = cc ? ws_im : ws_re;
  const u16* wdb = cc ? wd_im : wd_re;
  const bool down = j >= 28;
  const int fb = xcd * 28 + j, db = xcd * 4 + (j - 28);
  const auto rs = __builtin_amdgcn_make_buffer_rsrc(ws, 0, WS_END, 0x00020000);
  unsigned tag;
  STAMP_DECL;
  STAMP(0, 0);
  unsigned long long code_lo; unsigned code_len;
  asm volatile("s_getpc_b64 %0\n.Lfused_code_lo:\n\ts_mov_b32 %1, .Lfused_code_hi-.Lfused_code_lo" : "=s"(code_lo), "=s"(code_len));

  const unsigned lbase = (unsigned)(uintptr_t)(LAS char*)lds;
  {
    const int row = w * 8 + (l >> 3);
    const size_t o = (size_t)row * D + (((l & 7) ^ ((row >> 1) & 7)) * 8);
    const u16* xp = x + (size_t)s1 * 128 * D + o;
    const u16* wp_ = wdb + (size_t)(e1 & 7) * 64 * D + o;
#pragma unroll
    for (int k = 0; k < 4; ++k) {
      glds16_asm(xp + k * 64, lbase + x1slot(k >> 1) + (k & 1) * 8192 + w * 1024);
      glds16_asm(wp_ + k * 64, lbase + k * 8192 + w * 1024);
    }
  }
  if (w < 4) {
    const int wp = w >> 1, wq = w & 1;
    unsigned r0 = 0;
    {
      const int row = tid >> 3, ln = tid & 7;
      const int xs_tile = ((j >> 3) & 1) * 8 + (xcd & 3) * 2 + ((j >> 2) & 1);
      const u16* p = j < 16 ? x + (size_t)(xs_tile * 128 + (j & 3) * 32 + row) * D + ln * 64 : wdb + (size_t)((j - 16) * 32 + row) * D + ln * 64;
      if (T1MODE == 1) TOUCH(r0, p);
      if (T1MODE == 2) TOUCH_NT(r0, p);
    }
    {
      C1Ctx c; c.lds = lds;
#pragma unroll
      for (int ks = 0; ks < 2; ++ks) {
#pragma unroll
        for (int m = 0; m < 2; ++m) { const int row = wp * 32 + (m ^ wq) * 16 + fr; c.poff[m][ks] = row * 128 + (((ks * 4 + q) ^ ((row >> 1) & 7)) * 16); }
#pragma unroll
        for (int n = 0; n < 2; ++n) { const int row = wq * 32 + n * 16 + fr; c.qoff[n][ks] = row * 128 + (((ks * 4 + q) ^ ((row >> 1) & 7)) * 16); }
      }
      f32x4 sw[2] = {};
      Fr1 f0, f1;
      auto epilogue = [&](const Acc1& a, int pass) {
        float2* rst = (float2*)(lds + L_RST) + pass * 64;
        if ((fr >> 2) == q) {
          const int jj = fr & 3;
          const float s1v = jj == 0 ? a.s1[0] : jj == 1 ? a.s1[1] : jj == 2 ? a.s1[2] : a.s1[3];
          const float s2v = jj == 0 ? a.s2[0] : jj == 1 ? a.s2[1] : jj == 2 ? a.s2[2] : a.s2[3];
          const float mean = s1v * (1.0f / D);
          const float var = fmaxf(s2v * (1.0f / D) - mean * mean, 0.f);
          const float sd = rbf(sqrtf(rbf(rbf(var) + 1e-5f)));
          rst[wp * 32 + wq * 16 + fr] = make_float2(rbf(mean), 1.0f / sd);
        }
        WAIT_L(0);
        BAR();
        u32x2 pk[2][2];
#pragma unroll
        for (int m = 0; m < 2; ++m) {
          const int p0 = wp * 32 + (m ^ wq) * 16 + q * 4;
          const f32x4 sa = *(const f32x4*)(rst + p0), sb = *(const f32x4*)(rst + p0 + 2);
#pragma unroll
          for (int n = 0; n < 2; ++n) {
            const float swv = sw[n][0];
            const float v0 = (a.acc[m][n][0] - sa[0] * swv) * sa[1], v1 = (a.acc[m][n][1] - sa[2] * swv) * sa[3];
            const float v2 = (a.acc[m][n][2] - sb[0] * swv) * sb[1], v3 = (a.acc[m][n][3] - sb[2] * swv) * sb[3];
            pk[m][n] = (u32x2){pack2(v0, v1), pack2(v2, v3)};
          }
        }
        const int scol = s1 * 128 + pass * 64 + wp * 32 + ((q & 1) ^ wq) * 16 + (q >> 1) * 8;
#pragma unroll
        for (int n = 0; n < 2; ++n)
          __builtin_amdgcn_raw_buffer_store_b128(widen16(pk[0][n], pk[1][n]), rs, WS_ABT + (unsigned)((e1 * 64 + wq * 32 + n * 16 + fr) * S + scol) * 2, 0, 16);
      };
      {
        bf16x8 wa, wb;
#pragma unroll
        for (int i = 0; i < 8; ++i) { wa[i] = (short)(0x3f80 ^ ((tid * 2654435761u >> (i + 3)) & 0x807f)); wb[i] = (short)(0x3f00 ^ ((tid * 40503u >> i) & 0x807f)); }
        f32x4 d0 = {}, d1 = {};
#pragma unroll 1
        for (int i = 0; i < WARM; ++i) {
          d0 = __builtin_amdgcn_mfma_f32_16x16x32_bf16(wa, wb, d0, 0, 0, 0);
          d1 = __builtin_amdgcn_mfma_f32_16x16x32_bf16(wb, wa, d1, 0, 0, 0);
        }
        asm volatile("" ::"v"(d0), "v"(d1));
      }
      if (COOP) WAIT_V(5);
      BAR();
      c1_read<0>(c, f0);
      if (COOP) WAIT_V(1);
      {
        Acc1 a = {};
        c1_step<0>(c, a, sw, f0, f1); c1_step<1>(c, a, sw, f1, f0); c1_step<2>(c, a, sw, f0, f1); c1_step<3>(c, a, sw, f1, f0);
        STAMP(0, 2);
        epilogue(a, 0);
        STAMP(0, 3);
      }
      {
        Acc1 a = {};
        c1_step<4>(c, a, sw, f0, f1); c1_step<5>(c, a, sw, f1, f0); c1_step<6>(c, a, sw, f0, f1); c1_step<7>(c, a, sw, f1, f0);
        STAMP(0, 4);
        epilogue(a, 1);
        STAMP(0, 5);
      }
      tag = __builtin_amdgcn_readfirstlane(*(const unsigned*)(lds + L_TAG));
      WAIT_V(0);
      KEEP(r0);
      STAMP(0, 6);
      BAR();
    }
    using C2 = Cons<128, 128, 16, Ring2, false>;
    f32x4 acc[4][4] = {};
    C2::St st;
    C2::Fr fl;
    C2::run(lds, wp, wq, 0, l, acc, st, fl);
    STAMP(1, 2);
    {
      const unsigned yo = WS_Y + (unsigned)(cc * 2 + kh) * S * D * 2;
      const int chunk = (q >> 1) + 2 * (q & 1);
#pragma unroll
      for (int n = 0; n < 4; ++n) {
#pragma unroll
        for (int ks = 0; ks < 2; ++ks)
#pragma unroll
          for (int m = 0; m < 4; ++m) acc[m][n] = __builtin_amdgcn_mfma_f32_16x16x32_bf16(fl.pf[ks][m], fl.qf[ks][n], acc[m][n], 0, 0, 0);
#pragma unroll
        for (int mp = 0; mp < 2; ++mp) {
          const u32x2 a = {pack2(acc[2 * mp][n][0], acc[2 * mp][n][1]), pack2(acc[2 * mp][n][2], acc[2 * mp][n][3])};
          const u32x2 c = {pack2(acc[2 * mp + 1][n][0], acc[2 * mp + 1][n][1]), pack2(acc[2 * mp + 1][n][2], acc[2 * mp + 1][n][3])};
          const int row = wq * 64 + n * 16 + fr;
          __builtin_amdgcn_raw_buffer_store_b128(widen16(a, c), rs, yo + (unsigned)((tt * 128 + row) * D + et * 128 + wp * 64 + mp * 32 + chunk * 8) * 2, 0, 16);
        }
      }
    }
    STAMP(1, 3);
    WAIT_V(0);
    STAMP(1, 6);
  } else if (w < 6) {
    const int lw = w - 4;
    const int r8 = l >> 3, ch = l & 7;
    {
      L1Ctx c; c.lds = lds; c.w = lw;
      const u16* P = x + (size_t)s1 * 128 * D;
      const u16* Q = wdb + (size_t)(e1 & 7) * 64 * D;
#pragma unroll
      for (int i = 0; i < 4; ++i) {
        const int row = (lw * 4 + i) * 8 + r8;
        const size_t o = (size_t)row * D + ((ch ^ ((row >> 1) & 7)) * 8);
        c.xsrc[i] = P + o; c.wsrc[i] = Q + o;
      }
      l1_issue<2>(c);
      WAIT_V(20); STAMPW(0, 1, 4); BAR();
      WAIT_V(16); BAR();
      l1_issue<3>(c);
      WAIT_V(16); BAR();
      l1_issue<4>(c); l1_issue<5>(c);
      WAIT_V(16); BAR();
      l1_issue<6>(c); l1_issue<7>(c);
      WAIT_V(24); BAR();
      BAR();
      WAIT_V(16); BAR();
      WAIT_V(8); BAR();
      WAIT_V(0); BAR();
    }
    L2Ctx c2;
    c2.lds = lds; c2.w = lw;
    {
      const u16* P = (const u16*)(ws + WS_ABT) + (size_t)(cc * 512 + et * 128) * S + kh * 1024;
      const u16* Q = wsb + (size_t)tt * 128 * S + kh * 1024;
#pragma unroll
      for (int i = 0; i < 8; ++i) {
        const int row = (lw * 8 + i) * 8 + r8;
        const size_t o = (size_t)row * S + ((ch ^ ((row >> 1) & 7)) * 8);
        c2.psrc[i] = P + o; c2.qsrc[i] = Q + o;
      }
    }
    tag = __builtin_amdgcn_readfirstlane(*(const unsigned*)(lds + L_TAG));
    BAR();
    BAR();
    poll_words((const unsigned*)(ws + WS_F1) + g1 * 32 + fr, tag);
    STAMPW(1, 0, 4);
    l2_issueP<0>(c2); l2_issueP<1>(c2); l2_issueP<2>(c2);
    BAR();
    WAIT_V(16);
    STAMPW(1, 1, 4);
    BAR();
    l2_issueP<3>(c2); l2_issueP<4>(c2);
    l2_steps<0>(c2, std::make_integer_sequence<int, 15>{});
  } else if (w == 6) {
    const int r8 = l >> 3, ch = l & 7;
    const unsigned ep = __hip_atomic_load((gu32*)(ws + WS_EP), RLX_AGENT);
    const u16* Q = wsb + (size_t)tt * 128 * S + kh * 1024;
    const u16* qa = Q + (size_t)r8 * S + ((ch ^ (r8 >> 1)) * 8);
    const u16* qb = Q + (size_t)(8 + r8) * S + ((ch ^ (4 + (r8 >> 1))) * 8);
    auto issueQ = [&](int T) {
#pragma unroll
      for (int i = 0; i < 8; ++i) {
        if (QMODE == 0 || (QMODE == 1 && 2 * i < NPLAIN)) {
          GLDS16(qa + (size_t)i * 16 * S + T * 128, lds + L_QR + ((T + 3) % 5) * 16384 + (2 * i) * 1024, 0);
          GLDS16(qb + (size_t)i * 16 * S + T * 128, lds + L_QR + ((T + 3) % 5) * 16384 + (2 * i + 1) * 1024, 0);
        } else {
          GLDS16(qa + (size_t)i * 16 * S + T * 128, lds + L_QR + ((T + 3) % 5) * 16384 + (2 * i) * 1024, 2);
          GLDS16(qb + (size_t)i * 16 * S + T * 128, lds + L_QR + ((T + 3) % 5) * 16384 + (2 * i + 1) * 1024, 2);
        }
      }
    };
    if (COOP) WAIT_V(5);
    BAR();
    if (COOP) WAIT_V(1);
    BAR();
    tag = __builtin_amdgcn_readfirstlane(ep * 1664525u + 1013904223u);
    *(unsigned*)(lds + L_TAG) = tag;
    WAIT_L(0);
    BAR(); BAR(); BAR();
    BAR();
    BAR(); issueQ(0);
    BAR(); issueQ(1);
    BAR(); issueQ(2);
    BAR(); BAR();
    BAR(); issueQ(3); issueQ(4);
    WAIT_V(32);
    BAR();
    const unsigned* fB = (const unsigned*)(ws + WS_F1) + 512 + g1 * 32 + fr;
    BAR(); BAR();
    const unsigned vB = __hip_atomic_load((gu32*)fB, RLX_AGENT);
    WAIT_V(17);
    BAR();
    if (!__all(vB == tag)) poll_words(fB, tag);
    WAIT_V(0);
    BAR();
#pragma unroll
    for (int T = 4; T < 15; ++T) BAR();
  } else {
    unsigned r[4] = {0, 0, 0, 0};
    constexpr int LPI = 64 / (2 * NPLAIN) > 0 ? 64 / (2 * NPLAIN) : 1;
    const int pr = j * NPLAIN + (l % NPLAIN);
    const int prow = (pr / (NPLAIN * 8)) * 128 + (pr % (NPLAIN * 8));
    const u16* wb = wsb + (size_t)((xcd & 3) * 512 + prow) * S + ((l / NPLAIN) & 1) * 1024 + (l / (2 * NPLAIN)) * 128;
#define TW(i) TOUCH(r[(i) & 1], wb + ((i) % (8 / LPI)) * (LPI * 128) + ((i) / (8 / LPI)) * 64)
    auto ffn_ptr = [&](int i) -> const char* {
      const int li = j * 192 + i * 64 + l;
      if (li < 4096) { const int mrow = (li & 2047) >> 2, c4 = li & 3; return (const char*)((li < 2048 ? w_gate : w_up) + (size_t)mrow * F + xcd * 224 + c4 * 64); }
      const int rr = li - 4096;
      return (const char*)(w_down + (size_t)(rr < F ? rr : 0) * D + xcd * 64);
    };
    if (TLBW) {
      TOUCH_SC1(r[3], ws + (size_t)l * 65536);
      TOUCH_SC1(r[3], ws + WS_Y + (size_t)l * 65536);
      TOUCH_SC1(r[3], ws + WS_Y + (size_t)(64 + l) * 65536);
      TOUCH_SC1(r[3], (l < 32 ? (const char*)out + (size_t)l * 65536 : l < 34 ? (const char*)ws + WS_F1 + (size_t)(l - 32) * 65536
                       : (const char*)wsb + (size_t)(xcd & 3) * 2097152 + (size_t)(l - 34) * 65536));
    }
    if (CPF) {
      const unsigned off = (unsigned)(j * 16 + fr) * 128u;
      const char* cp = (const char*)code_lo + (off < code_len ? off : 0u);
      if (l < 16) TOUCH(r[3], cp);
    }
    const char* fp0 = ffn_ptr(0); const char* fp1 = ffn_ptr(1); const char* fp2 = ffn_ptr(2);
    const u16* wb2 = wsb + (size_t)((xcd & 3) * 512 + j * 16 + fr) * S + ((l >> 4) & 1) * 1024 + (l >> 5) * 128;
#define TF(i) TOUCH(r[(i) & 1], wb2 + ((i) & 3) * 256 + ((i) >> 2) * 64)
#define TFF(p) TOUCH(r[2], p)
    if (COOP) WAIT_V(4 + CPF);
    BAR();
    if (COOP) WAIT_V(CPF);
    BAR(); BAR(); TFF(fp0);
    tag = __builtin_amdgcn_readfirstlane(*(const unsigned*)(lds + L_TAG));
    BAR(); TFF(fp1); BAR(); TFF(fp2); BAR();
    BAR(); BAR(); BAR();
    BAR();
    if (l == 0) __hip_atomic_store((gu32*)(ws + WS_F1) + g1 * 32 + tt, tag, RLX_AGENT);
    BAR();
    BAR();
    if (l == 0) __hip_atomic_store((gu32*)(ws + WS_F1) + 512 + g1 * 32 + tt, tag, RLX_AGENT);
#pragma unroll
    for (int T = 0; T < 16; ++T) {
      BAR();
    }
    WAIT_V(0);
    KEEP(r[0]); KEEP(r[1]); KEEP(r[2]); KEEP(r[3]);
  }
  const int t_own = tt * 128 + g1 * 8 + w;
  unsigned rf = 0;
  const u32x4 wr = *(const u32x4*)(w_r + l * 8);
  const u32x4 x_own = *(const u32x4*)(x + (size_t)t_own * D + l * 8);
  {
    BAR();
    if (tid == 0) __hip_atomic_store((gu32*)(ws + WS_F2) + tt * 32 + g1, tag, RLX_AGENT);
    STAMP(1, 4);
    if (w == 0) poll_words((const unsigned*)(ws + WS_F2) + tt * 32 + fr, tag);
    if (FMODE == 0) {
    if (w >= 5) {
      const int li = j * 192 + (w - 5) * 64 + l;
      const char* p2;
      if (li < 4096) { const int mrow = (li & 2047) >> 2, c4 = li & 3; p2 = (const char*)((li < 2048 ? w_gate : w_up) + (size_t)mrow * F + xcd * 224 + c4 * 64); }
      else { const int rr = li - 4096; p2 = (const char*)(w_down + (size_t)(rr < F ? rr : 0) * D + xcd * 64); }
      TOUCH(rf, p2);
    }
    }
    BAR();
  }
  STAMP(2, 0);
  unsigned* misc = (unsigned*)(lds + L_MISC);
  u32x4 own;
  {
    float h[8];
    load_h(x_own, rs, t_own, l, h);
    STAMP(2, 7);
    const u32x4 hn = layernorm8(h);
    float r = 0.f, r2 = 0.f;
#pragma unroll
    for (int e = 0; e < 4; ++e) { r += bflo(hn[e]) * bflo(wr[e]); r2 += bfhi(hn[e]) * bfhi(wr[e]); }
    r = wave_sum(r + r2);
    const unsigned rb = f2bf(r);
    const unsigned ord = (rb & 0x8000u) ? (~rb & 0xffffu) : (rb | 0x8000u);
    if (l == 0) { ((u16*)misc)[w] = (u16)ord; if (w == 0) misc[8] = 0; }
    WAIT_L(0);
    BAR();
    if (tid < 4) __hip_atomic_store((gu64*)(ws + WS_GR) + (tt * 16 + g1) * 4 + tid, ((unsigned long long)tag << 32) | misc[tid], RLX_AGENT);
    bool pub = HNPUB == 1;
    if (HNPUB == 2) {
      const unsigned ov = ((const u16*)misc)[l & 7];
      pub = !__any(ov > ord || (ov == ord && (l & 7) < w));
    }
    if (pub) {
      __builtin_amdgcn_raw_buffer_store_b128((u32x4){hn[0], tag, hn[1], tag}, rs, WS_HN + (unsigned)t_own * 2048 + l * 32, 0, 16);
      __builtin_amdgcn_raw_buffer_store_b128((u32x4){hn[2], tag, hn[3], tag}, rs, WS_HN + (unsigned)t_own * 2048 + l * 32 + 16, 0, 16);
    }
#pragma unroll
    for (int e = 0; e < 4; ++e) own[e] = pack2(h[2 * e] + bflo(hn[e]), h[2 * e + 1] + bfhi(hn[e]));
  }
  KEEP(rf);
  {
    const u16* wsrc = down ? w_down + (size_t)(l >> 1) * D + db * 16 + (l & 1) * 8 : ((l & 1) ? w_up : w_gate) + (size_t)(l >> 1) * F + fb * 8;
    if (down) {
#pragma unroll
      for (int i = 0; i < 7; ++i) glds16_asm(wsrc + (size_t)(w * 7 + i) * 32 * D, lbase + L_W + (w * 7 + i) * 1024);
    } else {
#pragma unroll
      for (int i = 0; i < 2; ++i) glds16_asm(wsrc + (size_t)(w * 2 + i) * 32 * F, lbase + L_W + (w * 2 + i) * 1024);
    }
  }
  STAMP(2, 1);
  unsigned key;
  bool sel_own;
  bool fast = HNPUB == 1;
  if (!down) {
  unsigned kk[4];
  {
    u32x4 a;
    for (int it = 0; it < SPIN_MAX; ++it) {
      asm volatile("" ::: "memory");
      a = __builtin_amdgcn_raw_buffer_load_b128(rs, WS_GR + (unsigned)tid * 16, 0, 16);
      if (__all(a[1] == tag && a[3] == tag)) break;
      __builtin_amdgcn_s_sleep(1);
    }
    __builtin_amdgcn_fence(__ATOMIC_ACQUIRE, "wavefront");
    const unsigned ib = 0xffffu - (unsigned)tid * 4;
    kk[0] = (a[0] << 16) | ib; kk[1] = (a[0] & 0xffff0000u) | (ib - 1); kk[2] = (a[2] << 16) | (ib - 2); kk[3] = (a[2] & 0xffff0000u) | (ib - 3);
  }
  STAMP(2, 2);
  {
    const unsigned m = wave_max_u32(umax(umax(kk[0], kk[1]), umax(kk[2], kk[3])));
    if (l == 0) misc[16 + w] = m;
  }
  WAIT_L(0);
  BAR();
  STAMP(2, 5);
  if (b == 0 && tid == 0) __hip_atomic_store((gu32*)(ws + WS_EP), tag, RLX_AGENT);
  {
    const u32x4 ma = *(const u32x4*)(misc + 16), mb2 = *(const u32x4*)(misc + 20);
    const unsigned lo = min(min(min(ma[0], ma[1]), min(ma[2], ma[3])), min(min(mb2[0], mb2[1]), min(mb2[2], mb2[3])));
    if (l < 16) misc[64 + w * 16 + l] = 0u;
    unsigned base = 0;
#pragma unroll
    for (int e = 0; e < 4; ++e) {
      const unsigned long long bm = __ballot(kk[e] >= lo);
      const unsigned pos = base + __builtin_amdgcn_mbcnt_hi((unsigned)(bm >> 32), __builtin_amdgcn_mbcnt_lo((unsigned)bm, 0u));
      if (kk[e] >= lo && pos < 16) misc[64 + w * 16 + pos] = kk[e];
      base += (unsigned)__builtin_popcountll(bm);
    }
    if (base > 16 && l == 0) misc[8] = 1u;
  }
  WAIT_L(0);
  BAR();
  STAMP(2, 6);
  {
    const unsigned ovf = __builtin_amdgcn_readfirstlane(misc[8]);
    if (ovf == 0) {
      const unsigned cv = misc[64 + w * 16 + (l >> 2)];
      int rank = 0;
#pragma unroll
      for (int i = 0; i < 8; ++i) {
        const u32x4 c4 = *(const u32x4*)(misc + 64 + (l & 3) * 32 + i * 4);
#pragma unroll
        for (int e = 0; e < 4; ++e) rank += (c4[e] > cv) ? 1 : 0;
      }
      rank += (int)dpp_u<0xB1, 0xf>((unsigned)rank);
      rank += (int)dpp_u<0x4E, 0xf>((unsigned)rank);
      if (cv != 0u && rank < 8 && (l & 3) == 0) misc[32 + rank] = cv;
      WAIT_L(0);
      BAR();
      const unsigned sv = misc[32 + (l & 7)];
      if (b < 8 && tid < 8) __hip_atomic_store((gu64*)(ws + WS_SEL) + b * 32 + tid, ((unsigned long long)tag << 32) | sv, RLX_AGENT);
      key = (unsigned)__builtin_amdgcn_readlane((int)sv, w);
      sel_own = __any((int)(sv & 0xffffu) == 0xffff - t_own);
      fast = !__any(sv > key && ((0xffffu - (sv & 0xffffu)) >> 3) == ((0xffffu - (key & 0xffffu)) >> 3));
    } else {
      unsigned mine = 0;
#pragma unroll 1
      for (int it = 0; it < 8; ++it) {
        const unsigned m = wave_max_u32(umax(umax(kk[0], kk[1]), umax(kk[2], kk[3])));
#pragma unroll
        for (int e = 0; e < 4; ++e) kk[e] = (kk[e] == m) ? 0u : kk[e];
        if (l == it) mine = m;
      }
      if (l < 8) misc[192 + w * 8 + l] = mine;
      WAIT_L(0);
      BAR();
      unsigned cv = misc[192 + l];
      key = 0; sel_own = false;
#pragma unroll 1
      for (int it = 0; it < 8; ++it) {
        const unsigned m = wave_max_u32(cv);
        if (it == w) key = m;
        sel_own |= (int)(m & 0xffffu) == 0xffff - t_own;
        cv = (cv == m) ? 0u : cv;
      }
      if (b < 8 && l == 0) __hip_atomic_store((gu64*)(ws + WS_SEL) + b * 32 + w, ((unsigned long long)tag << 32) | key, RLX_AGENT);
    }
  }
  } else {
    if (w == 0) {
      unsigned long long g;
      for (int it = 0; it < SPIN_MAX; ++it) {
        g = __hip_atomic_load((gu64*)(ws + WS_SEL) + xcd * 32 + (l & 7), RLX_AGENT);
        if (__all((unsigned)(g >> 32) == tag)) break;
        __builtin_amdgcn_s_sleep(1);
      }
      if (l < 8) misc[32 + l] = (unsigned)g;
      WAIT_L(0);
    }
    BAR();
    const unsigned sv = misc[32 + (l & 7)];
    key = (unsigned)__builtin_amdgcn_readlane((int)sv, w);
    sel_own = __any((int)(sv & 0xffffu) == 0xffff - t_own);
    fast = !__any(sv > key && ((0xffffu - (sv & 0xffffu)) >> 3) == ((0xffffu - (key & 0xffffu)) >> 3));
  }
  STAMP(2, 3);
  const int t_sel = 0xffff - (int)(key & 0xffffu);
  bf16x8 bw[16];
  if (!down && w == 0) {
    const char* bp = lds + L_W + (q * 8 + (fr >> 2)) * 32 + (fr & 3) * 8;
#pragma unroll
    for (int ks = 0; ks < 16; ++ks) {
      const v4i16_t g0 = __builtin_amdgcn_ds_read_tr16_b64_v4i16((LAS v4i16_t*)(bp + ks * 1024));
      const v4i16_t g1v = __builtin_amdgcn_ds_read_tr16_b64_v4i16((LAS v4i16_t*)(bp + ks * 1024 + 128));
      bw[ks] = (bf16x8){g0[0], g0[1], g0[2], g0[3], g1v[0], g1v[1], g1v[2], g1v[3]};
    }
  }
  {
    float h[8];
    u32x4 hn;
    if (down || !fast) {
      const u32x4 xs = *(const u32x4*)(x + (size_t)t_sel * D + l * 8);
      load_h(xs, rs, t_sel, l, h);
    }
    if (fast) {
      u32x4 a, c;
      for (int it = 0; it < SPIN_MAX; ++it) {
        asm volatile("" ::: "memory");
        a = __builtin_amdgcn_raw_buffer_load_b128(rs, WS_HN + (unsigned)t_sel * 2048 + l * 32, 0, 16);
        c = __builtin_amdgcn_raw_buffer_load_b128(rs, WS_HN + (unsigned)t_sel * 2048 + l * 32 + 16, 0, 16);
        if (__all(a[1] == tag && a[3] == tag && c[1] == tag && c[3] == tag)) break;
        __builtin_amdgcn_s_sleep(1);
      }
      hn = (u32x4){a[0], a[2], c[0], c[2]};
    } else {
      hn = layernorm8(h);
    }
    if (!down) {
      *(u32x4*)(lds + L_XS + w * XS_STRIDE + l * 16) = hn;
    } else {
      if ((l >> 1) == db) {
        float2* hh = (float2*)(lds + L_HH) + w * 16 + (l & 1) * 8;
#pragma unroll
        for (int e = 0; e < 4; ++e) { hh[2 * e] = make_float2(h[2 * e], bflo(hn[e])); hh[2 * e + 1] = make_float2(h[2 * e + 1], bfhi(hn[e])); }
      }
      if (l == 0) {
        const unsigned ord = key >> 16;
        const unsigned rb = (ord & 0x8000u) ? (ord & 0x7fffu) : (~ord & 0xffffu);
        misc[24 + w] = key;
        misc[40 + w] = __float_as_uint(rbf(1.0f / (1.0f + expf(-bf2f(rb)))));
      }
    }
    if (!sel_own) *(u32x4*)(out + (size_t)t_own * D + l * 8) = own;
  }
  STAMP(2, 4);
  float* red = (float*)(lds + L_RED);
  if (!down) {
    WAIT_L(0);
    BAR();
    if (w == 0) {
      f32x4 a4[2] = {};
      const char* ap = lds + L_XS + (fr & 7) * XS_STRIDE + q * 16;
#pragma unroll
      for (int ks = 0; ks < 16; ++ks) {
        const bf16x8 a = *(const bf16x8*)(ap + ks * 64);
        a4[ks & 1] = __builtin_amdgcn_mfma_f32_16x16x32_bf16(a, bw[ks], a4[ks & 1], 0, 0, 0);
      }
      const f32x4 sum = a4[0] + a4[1];
      const bool hi = fr >= 8;
      unsigned actb[2];
#pragma unroll
      for (int k = 0; k < 2; ++k) {
        const float send = hi ? sum[k] : sum[2 + k];
        const float recv = dpp_f<0x128, 0xf>(send);
        const float g = rbf(hi ? recv : sum[k]), u = rbf(hi ? sum[2 + k] : recv);
        const float sg = rbf(g / (1.0f + expf(-g)));
        actb[k] = f2bf(sg * u);
      }
      const unsigned n0 = dpp_u<0xB1, 0xf>(actb[0]), n1 = dpp_u<0xB1, 0xf>(actb[1]);
      const unsigned val = (fr & 1) ? (n1 | (actb[1] << 16)) : (actb[0] | (n0 << 16));
      const int tok = q * 4 + (hi ? 2 : 0) + (fr & 1);
      if (q < 2) __hip_atomic_store((gu64*)(ws + WS_GA) + fb * 32 + tok * 4 + ((fr & 7) >> 1), ((unsigned long long)tag << 32) | val, RLX_AGENT);
    }
    STAMP(3, 0);
  } else {
    for (int it = 0; it < SPIN_MAX; ++it) {
      asm volatile("" ::: "memory");
      bool ok = true;
#pragma unroll
      for (int i = 0; i < 7; ++i) {
        const int rec = w * 28 + i * 4 + q;
        const u32x4 a = __builtin_amdgcn_raw_buffer_load_b128(rs, WS_GA + (unsigned)rec * 256 + fr * 16, 0, 16);
        ok &= (a[1] == tag) & (a[3] == tag);
        *(u32x2*)(lds + L_AS + (fr >> 1) * AS_STRIDE + rec * 16 + (fr & 1) * 8) = (u32x2){a[0], a[2]};
      }
      if (__all(ok)) break;
      __builtin_amdgcn_s_sleep(1);
    }
    STAMP(3, 1);
    WAIT_L(0);
    f32x4 a4 = {};
#pragma unroll
    for (int i = 0; i < 7; ++i) {
      const int ks = w * 7 + i;
      const bf16x8 a = *(const bf16x8*)(lds + L_AS + (fr & 7) * AS_STRIDE + ks * 64 + q * 16);
      const int r0 = ks * 32 + q * 8 + (fr >> 2);
      const int coff = (fr & 3) * 8;
      const v4i16_t g0 = __builtin_amdgcn_ds_read_tr16_b64_v4i16((LAS v4i16_t*)(lds + L_W + r0 * 32 + coff));
      const v4i16_t g1v = __builtin_amdgcn_ds_read_tr16_b64_v4i16((LAS v4i16_t*)(lds + L_W + (r0 + 4) * 32 + coff));
      const bf16x8 bg = {g0[0], g0[1], g0[2], g0[3], g1v[0], g1v[1], g1v[2], g1v[3]};
      a4 = __builtin_amdgcn_mfma_f32_16x16x32_bf16(a, bg, a4, 0, 0, 0);
    }
    if (q < 2) {
#pragma unroll
      for (int jj = 0; jj < 4; ++jj) red[w * 128 + (q * 4 + jj) * 16 + fr] = a4[jj];
    }
    WAIT_L(0);
    BAR();
    if (tid < 128) {
      float s = 0.f;
#pragma unroll
      for (int ww = 0; ww < 8; ++ww) s += red[ww * 128 + tid];
      const unsigned k2 = misc[24 + (tid >> 4)];
      const int t = 0xffff - (int)(k2 & 0xffffu);
      const float sig = __uint_as_float(misc[40 + (tid >> 4)]);
      const float upd = rbf(rbf(s) * sig);
      const float2 hh = ((const float2*)(lds + L_HH))[tid];
      out[(size_t)t * D + db * 16 + (tid & 15)] = (u16)f2bf(hh.x + rbf(hh.y + upd));
    }
    STAMP(3, 2);
  }
  STAMP_FLUSH;
  asm volatile(".Lfused_code_hi:");
}

extern "C" void kernel_launch(void* const* d_in, const int* in_sizes, int n_in, void* d_out, int out_size, void* d_ws,
                              size_t ws_size, hipStream_t stream) {
  fused<<<256, 512, 0, stream>>>((const u16*)d_in[0], (const u16*)d_in[1], (const u16*)d_in[2], (const u16*)d_in[3], (const u16*)d_in[4],
                                 (const u16*)d_in[6], (const u16*)d_in[7], (const u16*)d_in[8], (const u16*)d_in[9], (u16*)d_out, (char*)d_ws);
}
```

```cpp
#define OUTAUX 2
#include <hip/hip_runtime.h>
#include <hip/hip_bf16.h>
#include <stdint.h>
#include <utility>

typedef unsigned short u16;
typedef __attribute__((ext_vector_type(8))) short bf16x8;
typedef __attribute__((ext_vector_type(4))) short v4i16_t;
typedef __attribute__((ext_vector_type(4))) float f32x4;
typedef __attribute__((ext_vector_type(4))) unsigned u32x4;
typedef __attribute__((ext_vector_type(2))) unsigned u32x2;
#define GAS __attribute__((address_space(1)))
#define LAS __attribute__((address_space(3)))
typedef GAS unsigned gu32;
typedef GAS unsigned long long gu64;
#define RLX_AGENT __ATOMIC_RELAXED, __HIP_MEMORY_SCOPE_AGENT

constexpr int S = 2048, D = 512, F = 1792;

__device__ __forceinline__ float bf2f(unsigned v) { return __uint_as_float(v << 16); }
__device__ __forceinline__ float bflo(unsigned v) { return __uint_as_float(v << 16); }
__device__ __forceinline__ float bfhi(unsigned v) { return __uint_as_float(v & 0xffff0000u); }
__device__ __forceinline__ unsigned f2bf(float f) { __bf16 b = (__bf16)f; return (unsigned)__builtin_bit_cast(u16, b); }
__device__ __forceinline__ float rbf(float f) { return bf2f(f2bf(f)); }
__device__ __forceinline__ unsigned pack2(float lo, float hi) { return f2bf(lo) | (f2bf(hi) << 16); }

#define WAIT_V(n) asm volatile("s_waitcnt vmcnt(%0)" ::"n"(n) : "memory")
#define WAIT_L(n) asm volatile("s_waitcnt lgkmcnt(%0)" ::"n"(n) : "memory")
#define BAR() __builtin_amdgcn_s_barrier()
#define GLDS16(src, dst, aux) __builtin_amdgcn_global_load_lds((const unsigned*)(src), (unsigned*)(dst), 16, 0, aux)
#define TOUCH(reg, ptr) asm volatile("global_load_dword %0, %1, off" : "+v"(reg) : "v"(ptr) : "memory")
#define TOUCH_SC1(reg, ptr) asm volatile("global_load_dword %0, %1, off sc1" : "+v"(reg) : "v"(ptr) : "memory")
#define TOUCH_NT(reg, ptr) asm volatile("global_load_dword %0, %1, off nt" : "+v"(reg) : "v"(ptr) : "memory")
#define KEEP(reg) asm volatile("" ::"v"(reg))

template <int CTRL, int RM> __device__ __forceinline__ float dpp_f(float v) {
  return __builtin_bit_cast(float, __builtin_amdgcn_update_dpp(0, __builtin_bit_cast(int, v), CTRL, RM, 0xf, true));
}
__device__ __forceinline__ float wave_sum(float v) {
  v += dpp_f<0x111, 0xf>(v); v += dpp_f<0x112, 0xf>(v); v += dpp_f<0x114, 0xf>(v); v += dpp_f<0x118, 0xf>(v);
  v += dpp_f<0x142, 0xa>(v); v += dpp_f<0x143, 0xc>(v);
  return __builtin_bit_cast(float, __builtin_amdgcn_readlane(__builtin_bit_cast(int, v), 63));
}
template <int CTRL, int RM> __device__ __forceinline__ unsigned dpp_u(unsigned v) {
  return (unsigned)__builtin_amdgcn_update_dpp(0, (int)v, CTRL, RM, 0xf, true);
}
__device__ __forceinline__ unsigned umax(unsigned a, unsigned b) { return a > b ? a : b; }
__device__ __forceinline__ unsigned wave_max_u32(unsigned v) {
  v = umax(v, dpp_u<0x111, 0xf>(v)); v = umax(v, dpp_u<0x112, 0xf>(v)); v = umax(v, dpp_u<0x114, 0xf>(v)); v = umax(v, dpp_u<0x118, 0xf>(v));
  v = umax(v, dpp_u<0x142, 0xa>(v)); v = umax(v, dpp_u<0x143, 0xc>(v));
  return (unsigned)__builtin_amdgcn_readlane((int)v, 63);
}

#define STAMP_DECL
#define STAMPW(k, i, wv)
#define STAMP_FLUSH
#define STAMP(k, i) STAMPW(k, i, 0)

constexpr unsigned WS_ABT = 0;
constexpr unsigned WS_Y = WS_ABT + 1024u * 2048 * 2;
constexpr unsigned WS_F1 = WS_Y + 4u * 2048 * 512 * 2;
constexpr unsigned WS_F2 = WS_F1 + 4096;
constexpr unsigned WS_GR = WS_F2 + 2048;
constexpr unsigned WS_GA = WS_GR + 1024 * 8;
constexpr unsigned WS_EP = WS_GA + 224 * 256;
constexpr unsigned WS_SEL = WS_EP + 256;
constexpr unsigned WS_HN = WS_SEL + 2048;
constexpr unsigned WS_END = WS_HN + 2048u * 2048;

constexpr int SPIN_MAX = 1 << 18;
#define AUX1 0
#define CPF 1
#define FMODE 1
#define HNPUB 2
#define WPOS 0
#define COOP 1
#define TLBW 0
#define TS 0
#define QMODE 0
#define NPLAIN 8
#define T1MODE 1
#define FFNSTEP 2

constexpr int L_QR = 81920;
constexpr int L_RST = 65536;
constexpr int L_TAG = 66560;
constexpr int L_W = 16384;
constexpr int L_T2 = 131072;
constexpr int XS_STRIDE = 1024 + 16;
constexpr int L_XS = 81920;
constexpr int L_RED = L_XS + 8 * XS_STRIDE;
constexpr int L_MISC = L_RED + 8 * 128 * 4;
constexpr int L_HH = L_MISC + 1024;
constexpr int AS_STRIDE = F * 2 + 16;
constexpr int L_AS = L_HH + 1024;
static_assert(L_AS + 8 * AS_STRIDE <= L_T2, "lds");

struct Ring2 {
  static constexpr int p(int T) { return (T % 5) * 16384; }
  static constexpr int q(int T) { return L_QR + ((T + 3) % 5) * 16384; }
};
constexpr int koff(int T) { return T < 8 ? T * 128 : (T - 8) * 128 + 64; }
constexpr int x1slot(int s) { return L_QR + (s % 5) * 16384; }

template <int BP, int BQ, int NKT, class RG, bool STATS>
struct Cons {
  static constexpr int TP = BP / 32, TQ = BQ / 32;
  struct Ctx { const char* lds; int poff[TP][2]; int qoff[TQ][2]; };
  struct Fr { bf16x8 pf[2][TP], qf[2][TQ]; };
  struct St { f32x4 s1[2], s2[2], sw[TQ]; };
  template <int T> static __device__ __forceinline__ void readf(const Ctx& c, Fr& f) {
#pragma unroll
    for (int ks = 0; ks < 2; ++ks) {
#pragma unroll
      for (int m = 0; m < TP; ++m) f.pf[ks][m] = *(const bf16x8*)(c.lds + RG::p(T) + c.poff[m][ks]);
#pragma unroll
      for (int n = 0; n < TQ; ++n) f.qf[ks][n] = *(const bf16x8*)(c.lds + RG::q(T) + c.qoff[n][ks]);
    }
  }
  template <int T> static __device__ __forceinline__ void step(const Ctx& c, f32x4 (&acc)[TP][TQ], Fr& cur, Fr& nxt, St& st) {
    if constexpr (T + 1 < NKT) {
      WAIT_L(0);
      BAR();
      __builtin_amdgcn_sched_barrier(0);
      readf<T + 1>(c, nxt);
    }
    if constexpr (T + 1 == NKT) return;
#pragma unroll
    for (int ks = 0; ks < 2; ++ks) {
#pragma unroll
      for (int m = 0; m < TP; ++m)
#pragma unroll
        for (int n = 0; n < TQ; ++n) acc[m][n] = __builtin_amdgcn_mfma_f32_16x16x32_bf16(cur.pf[ks][m], cur.qf[ks][n], acc[m][n], 0, 0, 0);
      if constexpr (STATS) {
        const bf16x8 ones = {0x3f80, 0x3f80, 0x3f80, 0x3f80, 0x3f80, 0x3f80, 0x3f80, 0x3f80};
#pragma unroll
        for (int m = 0; m < 2; ++m) {
          st.s1[m] = __builtin_amdgcn_mfma_f32_16x16x32_bf16(cur.pf[ks][m], ones, st.s1[m], 0, 0, 0);
          st.s2[m] = __builtin_amdgcn_mfma_f32_16x16x32_bf16(cur.pf[ks][m], cur.pf[ks][m], st.s2[m], 0, 0, 0);
        }
#pragma unroll
        for (int n = 0; n < TQ; ++n) st.sw[n] = __builtin_amdgcn_mfma_f32_16x16x32_bf16(ones, cur.qf[ks][n], st.sw[n], 0, 0, 0);
      }
    }
    if constexpr (T + 1 < NKT) {
#pragma unroll
      for (int i = 0; i < 2 * (TP + TQ); ++i) { __builtin_amdgcn_sched_group_barrier(0x100, 1, 0); __builtin_amdgcn_sched_group_barrier(0x8, 1, 0); }
    }
    __builtin_amdgcn_sched_barrier(0);
  }
  template <int... Ts>
  static __device__ __forceinline__ void steps(const Ctx& c, f32x4 (&acc)[TP][TQ], Fr& f0, Fr& f1, St& st, std::integer_sequence<int, Ts...>) {
    ((Ts & 1 ? step<Ts>(c, acc, f1, f0, st) : step<Ts>(c, acc, f0, f1, st)), ...);
  }
  static __device__ __forceinline__ void run(const char* lds, int wp, int wq, int rot, int l, f32x4 (&acc)[TP][TQ], St& st, Fr& f1) {
    Ctx c; c.lds = lds;
    const int fr = l & 15, q = l >> 4;
#pragma unroll
    for (int ks = 0; ks < 2; ++ks) {
#pragma unroll
      for (int m = 0; m < TP; ++m) { const int row = wp * (BP / 2) + ((m + rot) & (TP - 1)) * 16 + fr; c.poff[m][ks] = row * 128 + (((ks * 4 + q) ^ ((row >> 1) & 7)) * 16); }
#pragma unroll
      for (int n = 0; n < TQ; ++n) { const int row = wq * (BQ / 2) + n * 16 + fr; c.qoff[n][ks] = row * 128 + (((ks * 4 + q) ^ ((row >> 1) & 7)) * 16); }
    }
    Fr f0;
    BAR();
    readf<0>(c, f0);
    steps(c, acc, f0, f1, st, std::make_integer_sequence<int, NKT>{});
  }
};

struct L1Ctx { const u16* xsrc[4]; const u16* wsrc[4]; char* lds; int w; };
template <int s> __device__ __forceinline__ void l1_issue(const L1Ctx& c) {
#pragma unroll
  for (int h = 0; h < 2; ++h) {
#pragma unroll
    for (int i = 0; i < 4; ++i) GLDS16(c.xsrc[i] + (size_t)(s >> 2) * 64 * D + (s & 3) * 128 + h * 64, c.lds + x1slot(s) + h * 8192 + (c.w * 4 + i) * 1024, AUX1);
    if constexpr (s < 4) {
#pragma unroll
      for (int i = 0; i < 4; ++i) GLDS16(c.wsrc[i] + s * 128 + h * 64, c.lds + (2 * s + h) * 8192 + (c.w * 4 + i) * 1024, AUX1);
    }
  }
}
struct Fr1 { bf16x8 pf[4][2], qf[4][2]; };
struct C1Ctx { const char* lds; int poff[2][2]; int qoff[2][2]; };
template <int s> __device__ __forceinline__ void c1_read(const C1Ctx& c, Fr1& f) {
#pragma unroll
  for (int ks = 0; ks < 4; ++ks) {
#pragma unroll
    for (int m = 0; m < 2; ++m) f.pf[ks][m] = *(const bf16x8*)(c.lds + x1slot(s) + (ks >> 1) * 8192 + c.poff[m][ks & 1]);
#pragma unroll
    for (int n = 0; n < 2; ++n) f.qf[ks][n] = *(const bf16x8*)(c.lds + (2 * (s & 3) + (ks >> 1)) * 8192 + c.qoff[n][ks & 1]);
  }
}
struct Acc1 { f32x4 acc[2][2], s1, s2; };
template <int s> __device__ __forceinline__ void c1_step(const C1Ctx& c, Acc1& a, f32x4 (&sw)[2], Fr1& cur, Fr1& nxt) {
  if constexpr (s == 7) WAIT_V(0);
  WAIT_L(0);
  BAR();
  __builtin_amdgcn_sched_barrier(0);
  if constexpr (s + 1 < 8) c1_read<s + 1>(c, nxt);
  const bf16x8 ones = {0x3f80, 0x3f80, 0x3f80, 0x3f80, 0x3f80, 0x3f80, 0x3f80, 0x3f80};
#pragma unroll
  for (int ks = 0; ks < 4; ++ks) {
#pragma unroll
    for (int m = 0; m < 2; ++m)
#pragma unroll
      for (int n = 0; n < 2; ++n) a.acc[m][n] = __builtin_amdgcn_mfma_f32_16x16x32_bf16(cur.pf[ks][m], cur.qf[ks][n], a.acc[m][n], 0, 0, 0);
    a.s1 = __builtin_amdgcn_mfma_f32_16x16x32_bf16(cur.pf[ks][0], ones, a.s1, 0, 0, 0);
    a.s2 = __builtin_amdgcn_mfma_f32_16x16x32_bf16(cur.pf[ks][0], cur.pf[ks][0], a.s2, 0, 0, 0);
    if constexpr (s < 4) {
#pragma unroll
      for (int n = 0; n < 2; ++n) sw[n] = __builtin_amdgcn_mfma_f32_16x16x32_bf16(ones, cur.qf[ks][n], sw[n], 0, 0, 0);
    }
  }
  if constexpr (s + 1 < 8) {
#pragma unroll
    for (int i = 0; i < 16; ++i) { __builtin_amdgcn_sched_group_barrier(0x100, 1, 0); __builtin_amdgcn_sched_group_barrier(0x8, 1, 0); }
  }
  __builtin_amdgcn_sched_barrier(0);
}

struct L2Ctx { const u16* psrc[8]; const u16* qsrc[8]; char* lds; int w; };
template <int T> __device__ __forceinline__ void l2_issueP(const L2Ctx& c) {
#pragma unroll
  for (int i = 0; i < 8; ++i) GLDS16(c.psrc[i] + koff(T), c.lds + Ring2::p(T) + (c.w * 8 + i) * 1024, 16);
}
template <int T, int AUX> __device__ __forceinline__ void l2_issueQ(const L2Ctx& c) {
#pragma unroll
  for (int i = 0; i < 8; ++i) GLDS16(c.qsrc[i] + koff(T), c.lds + Ring2::q(T) + (c.w * 8 + i) * 1024, AUX);
}
constexpr int l2_cnt(int s) { return s < 5 ? 8 : 16; }
constexpr int l2_younger(int T) { int up = T + 4 < 15 ? T + 4 : 15; int n = 0; for (int s = T + 2; s <= up; ++s) n += l2_cnt(s); return n; }
template <int T, int AUX> __device__ __forceinline__ void l2_step(const L2Ctx& c) {
  WAIT_V(l2_younger(T));
  BAR();
  if constexpr (T + 5 < 16) { l2_issueP<T + 5>(c); l2_issueQ<T + 5, AUX>(c); }
}
template <int AUX, int... Ts> __device__ __forceinline__ void l2_steps(const L2Ctx& c, std::integer_sequence<int, Ts...>) { (l2_step<Ts, AUX>(c), ...); }

__device__ __forceinline__ u32x4 widen16(u32x2 a, u32x2 c) {
  const auto s0 = __builtin_amdgcn_permlane16_swap(a[0], c[0], false, false);
  const auto s1 = __builtin_amdgcn_permlane16_swap(a[1], c[1], false, false);
  return (u32x4){s0[0], s1[0], s0[1], s1[1]};
}

__device__ __forceinline__ void glds16_asm(const void* gsrc, unsigned lds_dst) {
  unsigned keep;
  asm volatile("s_mov_b32 %0, m0\n\ts_mov_b32 m0, %2\n\ts_nop 0\n\tglobal_load_lds_dwordx4 %1, off\n\ts_mov_b32 m0, %0" : "=&s"(keep) : "v"(gsrc), "s"(lds_dst) : "memory");
}

__device__ __forceinline__ void poll_words(const unsigned* p, unsigned tag) {
  for (int it = 0; it < SPIN_MAX; ++it) {
    const unsigned v = __hip_atomic_load((gu32*)p, RLX_AGENT);
    if (__all(v == tag)) break;
    __builtin_amdgcn_s_sleep(1);
  }
  __builtin_amdgcn_fence(__ATOMIC_ACQUIRE, "wavefront");
}

__device__ __forceinline__ unsigned add_pk(unsigned a, unsigned b) { return pack2(bflo(a) + bflo(b), bfhi(a) + bfhi(b)); }
__device__ __forceinline__ unsigned sub_pk(unsigned a, unsigned b) { return pack2(bflo(a) - bflo(b), bfhi(a) - bfhi(b)); }
template <class R>
__device__ __forceinline__ void load_h(const u32x4 xv, R rs, int t, int l, float (&h)[8]) {
  const unsigned o = (unsigned)(t * D + l * 8) * 2;
  const u32x4 y0 = __builtin_amdgcn_raw_buffer_load_b128(rs, WS_Y + o, 0, 16);
  const u32x4 y1 = __builtin_amdgcn_raw_buffer_load_b128(rs, WS_Y + 1u * S * D * 2 + o, 0, 16);
  const u32x4 y2 = __builtin_amdgcn_raw_buffer_load_b128(rs, WS_Y + 2u * S * D * 2 + o, 0, 16);
  const u32x4 y3 = __builtin_amdgcn_raw_buffer_load_b128(rs, WS_Y + 3u * S * D * 2 + o, 0, 16);
#pragma unroll
  for (int e = 0; e < 4; ++e) {
    const unsigned yv = pack2((bflo(y0[e]) + bflo(y1[e])) - (bflo(y2[e]) + bflo(y3[e])), (bfhi(y0[e]) + bfhi(y1[e])) - (bfhi(y2[e]) + bfhi(y3[e])));
    const unsigned hv = add_pk(xv[e], yv);
    h[2 * e] = bflo(hv); h[2 * e + 1] = bfhi(hv);
  }
}
__device__ __forceinline__ u32x4 layernorm8(const float (&h)[8]) {
  const float pv = __builtin_bit_cast(float, __builtin_amdgcn_readfirstlane(__builtin_bit_cast(int, h[0])));
  float s = 0.f, s2 = 0.f, v = 0.f, v2 = 0.f;
#pragma unroll
  for (int e = 0; e < 4; ++e) { const float a = h[2 * e] - pv, c = h[2 * e + 1] - pv; s += a; s2 += c; v += a * a; v2 += c * c; }
  const float md = wave_sum(s + s2) * (1.0f / D);
  v = fmaxf(wave_sum(v + v2) - md * md * (float)D, 0.f);
  const float mean = pv + md;
  const float mb = rbf(mean);
  const float sd = rbf(sqrtf(rbf(rbf(v * (1.0f / D)) + 1e-5f)));
  const float inv = 1.0f / sd;
  u32x4 o;
#pragma unroll
  for (int e = 0; e < 4; ++e) {
    const unsigned d = pack2(h[2 * e] - mb, h[2 * e + 1] - mb);
    const float a = bflo(d), c = bfhi(d);
    float qa = a * inv, qc = c * inv;
    qa = fmaf(fmaf(-qa, sd, a), inv, qa); qc = fmaf(fmaf(-qc, sd, c), inv, qc);
    o[e] = pack2(qa, qc);
  }
  return o;
}

__global__ __launch_bounds__(512) void fused(const u16* __restrict__ x, const u16* __restrict__ ws_re, const u16* __restrict__ ws_im,
                                             const u16* __restrict__ wd_re, const u16* __restrict__ wd_im, const u16* __restrict__ w_r,
                                             const u16* __restrict__ w_gate, const u16* __restrict__ w_up, const u16* __restrict__ w_down,
                                             u16* __restrict__ out, char* __restrict__ ws) {
  __shared__ __attribute__((aligned(1024))) char lds[163840];
  const int b = blockIdx.x, xcd = b & 7, j = b >> 3;
  const int cc = xcd >> 2, et = j & 3, tt = (xcd & 3) * 4 + ((j >> 2) & 3), kh = j >> 4;
  const int g1 = (cc * 4 + et) * 2 + kh;
  const int e1 = cc * 8 + et * 2 + (tt & 1), s1 = kh * 8 + (tt >> 1);
  const int tid = threadIdx.x, w = __builtin_amdgcn_readfirstlane(tid >> 6), l = tid & 63, fr = l & 15, q = l >> 4;
  const u16* wsb = cc ? ws_im : ws_re;
  const u16* wdb = cc ? wd_im : wd_re;
  const bool down = j >= 28;
  const int fb = xcd * 28 + j, db = xcd * 4 + (j - 28);
  const auto rs = __builtin_amdgcn_make_buffer_rsrc(ws, 0, WS_END, 0x00020000);
  unsigned tag;
  STAMP_DECL;
  STAMP(0, 0);
  unsigned long long code_lo; unsigned code_len;
  asm volatile("s_getpc_b64 %0\n.Lfused_code_lo:\n\ts_mov_b32 %1, .Lfused_code_hi-.Lfused_code_lo" : "=s"(code_lo), "=s"(code_len));

  const unsigned lbase = (unsigned)(uintptr_t)(LAS char*)lds;
  {
    const int row = w * 8 + (l >> 3);
    const size_t o = (size_t)row * D + (((l & 7) ^ ((row >> 1) & 7)) * 8);
    const u16* xp = x + (size_t)s1 * 128 * D + o;
    const u16* wp_ = wdb + (size_t)(e1 & 7) * 64 * D + o;
#pragma unroll
    for (int k = 0; k < 4; ++k) {
      glds16_asm(xp + k * 64, lbase + x1slot(k >> 1) + (k & 1) * 8192 + w * 1024);
      glds16_asm(wp_ + k * 64, lbase + k * 8192 + w * 1024);
    }
  }
  if (w < 4) {
    const int wp = w >> 1, wq = w & 1;
    unsigned r0 = 0;
    {
      const int row = tid >> 3, ln = tid & 7;
      const int xs_tile = ((j >> 3) & 1) * 8 + (xcd & 3) * 2 + ((j >> 2) & 1);
      const u16* p = j < 16 ? x + (size_t)(xs_tile * 128 + (j & 3) * 32 + row) * D + ln * 64 : wdb + (size_t)((j - 16) * 32 + row) * D + ln * 64;
      if (T1MODE == 1) TOUCH(r0, p);
      if (T1MODE == 2) TOUCH_NT(r0, p);
    }
    {
      C1Ctx c; c.lds = lds;
#pragma unroll
      for (int ks = 0; ks < 2; ++ks) {
#pragma unroll
        for (int m = 0; m < 2; ++m) { const int row = wp * 32 + (m ^ wq) * 16 + fr; c.poff[m][ks] = row * 128 + (((ks * 4 + q) ^ ((row >> 1) & 7)) * 16); }
#pragma unroll
        for (int n = 0; n < 2; ++n) { const int row = wq * 32 + n * 16 + fr; c.qoff[n][ks] = row * 128 + (((ks * 4 + q) ^ ((row >> 1) & 7)) * 16); }
      }
      f32x4 sw[2] = {};
      Fr1 f0, f1;
      auto epilogue = [&](const Acc1& a, int pass) {
        float2* rst = (float2*)(lds + L_RST) + pass * 64;
        if ((fr >> 2) == q) {
          const int jj = fr & 3;
          const float s1v = jj == 0 ? a.s1[0] : jj == 1 ? a.s1[1] : jj == 2 ? a.s1[2] : a.s1[3];
          const float s2v = jj == 0 ? a.s2[0] : jj == 1 ? a.s2[1] : jj == 2 ? a.s2[2] : a.s2[3];
          const float mean = s1v * (1.0f / D);
          const float var = fmaxf(s2v * (1.0f / D) - mean * mean, 0.f);
          const float sd = rbf(sqrtf(rbf(rbf(var) + 1e-5f)));
          rst[wp * 32 + wq * 16 + fr] = make_float2(rbf(mean), 1.0f / sd);
        }
        WAIT_L(0);
        BAR();
        u32x2 pk[2][2];
#pragma unroll
        for (int m = 0; m < 2; ++m) {
          const int p0 = wp * 32 + (m ^ wq) * 16 + q * 4;
          const f32x4 sa = *(const f32x4*)(rst + p0), sb = *(const f32x4*)(rst + p0 + 2);
#pragma unroll
          for (int n = 0; n < 2; ++n) {
            const float swv = sw[n][0];
            const float v0 = (a.acc[m][n][0] - sa[0] * swv) * sa[1], v1 = (a.acc[m][n][1] - sa[2] * swv) * sa[3];
            const float v2 = (a.acc[m][n][2] - sb[0] * swv) * sb[1], v3 = (a.acc[m][n][3] - sb[2] * swv) * sb[3];
            pk[m][n] = (u32x2){pack2(v0, v1), pack2(v2, v3)};
          }
        }
        const int scol = s1 * 128 + pass * 64 + wp * 32 + ((q & 1) ^ wq) * 16 + (q >> 1) * 8;
#pragma unroll
        for (int n = 0; n < 2; ++n)
          __builtin_amdgcn_raw_buffer_store_b128(widen16(pk[0][n], pk[1][n]), rs, WS_ABT + (unsigned)((e1 * 64 + wq * 32 + n * 16 + fr) * S + scol) * 2, 0, 16);
      };
      if (COOP) WAIT_V(5);
      BAR();
      c1_read<0>(c, f0);
      if (COOP) WAIT_V(1);
      {
        Acc1 a = {};
        c1_step<0>(c, a, sw, f0, f1); c1_step<1>(c, a, sw, f1, f0); c1_step<2>(c, a, sw, f0, f1); c1_step<3>(c, a, sw, f1, f0);
        STAMP(0, 2);
        epilogue(a, 0);
        STAMP(0, 3);
      }
      {
        Acc1 a = {};
        c1_step<4>(c, a, sw, f0, f1); c1_step<5>(c, a, sw, f1, f0); c1_step<6>(c, a, sw, f0, f1); c1_step<7>(c, a, sw, f1, f0);
        STAMP(0, 4);
        epilogue(a, 1);
        STAMP(0, 5);
      }
      tag = __builtin_amdgcn_readfirstlane(*(const unsigned*)(lds + L_TAG));
      WAIT_V(0);
      KEEP(r0);
      STAMP(0, 6);
      BAR();
    }
    using C2 = Cons<128, 128, 16, Ring2, false>;
    f32x4 acc[4][4] = {};
    C2::St st;
    C2::Fr fl;
    C2::run(lds, wp, wq, 0, l, acc, st, fl);
    STAMP(1, 2);
    {
      const unsigned yo = WS_Y + (unsigned)(cc * 2 + kh) * S * D * 2;
      const int chunk = (q >> 1) + 2 * (q & 1);
#pragma unroll
      for (int n = 0; n < 4; ++n) {
#pragma unroll
        for (int ks = 0; ks < 2; ++ks)
#pragma unroll
          for (int m = 0; m < 4; ++m) acc[m][n] = __builtin_amdgcn_mfma_f32_16x16x32_bf16(fl.pf[ks][m], fl.qf[ks][n], acc[m][n], 0, 0, 0);
#pragma unroll
        for (int mp = 0; mp < 2; ++mp) {
          const u32x2 a = {pack2(acc[2 * mp][n][0], acc[2 * mp][n][1]), pack2(acc[2 * mp][n][2], acc[2 * mp][n][3])};
          const u32x2 c = {pack2(acc[2 * mp + 1][n][0], acc[2 * mp + 1][n][1]), pack2(acc[2 * mp + 1][n][2], acc[2 * mp + 1][n][3])};
          const int row = wq * 64 + n * 16 + fr;
          __builtin_amdgcn_raw_buffer_store_b128(widen16(a, c), rs, yo + (unsigned)((tt * 128 + row) * D + et * 128 + wp * 64 + mp * 32 + chunk * 8) * 2, 0, 16);
        }
      }
    }
    STAMP(1, 3);
    WAIT_V(0);
    STAMP(1, 6);
  } else if (w < 6) {
    const int lw = w - 4;
    const int r8 = l >> 3, ch = l & 7;
    {
      L1Ctx c; c.lds = lds; c.w = lw;
      const u16* P = x + (size_t)s1 * 128 * D;
      const u16* Q = wdb + (size_t)(e1 & 7) * 64 * D;
#pragma unroll
      for (int i = 0; i < 4; ++i) {
        const int row = (lw * 4 + i) * 8 + r8;
        const size_t o = (size_t)row * D + ((ch ^ ((row >> 1) & 7)) * 8);
        c.xsrc[i] = P + o; c.wsrc[i] = Q + o;
      }
      l1_issue<2>(c);
      WAIT_V(20); STAMPW(0, 1, 4); BAR();
      WAIT_V(16); BAR();
      l1_issue<3>(c);
      WAIT_V(16); BAR();
      l1_issue<4>(c); l1_issue<5>(c);
      WAIT_V(16); BAR();
      l1_issue<6>(c); l1_issue<7>(c);
      WAIT_V(24); BAR();
      BAR();
      WAIT_V(16); BAR();
      WAIT_V(8); BAR();
      WAIT_V(0); BAR();
    }
    L2Ctx c2;
    c2.lds = lds; c2.w = lw;
    {
      const u16* P = (const u16*)(ws + WS_ABT) + (size_t)(cc * 512 + et * 128) * S + kh * 1024;
      const u16* Q = wsb + (size_t)tt * 128 * S + kh * 1024;
#pragma unroll
      for (int i = 0; i < 8; ++i) {
        const int row = (lw * 8 + i) * 8 + r8;
        const size_t o = (size_t)row * S + ((ch ^ ((row >> 1) & 7)) * 8);
        c2.psrc[i] = P + o; c2.qsrc[i] = Q + o;
      }
    }
    tag = __builtin_amdgcn_readfirstlane(*(const unsigned*)(lds + L_TAG));
    BAR();
    BAR();
    poll_words((const unsigned*)(ws + WS_F1) + g1 * 32 + fr, tag);
    STAMPW(1, 0, 4);
    l2_issueP<0>(c2); l2_issueP<1>(c2); l2_issueP<2>(c2);
    BAR();
    WAIT_V(16);
    STAMPW(1, 1, 4);
    BAR();
    l2_issueP<3>(c2); l2_issueP<4>(c2);
    l2_steps<0>(c2, std::make_integer_sequence<int, 15>{});
  } else if (w == 6) {
    const int r8 = l >> 3, ch = l & 7;
    const unsigned ep = __hip_atomic_load((gu32*)(ws + WS_EP), RLX_AGENT);
    const u16* Q = wsb + (size_t)tt * 128 * S + kh * 1024;
    const u16* qa = Q + (size_t)r8 * S + ((ch ^ (r8 >> 1)) * 8);
    const u16* qb = Q + (size_t)(8 + r8) * S + ((ch ^ (4 + (r8 >> 1))) * 8);
    auto issueQ = [&](int T) {
#pragma unroll
      for (int i = 0; i < 8; ++i) {
        if (QMODE == 0 || (QMODE == 1 && 2 * i < NPLAIN)) {
          GLDS16(qa + (size_t)i * 16 * S + T * 128, lds + L_QR + ((T + 3) % 5) * 16384 + (2 * i) * 1024, 0);
          GLDS16(qb + (size_t)i * 16 * S + T * 128, lds + L_QR + ((T + 3) % 5) * 16384 + (2 * i + 1) * 1024, 0);
        } else {
          GLDS16(qa + (size_t)i * 16 * S + T * 128, lds + L_QR + ((T + 3) % 5) * 16384 + (2 * i) * 1024, 2);
          GLDS16(qb + (size_t)i * 16 * S + T * 128, lds + L_QR + ((T + 3) % 5) * 16384 + (2 * i + 1) * 1024, 2);
        }
      }
    };
    if (COOP) WAIT_V(5);
    BAR();
    if (COOP) WAIT_V(1);
    BAR();
    tag = __builtin_amdgcn_readfirstlane(ep * 1664525u + 1013904223u);
    *(unsigned*)(lds + L_TAG) = tag;
    WAIT_L(0);
    BAR(); BAR(); BAR();
    BAR();
    BAR(); issueQ(0);
    BAR(); issueQ(1);
    BAR(); issueQ(2);
    BAR(); BAR();
    BAR(); issueQ(3); issueQ(4);
    WAIT_V(32);
    BAR();
    const unsigned* fB = (const unsigned*)(ws + WS_F1) + 512 + g1 * 32 + fr;
    BAR(); BAR();
    const unsigned vB = __hip_atomic_load((gu32*)fB, RLX_AGENT);
    WAIT_V(17);
    BAR();
    if (!__all(vB == tag)) poll_words(fB, tag);
    WAIT_V(0);
    BAR();
#pragma unroll
    for (int T = 4; T < 15; ++T) BAR();
  } else {
    unsigned r[4] = {0, 0, 0, 0};
    constexpr int LPI = 64 / (2 * NPLAIN) > 0 ? 64 / (2 * NPLAIN) : 1;
    const int pr = j * NPLAIN + (l % NPLAIN);
    const int prow = (pr / (NPLAIN * 8)) * 128 + (pr % (NPLAIN * 8));
    const u16* wb = wsb + (size_t)((xcd & 3) * 512 + prow) * S + ((l / NPLAIN) & 1) * 1024 + (l / (2 * NPLAIN)) * 128;
#define TW(i) TOUCH(r[(i) & 1], wb + ((i) % (8 / LPI)) * (LPI * 128) + ((i) / (8 / LPI)) * 64)
    auto ffn_ptr = [&](int i) -> const char* {
      const int li = j * 192 + i * 64 + l;
      if (li < 4096) { const int mrow = (li & 2047) >> 2, c4 = li & 3; return (const char*)((li < 2048 ? w_gate : w_up) + (size_t)mrow * F + xcd * 224 + c4 * 64); }
      const int rr = li - 4096;
      return (const char*)(w_down + (size_t)(rr < F ? rr : 0) * D + xcd * 64);
    };
    if (TLBW) {
      TOUCH_SC1(r[3], ws + (size_t)l * 65536);
      TOUCH_SC1(r[3], ws + WS_Y + (size_t)l * 65536);
      TOUCH_SC1(r[3], ws + WS_Y + (size_t)(64 + l) * 65536);
      TOUCH_SC1(r[3], (l < 32 ? (const char*)out + (size_t)l * 65536 : l < 34 ? (const char*)ws + WS_F1 + (size_t)(l - 32) * 65536
                       : (const char*)wsb + (size_t)(xcd & 3) * 2097152 + (size_t)(l - 34) * 65536));
    }
    if (CPF) {
      const unsigned off = (unsigned)(j * 16 + fr) * 128u;
      const char* cp = (const char*)code_lo + (off < code_len ? off : 0u);
      if (l < 16) TOUCH(r[3], cp);
    }
    const char* fp0 = ffn_ptr(0); const char* fp1 = ffn_ptr(1); const char* fp2 = ffn_ptr(2);
    const u16* wb2 = wsb + (size_t)((xcd & 3) * 512 + j * 16 + fr) * S + ((l >> 4) & 1) * 1024 + (l >> 5) * 128;
#define TF(i) TOUCH(r[(i) & 1], wb2 + ((i) & 3) * 256 + ((i) >> 2) * 64)
#define TFF(p) TOUCH(r[2], p)
    if (COOP) WAIT_V(4 + CPF);
    BAR();
    if (COOP) WAIT_V(CPF);
    BAR(); BAR(); TFF(fp0);
    tag = __builtin_amdgcn_readfirstlane(*(const unsigned*)(lds + L_TAG));
    BAR(); TFF(fp1); BAR(); TFF(fp2); BAR();
    BAR(); BAR(); BAR();
    BAR();
    if (l == 0) __hip_atomic_store((gu32*)(ws + WS_F1) + g1 * 32 + tt, tag, RLX_AGENT);
    BAR();
    BAR();
    if (l == 0) __hip_atomic_store((gu32*)(ws + WS_F1) + 512 + g1 * 32 + tt, tag, RLX_AGENT);
#pragma unroll
    for (int T = 0; T < 16; ++T) {
      BAR();
    }
    WAIT_V(0);
    KEEP(r[0]); KEEP(r[1]); KEEP(r[2]); KEEP(r[3]);
  }
  const int t_own = tt * 128 + g1 * 8 + w;
  unsigned rf = 0;
  const u32x4 wr = *(const u32x4*)(w_r + l * 8);
  const u32x4 x_own = *(const u32x4*)(x + (size_t)t_own * D + l * 8);
  {
    BAR();
    if (tid == 0) __hip_atomic_store((gu32*)(ws + WS_F2) + tt * 32 + g1, tag, RLX_AGENT);
    STAMP(1, 4);
    if (w == 0) poll_words((const unsigned*)(ws + WS_F2) + tt * 32 + fr, tag);
    if (FMODE == 0) {
    if (w >= 5) {
      const int li = j * 192 + (w - 5) * 64 + l;
      const char* p2;
      if (li < 4096) { const int mrow = (li & 2047) >> 2, c4 = li & 3; p2 = (const char*)((li < 2048 ? w_gate : w_up) + (size_t)mrow * F + xcd * 224 + c4 * 64); }
      else { const int rr = li - 4096; p2 = (const char*)(w_down + (size_t)(rr < F ? rr : 0) * D + xcd * 64); }
      TOUCH(rf, p2);
    }
    }
    BAR();
  }
  STAMP(2, 0);
  unsigned* misc = (unsigned*)(lds + L_MISC);
  u32x4 own;
  {
    float h[8];
    load_h(x_own, rs, t_own, l, h);
    STAMP(2, 7);
    const u32x4 hn = layernorm8(h);
    float r = 0.f, r2 = 0.f;
#pragma unroll
    for (int e = 0; e < 4; ++e) { r += bflo(hn[e]) * bflo(wr[e]); r2 += bfhi(hn[e]) * bfhi(wr[e]); }
    r = wave_sum(r + r2);
    const unsigned rb = f2bf(r);
    const unsigned ord = (rb & 0x8000u) ? (~rb & 0xffffu) : (rb | 0x8000u);
    if (l == 0) { ((u16*)misc)[w] = (u16)ord; if (w == 0) misc[8] = 0; }
    WAIT_L(0);
    BAR();
    if (tid < 4) __hip_atomic_store((gu64*)(ws + WS_GR) + (tt * 16 + g1) * 4 + tid, ((unsigned long long)tag << 32) | misc[tid], RLX_AGENT);
    bool pub = HNPUB == 1;
    if (HNPUB == 2) {
      const unsigned ov = ((const u16*)misc)[l & 7];
      pub = !__any(ov > ord || (ov == ord && (l & 7) < w));
    }
    if (pub) {
      __builtin_amdgcn_raw_buffer_store_b128((u32x4){hn[0], tag, hn[1], tag}, rs, WS_HN + (unsigned)t_own * 2048 + l * 32, 0, 16);
      __builtin_amdgcn_raw_buffer_store_b128((u32x4){hn[2], tag, hn[3], tag}, rs, WS_HN + (unsigned)t_own * 2048 + l * 32 + 16, 0, 16);
    }
#pragma unroll
    for (int e = 0; e < 4; ++e) own[e] = pack2(h[2 * e] + bflo(hn[e]), h[2 * e + 1] + bfhi(hn[e]));
  }
  KEEP(rf);
  {
    const u16* wsrc = down ? w_down + (size_t)(l >> 1) * D + db * 16 + (l & 1) * 8 : ((l & 1) ? w_up : w_gate) + (size_t)(l >> 1) * F + fb * 8;
    if (down) {
#pragma unroll
      for (int i = 0; i < 7; ++i) glds16_asm(wsrc + (size_t)(w * 7 + i) * 32 * D, lbase + L_W + (w * 7 + i) * 1024);
    } else {
#pragma unroll
      for (int i = 0; i < 2; ++i) glds16_asm(wsrc + (size_t)(w * 2 + i) * 32 * F, lbase + L_W + (w * 2 + i) * 1024);
    }
  }
  STAMP(2, 1);
  unsigned key;
  bool sel_own;
  bool fast = HNPUB == 1;
  if (!down) {
  unsigned kk[4];
  {
    u32x4 a;
    for (int it = 0; it < SPIN_MAX; ++it) {
      asm volatile("" ::: "memory");
      a = __builtin_amdgcn_raw_buffer_load_b128(rs, WS_GR + (unsigned)tid * 16, 0, 16);
      if (__all(a[1] == tag && a[3] == tag)) break;
      __builtin_amdgcn_s_sleep(1);
    }
    __builtin_amdgcn_fence(__ATOMIC_ACQUIRE, "wavefront");
    const unsigned ib = 0xffffu - (unsigned)tid * 4;
    kk[0] = (a[0] << 16) | ib; kk[1] = (a[0] & 0xffff0000u) | (ib - 1); kk[2] = (a[2] << 16) | (ib - 2); kk[3] = (a[2] & 0xffff0000u) | (ib - 3);
  }
  STAMP(2, 2);
  {
    const unsigned m = wave_max_u32(umax(umax(kk[0], kk[1]), umax(kk[2], kk[3])));
    if (l == 0) misc[16 + w] = m;
  }
  WAIT_L(0);
  BAR();
  STAMP(2, 5);
  if (b == 0 && tid == 0) __hip_atomic_store((gu32*)(ws + WS_EP), tag, RLX_AGENT);
  {
    const u32x4 ma = *(const u32x4*)(misc + 16), mb2 = *(const u32x4*)(misc + 20);
    const unsigned lo = min(min(min(ma[0], ma[1]), min(ma[2], ma[3])), min(min(mb2[0], mb2[1]), min(mb2[2], mb2[3])));
    if (l < 16) misc[64 + w * 16 + l] = 0u;
    unsigned base = 0;
#pragma unroll
    for (int e = 0; e < 4; ++e) {
      const unsigned long long bm = __ballot(kk[e] >= lo);
      const unsigned pos = base + __builtin_amdgcn_mbcnt_hi((unsigned)(bm >> 32), __builtin_amdgcn_mbcnt_lo((unsigned)bm, 0u));
      if (kk[e] >= lo && pos < 16) misc[64 + w * 16 + pos] = kk[e];
      base += (unsigned)__builtin_popcountll(bm);
    }
    if (base > 16 && l == 0) misc[8] = 1u;
  }
  WAIT_L(0);
  BAR();
  STAMP(2, 6);
  {
    const unsigned ovf = __builtin_amdgcn_readfirstlane(misc[8]);
    if (ovf == 0) {
      const unsigned cv = misc[64 + w * 16 + (l >> 2)];
      int rank = 0;
#pragma unroll
      for (int i = 0; i < 8; ++i) {
        const u32x4 c4 = *(const u32x4*)(misc + 64 + (l & 3) * 32 + i * 4);
#pragma unroll
        for (int e = 0; e < 4; ++e) rank += (c4[e] > cv) ? 1 : 0;
      }
      rank += (int)dpp_u<0xB1, 0xf>((unsigned)rank);
      rank += (int)dpp_u<0x4E, 0xf>((unsigned)rank);
      if (cv != 0u && rank < 8 && (l & 3) == 0) misc[32 + rank] = cv;
      WAIT_L(0);
      BAR();
      const unsigned sv = misc[32 + (l & 7)];
      if (b < 8 && tid < 8) __hip_atomic_store((gu64*)(ws + WS_SEL) + b * 32 + tid, ((unsigned long long)tag << 32) | sv, RLX_AGENT);
      key = (unsigned)__builtin_amdgcn_readlane((int)sv, w);
      sel_own = __any((int)(sv & 0xffffu) == 0xffff - t_own);
      fast = !__any(sv > key && ((0xffffu - (sv & 0xffffu)) >> 3) == ((0xffffu - (key & 0xffffu)) >> 3));
    } else {
      unsigned mine = 0;
#pragma unroll 1
      for (int it = 0; it < 8; ++it) {
        const unsigned m = wave_max_u32(umax(umax(kk[0], kk[1]), umax(kk[2], kk[3])));
#pragma unroll
        for (int e = 0; e < 4; ++e) kk[e] = (kk[e] == m) ? 0u : kk[e];
        if (l == it) mine = m;
      }
      if (l < 8) misc[192 + w * 8 + l] = mine;
      WAIT_L(0);
      BAR();
      unsigned cv = misc[192 + l];
      key = 0; sel_own = false;
#pragma unroll 1
      for (int it = 0; it < 8; ++it) {
        const unsigned m = wave_max_u32(cv);
        if (it == w) key = m;
        sel_own |= (int)(m & 0xffffu) == 0xffff - t_own;
        cv = (cv == m) ? 0u : cv;
      }
      if (b < 8 && l == 0) __hip_atomic_store((gu64*)(ws + WS_SEL) + b * 32 + w, ((unsigned long long)tag << 32) | key, RLX_AGENT);
    }
  }
  } else {
    if (w == 0) {
      unsigned long long g;
      for (int it = 0; it < SPIN_MAX; ++it) {
        g = __hip_atomic_load((gu64*)(ws + WS_SEL) + xcd * 32 + (l & 7), RLX_AGENT);
        if (__all((unsigned)(g >> 32) == tag)) break;
        __builtin_amdgcn_s_sleep(1);
      }
      if (l < 8) misc[32 + l] = (unsigned)g;
      WAIT_L(0);
    }
    BAR();
    const unsigned sv = misc[32 + (l & 7)];
    key = (unsigned)__builtin_amdgcn_readlane((int)sv, w);
    sel_own = __any((int)(sv & 0xffffu) == 0xffff - t_own);
    fast = !__any(sv > key && ((0xffffu - (sv & 0xffffu)) >> 3) == ((0xffffu - (key & 0xffffu)) >> 3));
  }
  STAMP(2, 3);
  const int t_sel = 0xffff - (int)(key & 0xffffu);
  bf16x8 bw[16];
  if (!down && w == 0) {
    const char* bp = lds + L_W + (q * 8 + (fr >> 2)) * 32 + (fr & 3) * 8;
#pragma unroll
    for (int ks = 0; ks < 16; ++ks) {
      const v4i16_t g0 = __builtin_amdgcn_ds_read_tr16_b64_v4i16((LAS v4i16_t*)(bp + ks * 1024));
      const v4i16_t g1v = __builtin_amdgcn_ds_read_tr16_b64_v4i16((LAS v4i16_t*)(bp + ks * 1024 + 128));
      bw[ks] = (bf16x8){g0[0], g0[1], g0[2], g0[3], g1v[0], g1v[1], g1v[2], g1v[3]};
    }
  }
  {
    float h[8];
    u32x4 hn;
    if (down || !fast) {
      const u32x4 xs = *(const u32x4*)(x + (size_t)t_sel * D + l * 8);
      load_h(xs, rs, t_sel, l, h);
    }
    if (fast) {
      u32x4 a, c;
      for (int it = 0; it < SPIN_MAX; ++it) {
        asm volatile("" ::: "memory");
        a = __builtin_amdgcn_raw_buffer_load_b128(rs, WS_HN + (unsigned)t_sel * 2048 + l * 32, 0, 16);
        c = __builtin_amdgcn_raw_buffer_load_b128(rs, WS_HN + (unsigned)t_sel * 2048 + l * 32 + 16, 0, 16);
        if (__all(a[1] == tag && a[3] == tag && c[1] == tag && c[3] == tag)) break;
        __builtin_amdgcn_s_sleep(1);
      }
      hn = (u32x4){a[0], a[2], c[0], c[2]};
    } else {
      hn = layernorm8(h);
    }
    if (!down) {
      *(u32x4*)(lds + L_XS + w * XS_STRIDE + l * 16) = hn;
    } else {
      if ((l >> 1) == db) {
        float2* hh = (float2*)(lds + L_HH) + w * 16 + (l & 1) * 8;
#pragma unroll
        for (int e = 0; e < 4; ++e) { hh[2 * e] = make_float2(h[2 * e], bflo(hn[e])); hh[2 * e + 1] = make_float2(h[2 * e + 1], bfhi(hn[e])); }
      }
      if (l == 0) {
        const unsigned ord = key >> 16;
        const unsigned rb = (ord & 0x8000u) ? (ord & 0x7fffu) : (~ord & 0xffffu);
        misc[24 + w] = key;
        misc[40 + w] = __float_as_uint(rbf(1.0f / (1.0f + expf(-bf2f(rb)))));
      }
    }
    if (!sel_own) {
      const auto ro = __builtin_amdgcn_make_buffer_rsrc(out, 0, S * D * 2, 0x00020000);
      __builtin_amdgcn_raw_buffer_store_b128(own, ro, (unsigned)(t_own * D + l * 8) * 2, 0, OUTAUX);
    }
  }
  STAMP(2, 4);
  float* red = (float*)(lds + L_RED);
  if (!down) {
    WAIT_L(0);
    BAR();
    if (w == 0) {
      f32x4 a4[2] = {};
      const char* ap = lds + L_XS + (fr & 7) * XS_STRIDE + q * 16;
#pragma unroll
      for (int ks = 0; ks < 16; ++ks) {
        const bf16x8 a = *(const bf16x8*)(ap + ks * 64);
        a4[ks & 1] = __builtin_amdgcn_mfma_f32_16x16x32_bf16(a, bw[ks], a4[ks & 1], 0, 0, 0);
      }
      const f32x4 sum = a4[0] + a4[1];
      const bool hi = fr >= 8;
      unsigned actb[2];
#pragma unroll
      for (int k = 0; k < 2; ++k) {
        const float send = hi ? sum[k] : sum[2 + k];
        const float recv = dpp_f<0x128, 0xf>(send);
        const float g = rbf(hi ? recv : sum[k]), u = rbf(hi ? sum[2 + k] : recv);
        const float sg = rbf(g / (1.0f + expf(-g)));
        actb[k] = f2bf(sg * u);
      }
      const unsigned n0 = dpp_u<0xB1, 0xf>(actb[0]), n1 = dpp_u<0xB1, 0xf>(actb[1]);
      const unsigned val = (fr & 1) ? (n1 | (actb[1] << 16)) : (actb[0] | (n0 << 16));
      const int tok = q * 4 + (hi ? 2 : 0) + (fr & 1);
      if (q < 2) __hip_atomic_store((gu64*)(ws + WS_GA) + fb * 32 + tok * 4 + ((fr & 7) >> 1), ((unsigned long long)tag << 32) | val, RLX_AGENT);
    }
    STAMP(3, 0);
  } else {
    for (int it = 0; it < SPIN_MAX; ++it) {
      asm volatile("" ::: "memory");
      bool ok = true;
#pragma unroll
      for (int i = 0; i < 7; ++i) {
        const int rec = w * 28 + i * 4 + q;
        const u32x4 a = __builtin_amdgcn_raw_buffer_load_b128(rs, WS_GA + (unsigned)rec * 256 + fr * 16, 0, 16);
        ok &= (a[1] == tag) & (a[3] == tag);
        *(u32x2*)(lds + L_AS + (fr >> 1) * AS_STRIDE + rec * 16 + (fr & 1) * 8) = (u32x2){a[0], a[2]};
      }
      if (__all(ok)) break;
      __builtin_amdgcn_s_sleep(1);
    }
    STAMP(3, 1);
    WAIT_L(0);
    f32x4 a4 = {};
#pragma unroll
    for (int i = 0; i < 7; ++i) {
      const int ks = w * 7 + i;
      const bf16x8 a = *(const bf16x8*)(lds + L_AS + (fr & 7) * AS_STRIDE + ks * 64 + q * 16);
      const int r0 = ks * 32 + q * 8 + (fr >> 2);
      const int coff = (fr & 3) * 8;
      const v4i16_t g0 = __builtin_amdgcn_ds_read_tr16_b64_v4i16((LAS v4i16_t*)(lds + L_W + r0 * 32 + coff));
      const v4i16_t g1v = __builtin_amdgcn_ds_read_tr16_b64_v4i16((LAS v4i16_t*)(lds + L_W + (r0 + 4) * 32 + coff));
      const bf16x8 bg = {g0[0], g0[1], g0[2], g0[3], g1v[0], g1v[1], g1v[2], g1v[3]};
      a4 = __builtin_amdgcn_mfma_f32_16x16x32_bf16(a, bg, a4, 0, 0, 0);
    }
    if (q < 2) {
#pragma unroll
      for (int jj = 0; jj < 4; ++jj) red[w * 128 + (q * 4 + jj) * 16 + fr] = a4[jj];
    }
    WAIT_L(0);
    BAR();
    if (tid < 128) {
      float s = 0.f;
#pragma unroll
      for (int ww = 0; ww < 8; ++ww) s += red[ww * 128 + tid];
      const unsigned k2 = misc[24 + (tid >> 4)];
      const int t = 0xffff - (int)(k2 & 0xffffu);
      const float sig = __uint_as_float(misc[40 + (tid >> 4)]);
      const float upd = rbf(rbf(s) * sig);
      const float2 hh = ((const float2*)(lds + L_HH))[tid];
      out[(size_t)t * D + db * 16 + (tid & 15)] = (u16)f2bf(hh.x + rbf(hh.y + upd));
    }
    STAMP(3, 2);
  }
  STAMP_FLUSH;
  asm volatile(".Lfused_code_hi:");
}

extern "C" void kernel_launch(void* const* d_in, const int* in_sizes, int n_in, void* d_out, int out_size, void* d_ws,
                              size_t ws_size, hipStream_t stream) {
  fused<<<256, 512, 0, stream>>>((const u16*)d_in[0], (const u16*)d_in[1], (const u16*)d_in[2], (const u16*)d_in[3], (const u16*)d_in[4],
                                 (const u16*)d_in[6], (const u16*)d_in[7], (const u16*)d_in[8], (const u16*)d_in[9], (u16*)d_out, (char*)d_ws);
}
```

```cpp
#include <hip/hip_runtime.h>
#include <hip/hip_bf16.h>
#include <stdint.h>
#include <utility>

typedef unsigned short u16;
typedef __attribute__((ext_vector_type(8))) short bf16x8;
typedef __attribute__((ext_vector_type(4))) short v4i16_t;
typedef __attribute__((ext_vector_type(4))) float f32x4;
typedef __attribute__((ext_vector_type(4))) unsigned u32x4;
typedef __attribute__((ext_vector_type(2))) unsigned u32x2;
#define GAS __attribute__((address_space(1)))
#define LAS __attribute__((address_space(3)))
typedef GAS unsigned gu32;
typedef GAS unsigned long long gu64;
#define RLX_AGENT __ATOMIC_RELAXED, __HIP_MEMORY_SCOPE_AGENT

constexpr int S = 2048, D = 512, F = 1792;

__device__ __forceinline__ float bf2f(unsigned v) { return __uint_as_float(v << 16); }
__device__ __forceinline__ float bflo(unsigned v) { return __uint_as_float(v << 16); }
__device__ __forceinline__ float bfhi(unsigned v) { return __uint_as_float(v & 0xffff0000u); }
__device__ __forceinline__ unsigned f2bf(float f) { __bf16 b = (__bf16)f; return (unsigned)__builtin_bit_cast(u16, b); }
__device__ __forceinline__ float rbf(float f) { return bf2f(f2bf(f)); }
__device__ __forceinline__ unsigned pack2(float lo, float hi) { return f2bf(lo) | (f2bf(hi) << 16); }

#define WAIT_V(n) asm volatile("s_waitcnt vmcnt(%0)" ::"n"(n) : "memory")
#define WAIT_L(n) asm volatile("s_waitcnt lgkmcnt(%0)" ::"n"(n) : "memory")
#define BAR() __builtin_amdgcn_s_barrier()
#define GLDS16(src, dst, aux) __builtin_amdgcn_global_load_lds((const unsigned*)(src), (unsigned*)(dst), 16, 0, aux)
#define TOUCH(reg, ptr) asm volatile("global_load_dword %0, %1, off" : "+v"(reg) : "v"(ptr) : "memory")
#define KEEP(reg) asm volatile("" ::"v"(reg))
constexpr int SC1 = 16;

template <int CTRL, int RM> __device__ __forceinline__ float dpp_f(float v) {
  return __builtin_bit_cast(float, __builtin_amdgcn_update_dpp(0, __builtin_bit_cast(int, v), CTRL, RM, 0xf, true));
}
__device__ __forceinline__ float wave_sum(float v) {
  v += dpp_f<0x111, 0xf>(v); v += dpp_f<0x112, 0xf>(v); v += dpp_f<0x114, 0xf>(v); v += dpp_f<0x118, 0xf>(v);
  v += dpp_f<0x142, 0xa>(v); v += dpp_f<0x143, 0xc>(v);
  return __builtin_bit_cast(float, __builtin_amdgcn_readlane(__builtin_bit_cast(int, v), 63));
}
template <int CTRL, int RM> __device__ __forceinline__ unsigned dpp_u(unsigned v) {
  return (unsigned)__builtin_amdgcn_update_dpp(0, (int)v, CTRL, RM, 0xf, true);
}
__device__ __forceinline__ unsigned umax(unsigned a, unsigned b) { return a > b ? a : b; }
__device__ __forceinline__ unsigned wave_max_u32(unsigned v) {
  v = umax(v, dpp_u<0x111, 0xf>(v)); v = umax(v, dpp_u<0x112, 0xf>(v)); v = umax(v, dpp_u<0x114, 0xf>(v)); v = umax(v, dpp_u<0x118, 0xf>(v));
  v = umax(v, dpp_u<0x142, 0xa>(v)); v = umax(v, dpp_u<0x143, 0xc>(v));
  return (unsigned)__builtin_amdgcn_readlane((int)v, 63);
}

constexpr unsigned WS_ABT = 0;
constexpr unsigned WS_Y = WS_ABT + 1024u * 2048 * 2;
constexpr unsigned WS_F1 = WS_Y + 4u * 2048 * 512 * 2;
constexpr unsigned WS_F2 = WS_F1 + 4096;
constexpr unsigned WS_GR = WS_F2 + 2048;
constexpr unsigned WS_GA = WS_GR + 1024 * 8;
constexpr unsigned WS_EP = WS_GA + 224 * 256;
constexpr unsigned WS_SEL = WS_EP + 256;
constexpr unsigned WS_HN = WS_SEL + 2048;
constexpr unsigned WS_END = WS_HN + 2048u * 2048;

constexpr int SPIN_MAX = 1 << 18;

constexpr int L_QR = 81920;
constexpr int L_RST = 65536;
constexpr int L_TAG = 66560;
constexpr int L_W = 16384;
constexpr int XS_STRIDE = 1024 + 16;
constexpr int L_XS = 81920;
constexpr int L_RED = L_XS + 8 * XS_STRIDE;
constexpr int L_MISC = L_RED + 8 * 128 * 4;
constexpr int L_HH = L_MISC + 1024;
constexpr int AS_STRIDE = F * 2 + 16;
constexpr int L_AS = L_HH + 1024;
static_assert(L_AS + 8 * AS_STRIDE <= 163840, "lds");

constexpr int ring_p(int T) { return (T % 5) * 16384; }
constexpr int ring_q(int T) { return L_QR + ((T + 3) % 5) * 16384; }
constexpr int koff(int T) { return T < 8 ? T * 128 : (T - 8) * 128 + 64; }
constexpr int x1slot(int s) { return L_QR + (s % 5) * 16384; }

struct C2Ctx { const char* lds; int poff[4][2]; int qoff[4][2]; };
struct Fr2 { bf16x8 pf[2][4], qf[2][4]; };
template <int T> __device__ __forceinline__ void c2_read(const C2Ctx& c, Fr2& f) {
#pragma unroll
  for (int ks = 0; ks < 2; ++ks) {
#pragma unroll
    for (int m = 0; m < 4; ++m) f.pf[ks][m] = *(const bf16x8*)(c.lds + ring_p(T) + c.poff[m][ks]);
#pragma unroll
    for (int n = 0; n < 4; ++n) f.qf[ks][n] = *(const bf16x8*)(c.lds + ring_q(T) + c.qoff[n][ks]);
  }
}
template <int T> __device__ __forceinline__ void c2_step(const C2Ctx& c, f32x4 (&acc)[4][4], Fr2& cur, Fr2& nxt) {
  WAIT_L(0);
  BAR();
  __builtin_amdgcn_sched_barrier(0);
  c2_read<T + 1>(c, nxt);
#pragma unroll
  for (int ks = 0; ks < 2; ++ks)
#pragma unroll
    for (int m = 0; m < 4; ++m)
#pragma unroll
      for (int n = 0; n < 4; ++n) acc[m][n] = __builtin_amdgcn_mfma_f32_16x16x32_bf16(cur.pf[ks][m], cur.qf[ks][n], acc[m][n], 0, 0, 0);
#pragma unroll
  for (int i = 0; i < 16; ++i) { __builtin_amdgcn_sched_group_barrier(0x100, 1, 0); __builtin_amdgcn_sched_group_barrier(0x8, 1, 0); }
  __builtin_amdgcn_sched_barrier(0);
}
template <int... Ts>
__device__ __forceinline__ void c2_steps(const C2Ctx& c, f32x4 (&acc)[4][4], Fr2& f0, Fr2& f1, std::integer_sequence<int, Ts...>) {
  ((Ts & 1 ? c2_step<Ts>(c, acc, f1, f0) : c2_step<Ts>(c, acc, f0, f1)), ...);
}

struct L1Ctx { const u16* xsrc[4]; const u16* wsrc[4]; char* lds; int w; };
template <int s> __device__ __forceinline__ void l1_issue(const L1Ctx& c) {
#pragma unroll
  for (int h = 0; h < 2; ++h) {
#pragma unroll
    for (int i = 0; i < 4; ++i) GLDS16(c.xsrc[i] + (size_t)(s >> 2) * 64 * D + (s & 3) * 128 + h * 64, c.lds + x1slot(s) + h * 8192 + (c.w * 4 + i) * 1024, 0);
    if constexpr (s < 4) {
#pragma unroll
      for (int i = 0; i < 4; ++i) GLDS16(c.wsrc[i] + s * 128 + h * 64, c.lds + (2 * s + h) * 8192 + (c.w * 4 + i) * 1024, 0);
    }
  }
}
struct Fr1 { bf16x8 pf[4][2], qf[4][2]; };
struct C1Ctx { const char* lds; int poff[2][2]; int qoff[2][2]; };
template <int s> __device__ __forceinline__ void c1_read(const C1Ctx& c, Fr1& f) {
#pragma unroll
  for (int ks = 0; ks < 4; ++ks) {
#pragma unroll
    for (int m = 0; m < 2; ++m) f.pf[ks][m] = *(const bf16x8*)(c.lds + x1slot(s) + (ks >> 1) * 8192 + c.poff[m][ks & 1]);
#pragma unroll
    for (int n = 0; n < 2; ++n) f.qf[ks][n] = *(const bf16x8*)(c.lds + (2 * (s & 3) + (ks >> 1)) * 8192 + c.qoff[n][ks & 1]);
  }
}
struct Acc1 { f32x4 acc[2][2], s1, s2; };
template <int s> __device__ __forceinline__ void c1_step(const C1Ctx& c, Acc1& a, f32x4 (&sw)[2], Fr1& cur, Fr1& nxt) {
  if constexpr (s == 7) WAIT_V(0);
  WAIT_L(0);
  BAR();
  __builtin_amdgcn_sched_barrier(0);
  if constexpr (s + 1 < 8) c1_read<s + 1>(c, nxt);
  const bf16x8 ones = {0x3f80, 0x3f80, 0x3f80, 0x3f80, 0x3f80, 0x3f80, 0x3f80, 0x3f80};
#pragma unroll
  for (int ks = 0; ks < 4; ++ks) {
#pragma unroll
    for (int m = 0; m < 2; ++m)
#pragma unroll
      for (int n = 0; n < 2; ++n) a.acc[m][n] = __builtin_amdgcn_mfma_f32_16x16x32_bf16(cur.pf[ks][m], cur.qf[ks][n], a.acc[m][n], 0, 0, 0);
    a.s1 = __builtin_amdgcn_mfma_f32_16x16x32_bf16(cur.pf[ks][0], ones, a.s1, 0, 0, 0);
    a.s2 = __builtin_amdgcn_mfma_f32_16x16x32_bf16(cur.pf[ks][0], cur.pf[ks][0], a.s2, 0, 0, 0);
    if constexpr (s < 4) {
#pragma unroll
      for (int n = 0; n < 2; ++n) sw[n] = __builtin_amdgcn_mfma_f32_16x16x32_bf16(ones, cur.qf[ks][n], sw[n], 0, 0, 0);
    }
  }
  if constexpr (s + 1 < 8) {
#pragma unroll
    for (int i = 0; i < 16; ++i) { __builtin_amdgcn_sched_group_barrier(0x100, 1, 0); __builtin_amdgcn_sched_group_barrier(0x8, 1, 0); }
  }
  __builtin_amdgcn_sched_barrier(0);
}

struct L2Ctx { const char* pbase; const char* qbase; unsigned voff[8]; unsigned lbase; };
#define DMA8(POL) asm volatile("s_mov_b32 %0, m0\n\ts_mov_b32 m0, %10\n\ts_nop 0\n\tglobal_load_lds_dwordx4 %1, %9" POL "\n\t" \
    "s_add_u32 m0, m0, 0x400\n\ts_nop 0\n\tglobal_load_lds_dwordx4 %2, %9" POL "\n\t" \
    "s_add_u32 m0, m0, 0x400\n\ts_nop 0\n\tglobal_load_lds_dwordx4 %3, %9" POL "\n\t" \
    "s_add_u32 m0, m0, 0x400\n\ts_nop 0\n\tglobal_load_lds_dwordx4 %4, %9" POL "\n\t" \
    "s_add_u32 m0, m0, 0x400\n\ts_nop 0\n\tglobal_load_lds_dwordx4 %5, %9" POL "\n\t" \
    "s_add_u32 m0, m0, 0x400\n\ts_nop 0\n\tglobal_load_lds_dwordx4 %6, %9" POL "\n\t" \
    "s_add_u32 m0, m0, 0x400\n\ts_nop 0\n\tglobal_load_lds_dwordx4 %7, %9" POL "\n\t" \
    "s_add_u32 m0, m0, 0x400\n\ts_nop 0\n\tglobal_load_lds_dwordx4 %8, %9" POL "\n\t" \
    "s_mov_b32 m0, %0" : "=&s"(keep) : "v"(c.voff[0]), "v"(c.voff[1]), "v"(c.voff[2]), "v"(c.voff[3]), "v"(c.voff[4]), "v"(c.voff[5]), \
    "v"(c.voff[6]), "v"(c.voff[7]), "s"(ub), "s"(dst) : "memory", "scc")
template <int T> __device__ __forceinline__ void l2_issueP(const L2Ctx& c) {
  const char* ub = c.pbase + koff(T) * 2;
  const unsigned dst = c.lbase + ring_p(T);
  unsigned keep;
  DMA8(" sc1");
}
template <int T> __device__ __forceinline__ void l2_issueQ(const L2Ctx& c) {
  const char* ub = c.qbase + koff(T) * 2;
  const unsigned dst = c.lbase + ring_q(T);
  unsigned keep;
  DMA8("");
}
constexpr int l2_cnt(int s) { return s < 5 ? 8 : 16; }
constexpr int l2_younger(int T) { int up = T + 4 < 15 ? T + 4 : 15; int n = 0; for (int s = T + 2; s <= up; ++s) n += l2_cnt(s); return n; }
template <int T> __device__ __forceinline__ void l2_step(const L2Ctx& c) {
  WAIT_V(l2_younger(T));
  BAR();
  if constexpr (T + 5 < 16) { l2_issueP<T + 5>(c); l2_issueQ<T + 5>(c); }
}
template <int... Ts> __device__ __forceinline__ void l2_steps(const L2Ctx& c, std::integer_sequence<int, Ts...>) { (l2_step<Ts>(c), ...); }

__device__ __forceinline__ u32x4 widen16(u32x2 a, u32x2 c) {
  const auto s0 = __builtin_amdgcn_permlane16_swap(a[0], c[0], false, false);
  const auto s1 = __builtin_amdgcn_permlane16_swap(a[1], c[1], false, false);
  return (u32x4){s0[0], s1[0], s0[1], s1[1]};
}

__device__ __forceinline__ void glds16_asm(const void* gsrc, unsigned lds_dst) {
  unsigned keep;
  asm volatile("s_mov_b32 %0, m0\n\ts_mov_b32 m0, %2\n\ts_nop 0\n\tglobal_load_lds_dwordx4 %1, off\n\ts_mov_b32 m0, %0" : "=&s"(keep) : "v"(gsrc), "s"(lds_dst) : "memory");
}

__device__ __forceinline__ void poll_words(const unsigned* p, unsigned tag) {
  for (int it = 0; it < SPIN_MAX; ++it) {
    const unsigned v = __hip_atomic_load((gu32*)p, RLX_AGENT);
    if (__all(v == tag)) break;
    __builtin_amdgcn_s_sleep(1);
  }
  __builtin_amdgcn_fence(__ATOMIC_ACQUIRE, "wavefront");
}

template <class R>
__device__ __forceinline__ void load_h(const u32x4 xv, R rs, int t, int l, float (&h)[8]) {
  const unsigned o = (unsigned)(t * D + l * 8) * 2;
  const u32x4 y0 = __builtin_amdgcn_raw_buffer_load_b128(rs, WS_Y + o, 0, SC1);
  const u32x4 y1 = __builtin_amdgcn_raw_buffer_load_b128(rs, WS_Y + 1u * S * D * 2 + o, 0, SC1);
  const u32x4 y2 = __builtin_amdgcn_raw_buffer_load_b128(rs, WS_Y + 2u * S * D * 2 + o, 0, SC1);
  const u32x4 y3 = __builtin_amdgcn_raw_buffer_load_b128(rs, WS_Y + 3u * S * D * 2 + o, 0, SC1);
#pragma unroll
  for (int e = 0; e < 4; ++e) {
    const unsigned yv = pack2((bflo(y0[e]) + bflo(y1[e])) - (bflo(y2[e]) + bflo(y3[e])), (bfhi(y0[e]) + bfhi(y1[e])) - (bfhi(y2[e]) + bfhi(y3[e])));
    const unsigned hv = pack2(bflo(xv[e]) + bflo(yv), bfhi(xv[e]) + bfhi(yv));
    h[2 * e] = bflo(hv); h[2 * e + 1] = bfhi(hv);
  }
}
__device__ __forceinline__ u32x4 layernorm8(const float (&h)[8]) {
  const float pv = __builtin_bit_cast(float, __builtin_amdgcn_readfirstlane(__builtin_bit_cast(int, h[0])));
  float s = 0.f, s2 = 0.f, v = 0.f, v2 = 0.f;
#pragma unroll
  for (int e = 0; e < 4; ++e) { const float a = h[2 * e] - pv, c = h[2 * e + 1] - pv; s += a; s2 += c; v += a * a; v2 += c * c; }
  const float md = wave_sum(s + s2) * (1.0f / D);
  v = fmaxf(wave_sum(v + v2) - md * md * (float)D, 0.f);
  const float mean = pv + md;
  const float mb = rbf(mean);
  const float sd = rbf(sqrtf(rbf(rbf(v * (1.0f / D)) + 1e-5f)));
  const float inv = 1.0f / sd;
  u32x4 o;
#pragma unroll
  for (int e = 0; e < 4; ++e) {
    const unsigned d = pack2(h[2 * e] - mb, h[2 * e + 1] - mb);
    const float a = bflo(d), c = bfhi(d);
    float qa = a * inv, qc = c * inv;
    qa = fmaf(fmaf(-qa, sd, a), inv, qa); qc = fmaf(fmaf(-qc, sd, c), inv, qc);
    o[e] = pack2(qa, qc);
  }
  return o;
}
__device__ __forceinline__ int key_row(unsigned key) { return (int)((0xffffu - (key & 0xffffu)) & (unsigned)(S - 1)); }

__global__ __launch_bounds__(512) void fused(const u16* __restrict__ x, const u16* __restrict__ ws_re, const u16* __restrict__ ws_im,
                                             const u16* __restrict__ wd_re, const u16* __restrict__ wd_im, const u16* __restrict__ w_r,
                                             const u16* __restrict__ w_gate, const u16* __restrict__ w_up, const u16* __restrict__ w_down,
                                             u16* __restrict__ out, char* __restrict__ ws) {
  __shared__ __attribute__((aligned(1024))) char lds[163840];
  const int b = blockIdx.x, xcd = b & 7, j = b >> 3;
  const int cc = xcd >> 2, et = j & 3, tt = (xcd & 3) * 4 + ((j >> 2) & 3), kh = j >> 4;
  const int g1 = (cc * 4 + et) * 2 + kh;
  const int e1 = cc * 8 + et * 2 + (tt & 1), s1 = kh * 8 + (tt >> 1);
  const int tid = threadIdx.x, w = __builtin_amdgcn_readfirstlane(tid >> 6), l = tid & 63, fr = l & 15, q = l >> 4;
  const u16* wsb = cc ? ws_im : ws_re;
  const u16* wdb = cc ? wd_im : wd_re;
  const bool down = j >= 28;
  const int fb = xcd * 28 + j, db = xcd * 4 + (j - 28);
  const auto rs = __builtin_amdgcn_make_buffer_rsrc(ws, 0, WS_END, 0x00020000);
  unsigned tag;
  unsigned long long code_lo; unsigned code_len;
  asm volatile("s_getpc_b64 %0\n.Lfused_code_lo:\n\ts_mov_b32 %1, .Lfused_code_hi-.Lfused_code_lo" : "=s"(code_lo), "=s"(code_len));

  const unsigned lbase = (unsigned)(uintptr_t)(LAS char*)lds;
  {
    const int row = w * 8 + (l >> 3);
    const size_t o = (size_t)row * D + (((l & 7) ^ ((row >> 1) & 7)) * 8);
    const u16* xp = x + (size_t)s1 * 128 * D + o;
    const u16* wp_ = wdb + (size_t)(e1 & 7) * 64 * D + o;
#pragma unroll
    for (int k = 0; k < 4; ++k) {
      glds16_asm(xp + k * 64, lbase + x1slot(k >> 1) + (k & 1) * 8192 + w * 1024);
      glds16_asm(wp_ + k * 64, lbase + k * 8192 + w * 1024);
    }
  }
  if (w < 4) {
    const int wp = w >> 1, wq = w & 1;
    unsigned r0 = 0;
    {
      const int row = tid >> 3, ln = tid & 7;
      const int xs_tile = ((j >> 3) & 1) * 8 + (xcd & 3) * 2 + ((j >> 2) & 1);
      const u16* p = j < 16 ? x + (size_t)(xs_tile * 128 + (j & 3) * 32 + row) * D + ln * 64 : wdb + (size_t)((j - 16) * 32 + row) * D + ln * 64;
      TOUCH(r0, p);
    }
    {
      C1Ctx c; c.lds = lds;
#pragma unroll
      for (int ks = 0; ks < 2; ++ks) {
#pragma unroll
        for (int m = 0; m < 2; ++m) { const int row = wp * 32 + (m ^ wq) * 16 + fr; c.poff[m][ks] = row * 128 + (((ks * 4 + q) ^ ((row >> 1) & 7)) * 16); }
#pragma unroll
        for (int n = 0; n < 2; ++n) { const int row = wq * 32 + n * 16 + fr; c.qoff[n][ks] = row * 128 + (((ks * 4 + q) ^ ((row >> 1) & 7)) * 16); }
      }
      f32x4 sw[2] = {};
      Fr1 f0, f1;
      auto epilogue = [&](const Acc1& a, int pass) {
        float2* rst = (float2*)(lds + L_RST) + pass * 64;
        if ((fr >> 2) == q) {
          const int jj = fr & 3;
          const float s1v = jj == 0 ? a.s1[0] : jj == 1 ? a.s1[1] : jj == 2 ? a.s1[2] : a.s1[3];
          const float s2v = jj == 0 ? a.s2[0] : jj == 1 ? a.s2[1] : jj == 2 ? a.s2[2] : a.s2[3];
          const float mean = s1v * (1.0f / D);
          const float var = fmaxf(s2v * (1.0f / D) - mean * mean, 0.f);
          const float sd = rbf(sqrtf(rbf(rbf(var) + 1e-5f)));
          rst[wp * 32 + wq * 16 + fr] = make_float2(rbf(mean), 1.0f / sd);
        }
        WAIT_L(0);
        BAR();
        u32x2 pk[2][2];
#pragma unroll
        for (int m = 0; m < 2; ++m) {
          const int p0 = wp * 32 + (m ^ wq) * 16 + q * 4;
          const f32x4 sa = *(const f32x4*)(rst + p0), sb = *(const f32x4*)(rst + p0 + 2);
#pragma unroll
          for (int n = 0; n < 2; ++n) {
            const float swv = sw[n][0];
            const float v0 = (a.acc[m][n][0] - sa[0] * swv) * sa[1], v1 = (a.acc[m][n][1] - sa[2] * swv) * sa[3];
            const float v2 = (a.acc[m][n][2] - sb[0] * swv) * sb[1], v3 = (a.acc[m][n][3] - sb[2] * swv) * sb[3];
            pk[m][n] = (u32x2){pack2(v0, v1), pack2(v2, v3)};
          }
        }
        const int scol = s1 * 128 + pass * 64 + wp * 32 + ((q & 1) ^ wq) * 16 + (q >> 1) * 8;
#pragma unroll
        for (int n = 0; n < 2; ++n)
          __builtin_amdgcn_raw_buffer_store_b128(widen16(pk[0][n], pk[1][n]), rs, WS_ABT + (unsigned)((e1 * 64 + wq * 32 + n * 16 + fr) * S + scol) * 2, 0, SC1);
      };
      WAIT_V(5);
      BAR();
      c1_read<0>(c, f0);
      WAIT_V(1);
      {
        Acc1 a = {};
        c1_step<0>(c, a, sw, f0, f1); c1_step<1>(c, a, sw, f1, f0); c1_step<2>(c, a, sw, f0, f1); c1_step<3>(c, a, sw, f1, f0);
        epilogue(a, 0);
      }
      {
        Acc1 a = {};
        c1_step<4>(c, a, sw, f0, f1); c1_step<5>(c, a, sw, f1, f0); c1_step<6>(c, a, sw, f0, f1); c1_step<7>(c, a, sw, f1, f0);
        epilogue(a, 1);
      }
      tag = __builtin_amdgcn_readfirstlane(*(const unsigned*)(lds + L_TAG));
      WAIT_V(0);
      KEEP(r0);
      BAR();
    }
    C2Ctx c; c.lds = lds;
#pragma unroll
    for (int ks = 0; ks < 2; ++ks) {
#pragma unroll
      for (int m = 0; m < 4; ++m) { const int row = wp * 64 + m * 16 + fr; c.poff[m][ks] = row * 128 + (((ks * 4 + q) ^ ((row >> 1) & 7)) * 16); }
#pragma unroll
      for (int n = 0; n < 4; ++n) { const int row = wq * 64 + n * 16 + fr; c.qoff[n][ks] = row * 128 + (((ks * 4 + q) ^ ((row >> 1) & 7)) * 16); }
    }
    f32x4 acc[4][4] = {};
    Fr2 f0, f1;
    BAR();
    c2_read<0>(c, f0);
    c2_steps(c, acc, f0, f1, std::make_integer_sequence<int, 15>{});
    {
      const unsigned yo = WS_Y + (unsigned)(cc * 2 + kh) * S * D * 2;
      const int chunk = (q >> 1) + 2 * (q & 1);
#pragma unroll
      for (int n = 0; n < 4; ++n) {
#pragma unroll
        for (int ks = 0; ks < 2; ++ks)
#pragma unroll
          for (int m = 0; m < 4; ++m) acc[m][n] = __builtin_amdgcn_mfma_f32_16x16x32_bf16(f1.pf[ks][m], f1.qf[ks][n], acc[m][n], 0, 0, 0);
#pragma unroll
        for (int mp = 0; mp < 2; ++mp) {
          const u32x2 a = {pack2(acc[2 * mp][n][0], acc[2 * mp][n][1]), pack2(acc[2 * mp][n][2], acc[2 * mp][n][3])};
          const u32x2 c2 = {pack2(acc[2 * mp + 1][n][0], acc[2 * mp + 1][n][1]), pack2(acc[2 * mp + 1][n][2], acc[2 * mp + 1][n][3])};
          const int row = wq * 64 + n * 16 + fr;
          __builtin_amdgcn_raw_buffer_store_b128(widen16(a, c2), rs, yo + (unsigned)((tt * 128 + row) * D + et * 128 + wp * 64 + mp * 32 + chunk * 8) * 2, 0, SC1);
        }
      }
    }
    WAIT_V(0);
  } else if (w < 6) {
    const int lw = w - 4;
    const int r8 = l >> 3, ch = l & 7;
    {
      L1Ctx c; c.lds = lds; c.w = lw;
      const u16* P = x + (size_t)s1 * 128 * D;
      const u16* Q = wdb + (size_t)(e1 & 7) * 64 * D;
#pragma unroll
      for (int i = 0; i < 4; ++i) {
        const int row = (lw * 4 + i) * 8 + r8;
        const size_t o = (size_t)row * D + ((ch ^ ((row >> 1) & 7)) * 8);
        c.xsrc[i] = P + o; c.wsrc[i] = Q + o;
      }
      l1_issue<2>(c);
      WAIT_V(20); BAR();
      WAIT_V(16); BAR();
      l1_issue<3>(c);
      WAIT_V(16); BAR();
      l1_issue<4>(c); l1_issue<5>(c);
      WAIT_V(16); BAR();
      l1_issue<6>(c); l1_issue<7>(c);
      WAIT_V(24); BAR();
      BAR();
      WAIT_V(16); BAR();
      WAIT_V(8); BAR();
      WAIT_V(0); BAR();
    }
    L2Ctx c2;
    c2.lbase = lbase + lw * 8192;
    {
      c2.pbase = (const char*)((const u16*)(ws + WS_ABT) + (size_t)(cc * 512 + et * 128) * S + kh * 1024);
      c2.qbase = (const char*)(wsb + (size_t)tt * 128 * S + kh * 1024);
#pragma unroll
      for (int i = 0; i < 8; ++i) {
        const int row = (lw * 8 + i) * 8 + r8;
        c2.voff[i] = (unsigned)(row * S + ((ch ^ ((row >> 1) & 7)) * 8)) * 2u;
      }
    }
    tag = __builtin_amdgcn_readfirstlane(*(const unsigned*)(lds + L_TAG));
    BAR();
    BAR();
    poll_words((const unsigned*)(ws + WS_F1) + g1 * 32 + fr, tag);
    l2_issueP<0>(c2); l2_issueP<1>(c2); l2_issueP<2>(c2);
    BAR();
    WAIT_V(16);
    BAR();
    l2_issueP<3>(c2); l2_issueP<4>(c2);
    l2_steps(c2, std::make_integer_sequence<int, 15>{});
  } else if (w == 6) {
    const int r8 = l >> 3, ch = l & 7;
    const unsigned ep = __hip_atomic_load((gu32*)(ws + WS_EP), RLX_AGENT);
    const u16* Q = wsb + (size_t)tt * 128 * S + kh * 1024;
    const u16* qa = Q + (size_t)r8 * S + ((ch ^ (r8 >> 1)) * 8);
    const u16* qb = Q + (size_t)(8 + r8) * S + ((ch ^ (4 + (r8 >> 1))) * 8);
    auto issueQ = [&](int T) {
#pragma unroll
      for (int i = 0; i < 8; ++i) {
        GLDS16(qa + (size_t)i * 16 * S + T * 128, lds + L_QR + ((T + 3) % 5) * 16384 + (2 * i) * 1024, 0);
        GLDS16(qb + (size_t)i * 16 * S + T * 128, lds + L_QR + ((T + 3) % 5) * 16384 + (2 * i + 1) * 1024, 0);
      }
    };
    WAIT_V(5);
    BAR();
    WAIT_V(1);
    BAR();
    tag = __builtin_amdgcn_readfirstlane(ep * 1664525u + 1013904223u);
    *(unsigned*)(lds + L_TAG) = tag;
    WAIT_L(0);
    BAR(); BAR(); BAR();
    BAR();
    BAR(); issueQ(0);
    BAR(); issueQ(1);
    BAR(); issueQ(2);
    BAR(); BAR();
    BAR(); issueQ(3); issueQ(4);
    WAIT_V(32);
    BAR();
    const unsigned* fB = (const unsigned*)(ws + WS_F1) + 512 + g1 * 32 + fr;
    BAR(); BAR();
    const unsigned vB = __hip_atomic_load((gu32*)fB, RLX_AGENT);
    WAIT_V(17);
    BAR();
    if (!__all(vB == tag)) poll_words(fB, tag);
    WAIT_V(0);
    BAR();
#pragma unroll
    for (int T = 4; T < 15; ++T) BAR();
  } else {
    unsigned r0 = 0, r1 = 0;
    auto ffn_ptr = [&](int i) -> const char* {
      const int li = j * 192 + i * 64 + l;
      if (li < 4096) { const int mrow = (li & 2047) >> 2, c4 = li & 3; return (const char*)((li < 2048 ? w_gate : w_up) + (size_t)mrow * F + xcd * 224 + c4 * 64); }
      const int rr = li - 4096;
      return (const char*)(w_down + (size_t)(rr < F ? rr : 0) * D + xcd * 64);
    };
    {
      const unsigned off = (unsigned)(j * 16 + fr) * 128u;
      const char* cp = (const char*)code_lo + ((code_len <= 65536u && off < code_len) ? off : 0u);
      TOUCH(r0, cp);
    }
    const char* fp0 = ffn_ptr(0); const char* fp1 = ffn_ptr(1); const char* fp2 = ffn_ptr(2);
    WAIT_V(5);
    BAR();
    WAIT_V(1);
    BAR();
    BAR(); TOUCH(r1, fp0);
    tag = __builtin_amdgcn_readfirstlane(*(const unsigned*)(lds + L_TAG));
    BAR(); TOUCH(r1, fp1);
    BAR(); TOUCH(r1, fp2);
    BAR();
    BAR(); BAR(); BAR();
    BAR();
    if (l == 0) __hip_atomic_store((gu32*)(ws + WS_F1) + g1 * 32 + tt, tag, RLX_AGENT);
    BAR();
    BAR();
    if (l == 0) __hip_atomic_store((gu32*)(ws + WS_F1) + 512 + g1 * 32 + tt, tag, RLX_AGENT);
#pragma unroll
    for (int T = 0; T < 16; ++T) BAR();
    WAIT_V(0);
    KEEP(r0); KEEP(r1);
  }
  const int t_own = tt * 128 + g1 * 8 + w;
  const u32x4 wr = *(const u32x4*)(w_r + l * 8);
  const u32x4 x_own = *(const u32x4*)(x + (size_t)t_own * D + l * 8);
  BAR();
  if (tid == 0) __hip_atomic_store((gu32*)(ws + WS_F2) + tt * 32 + g1, tag, RLX_AGENT);
  if (w == 0) poll_words((const unsigned*)(ws + WS_F2) + tt * 32 + fr, tag);
  BAR();
  unsigned* misc = (unsigned*)(lds + L_MISC);
  u32x4 own;
  {
    float h[8];
    load_h(x_own, rs, t_own, l, h);
    const u32x4 hn = layernorm8(h);
    float r = 0.f, r2 = 0.f;
#pragma unroll
    for (int e = 0; e < 4; ++e) { r += bflo(hn[e]) * bflo(wr[e]); r2 += bfhi(hn[e]) * bfhi(wr[e]); }
    r = wave_sum(r + r2);
    const unsigned rb = f2bf(r);
    const unsigned ord = (rb & 0x8000u) ? (~rb & 0xffffu) : (rb | 0x8000u);
    if (l == 0) { ((u16*)misc)[w] = (u16)ord; if (w == 0) misc[8] = 0; }
    WAIT_L(0);
    BAR();
    if (tid < 4) __hip_atomic_store((gu64*)(ws + WS_GR) + (tt * 16 + g1) * 4 + tid, ((unsigned long long)tag << 32) | misc[tid], RLX_AGENT);
    const unsigned ov = ((const u16*)misc)[l & 7];
    if (!__any(ov > ord || (ov == ord && (l & 7) < w))) {
      __builtin_amdgcn_raw_buffer_store_b128((u32x4){hn[0], tag, hn[1], tag}, rs, WS_HN + (unsigned)t_own * 2048 + l * 32, 0, SC1);
      __builtin_amdgcn_raw_buffer_store_b128((u32x4){hn[2], tag, hn[3], tag}, rs, WS_HN + (unsigned)t_own * 2048 + l * 32 + 16, 0, SC1);
    }
#pragma unroll
    for (int e = 0; e < 4; ++e) own[e] = pack2(h[2 * e] + bflo(hn[e]), h[2 * e + 1] + bfhi(hn[e]));
  }
  {
    const u16* wsrc = down ? w_down + (size_t)(l >> 1) * D + db * 16 + (l & 1) * 8 : ((l & 1) ? w_up : w_gate) + (size_t)(l >> 1) * F + fb * 8;
    if (down) {
#pragma unroll
      for (int i = 0; i < 7; ++i) glds16_asm(wsrc + (size_t)(w * 7 + i) * 32 * D, lbase + L_W + (w * 7 + i) * 1024);
    } else {
#pragma unroll
      for (int i = 0; i < 2; ++i) glds16_asm(wsrc + (size_t)(w * 2 + i) * 32 * F, lbase + L_W + (w * 2 + i) * 1024);
    }
  }
  unsigned key;
  bool sel_own;
  bool fast = false;
  if (!down) {
    unsigned kk[4];
    {
      u32x4 a;
      for (int it = 0; it < SPIN_MAX; ++it) {
        asm volatile("" ::: "memory");
        a = __builtin_amdgcn_raw_buffer_load_b128(rs, WS_GR + (unsigned)tid * 16, 0, SC1);
        if (__all(a[1] == tag && a[3] == tag)) break;
        __builtin_amdgcn_s_sleep(1);
      }
      __builtin_amdgcn_fence(__ATOMIC_ACQUIRE, "wavefront");
      const unsigned ib = 0xffffu - (unsigned)tid * 4;
      kk[0] = (a[0] << 16) | ib; kk[1] = (a[0] & 0xffff0000u) | (ib - 1); kk[2] = (a[2] << 16) | (ib - 2); kk[3] = (a[2] & 0xffff0000u) | (ib - 3);
    }
    {
      const unsigned m = wave_max_u32(umax(umax(kk[0], kk[1]), umax(kk[2], kk[3])));
      if (l == 0) misc[16 + w] = m;
    }
    WAIT_L(0);
    BAR();
    if (b == 0 && tid == 0) __hip_atomic_store((gu32*)(ws + WS_EP), tag, RLX_AGENT);
    {
      const u32x4 ma = *(const u32x4*)(misc + 16), mb2 = *(const u32x4*)(misc + 20);
      const unsigned lo = min(min(min(ma[0], ma[1]), min(ma[2], ma[3])), min(min(mb2[0], mb2[1]), min(mb2[2], mb2[3])));
      if (l < 16) misc[64 + w * 16 + l] = 0u;
      unsigned base = 0;
#pragma unroll
      for (int e = 0; e < 4; ++e) {
        const unsigned long long bm = __ballot(kk[e] >= lo);
        const unsigned pos = base + __builtin_amdgcn_mbcnt_hi((unsigned)(bm >> 32), __builtin_amdgcn_mbcnt_lo((unsigned)bm, 0u));
        if (kk[e] >= lo && pos < 16) misc[64 + w * 16 + pos] = kk[e];
        base += (unsigned)__builtin_popcountll(bm);
      }
      if (base > 16 && l == 0) misc[8] = 1u;
    }
    WAIT_L(0);
    BAR();
    const unsigned ovf = __builtin_amdgcn_readfirstlane(misc[8]);
    if (ovf == 0) {
      const unsigned cv = misc[64 + w * 16 + (l >> 2)];
      int rank = 0;
#pragma unroll
      for (int i = 0; i < 8; ++i) {
        const u32x4 c4 = *(const u32x4*)(misc + 64 + (l & 3) * 32 + i * 4);
#pragma unroll
        for (int e = 0; e < 4; ++e) rank += (c4[e] > cv) ? 1 : 0;
      }
      rank += (int)dpp_u<0xB1, 0xf>((unsigned)rank);
      rank += (int)dpp_u<0x4E, 0xf>((unsigned)rank);
      if (cv != 0u && rank < 8 && (l & 3) == 0) misc[32 + rank] = cv;
      WAIT_L(0);
      BAR();
      const unsigned sv = misc[32 + (l & 7)];
      if (b < 8 && tid < 8) __hip_atomic_store((gu64*)(ws + WS_SEL) + b * 32 + tid, ((unsigned long long)tag << 32) | sv, RLX_AGENT);
      key = (unsigned)__builtin_amdgcn_readlane((int)sv, w);
      sel_own = __any(key_row(sv) == t_own);
      fast = !__any(sv > key && (key_row(sv) >> 3) == (key_row(key) >> 3));
    } else {
      unsigned mine = 0;
#pragma unroll 1
      for (int it = 0; it < 8; ++it) {
        const unsigned m = wave_max_u32(umax(umax(kk[0], kk[1]), umax(kk[2], kk[3])));
#pragma unroll
        for (int e = 0; e < 4; ++e) kk[e] = (kk[e] == m) ? 0u : kk[e];
        if (l == it) mine = m;
      }
      if (l < 8) misc[192 + w * 8 + l] = mine;
      WAIT_L(0);
      BAR();
      unsigned cv = misc[192 + l];
      key = 0; sel_own = false;
#pragma unroll 1
      for (int it = 0; it < 8; ++it) {
        const unsigned m = wave_max_u32(cv);
        if (it == w) key = m;
        sel_own |= key_row(m) == t_own;
        cv = (cv == m) ? 0u : cv;
      }
      if (b < 8 && l == 0) __hip_atomic_store((gu64*)(ws + WS_SEL) + b * 32 + w, ((unsigned long long)tag << 32) | key, RLX_AGENT);
    }
  } else {
    if (w == 0) {
      unsigned long long g;
      for (int it = 0; it < SPIN_MAX; ++it) {
        g = __hip_atomic_load((gu64*)(ws + WS_SEL) + xcd * 32 + (l & 7), RLX_AGENT);
        if (__all((unsigned)(g >> 32) == tag)) break;
        __builtin_amdgcn_s_sleep(1);
      }
      if (l < 8) misc[32 + l] = (unsigned)g;
      WAIT_L(0);
    }
    BAR();
    const unsigned sv = misc[32 + (l & 7)];
    key = (unsigned)__builtin_amdgcn_readlane((int)sv, w);
    sel_own = __any(key_row(sv) == t_own);
    fast = !__any(sv > key && (key_row(sv) >> 3) == (key_row(key) >> 3));
  }
  const int t_sel = key_row(key);
  bf16x8 bw[16];
  if (!down && w == 0) {
    const char* bp = lds + L_W + (q * 8 + (fr >> 2)) * 32 + (fr & 3) * 8;
#pragma unroll
    for (int ks = 0; ks < 16; ++ks) {
      const v4i16_t g0 = __builtin_amdgcn_ds_read_tr16_b64_v4i16((LAS v4i16_t*)(bp + ks * 1024));
      const v4i16_t g1v = __builtin_amdgcn_ds_read_tr16_b64_v4i16((LAS v4i16_t*)(bp + ks * 1024 + 128));
      bw[ks] = (bf16x8){g0[0], g0[1], g0[2], g0[3], g1v[0], g1v[1], g1v[2], g1v[3]};
    }
  }
  {
    float h[8];
    u32x4 hn;
    if (down || !fast) {
      const u32x4 xs = *(const u32x4*)(x + (size_t)t_sel * D + l * 8);
      load_h(xs, rs, t_sel, l, h);
    }
    if (fast) {
      u32x4 a, c;
      for (int it = 0; it < SPIN_MAX; ++it) {
        asm volatile("" ::: "memory");
        a = __builtin_amdgcn_raw_buffer_load_b128(rs, WS_HN + (unsigned)t_sel * 2048 + l * 32, 0, SC1);
        c = __builtin_amdgcn_raw_buffer_load_b128(rs, WS_HN + (unsigned)t_sel * 2048 + l * 32 + 16, 0, SC1);
        if (__all(a[1] == tag && a[3] == tag && c[1] == tag && c[3] == tag)) break;
        __builtin_amdgcn_s_sleep(1);
      }
      hn = (u32x4){a[0], a[2], c[0], c[2]};
    } else {
      hn = layernorm8(h);
    }
    if (!down) {
      *(u32x4*)(lds + L_XS + w * XS_STRIDE + l * 16) = hn;
    } else {
      if ((l >> 1) == db) {
        float2* hh = (float2*)(lds + L_HH) + w * 16 + (l & 1) * 8;
#pragma unroll
        for (int e = 0; e < 4; ++e) { hh[2 * e] = make_float2(h[2 * e], bflo(hn[e])); hh[2 * e + 1] = make_float2(h[2 * e + 1], bfhi(hn[e])); }
      }
      if (l == 0) {
        const unsigned ord = key >> 16;
        const unsigned rb = (ord & 0x8000u) ? (ord & 0x7fffu) : (~ord & 0xffffu);
        misc[24 + w] = key;
        misc[40 + w] = __float_as_uint(rbf(1.0f / (1.0f + expf(-bf2f(rb)))));
      }
    }
    if (!sel_own) *(u32x4*)(out + (size_t)t_own * D + l * 8) = own;
  }
  if (!down) {
    WAIT_L(0);
    BAR();
    if (w == 0) {
      f32x4 a4[2] = {};
      const char* ap = lds + L_XS + (fr & 7) * XS_STRIDE + q * 16;
#pragma unroll
      for (int ks = 0; ks < 16; ++ks) {
        const bf16x8 a = *(const bf16x8*)(ap + ks * 64);
        a4[ks & 1] = __builtin_amdgcn_mfma_f32_16x16x32_bf16(a, bw[ks], a4[ks & 1], 0, 0, 0);
      }
      const f32x4 sum = a4[0] + a4[1];
      const bool hi = fr >= 8;
      unsigned actb[2];
#pragma unroll
      for (int k = 0; k < 2; ++k) {
        const float send = hi ? sum[k] : sum[2 + k];
        const float recv = dpp_f<0x128, 0xf>(send);
        const float g = rbf(hi ? recv : sum[k]), u = rbf(hi ? sum[2 + k] : recv);
        const float sg = rbf(g / (1.0f + expf(-g)));
        actb[k] = f2bf(sg * u);
      }
      const unsigned n0 = dpp_u<0xB1, 0xf>(actb[0]), n1 = dpp_u<0xB1, 0xf>(actb[1]);
      const unsigned val = (fr & 1) ? (n1 | (actb[1] << 16)) : (actb[0] | (n0 << 16));
      const int tok = q * 4 + (hi ? 2 : 0) + (fr & 1);
      if (q < 2) __hip_atomic_store((gu64*)(ws + WS_GA) + fb * 32 + tok * 4 + ((fr & 7) >> 1), ((unsigned long long)tag << 32) | val, RLX_AGENT);
    }
  } else {
    float* red = (float*)(lds + L_RED);
    for (int it = 0; it < SPIN_MAX; ++it) {
      asm volatile("" ::: "memory");
      bool ok = true;
#pragma unroll
      for (int i = 0; i < 7; ++i) {
        const int rec = w * 28 + i * 4 + q;
        const u32x4 a = __builtin_amdgcn_raw_buffer_load_b128(rs, WS_GA + (unsigned)rec * 256 + fr * 16, 0, SC1);
        ok &= (a[1] == tag) & (a[3] == tag);
        *(u32x2*)(lds + L_AS + (fr >> 1) * AS_STRIDE + rec * 16 + (fr & 1) * 8) = (u32x2){a[0], a[2]};
      }
      if (__all(ok)) break;
      __builtin_amdgcn_s_sleep(1);
    }
    WAIT_L(0);
    f32x4 a4 = {};
#pragma unroll
    for (int i = 0; i < 7; ++i) {
      const int ks = w * 7 + i;
      const bf16x8 a = *(const bf16x8*)(lds + L_AS + (fr & 7) * AS_STRIDE + ks * 64 + q * 16);
      const int r0 = ks * 32 + q * 8 + (fr >> 2);
      const int coff = (fr & 3) * 8;
      const v4i16_t g0 = __builtin_amdgcn_ds_read_tr16_b64_v4i16((LAS v4i16_t*)(lds + L_W + r0 * 32 + coff));
      const v4i16_t g1v = __builtin_amdgcn_ds_read_tr16_b64_v4i16((LAS v4i16_t*)(lds + L_W + (r0 + 4) * 32 + coff));
      const bf16x8 bg = {g0[0], g0[1], g0[2], g0[3], g1v[0], g1v[1], g1v[2], g1v[3]};
      a4 = __builtin_amdgcn_mfma_f32_16x16x32_bf16(a, bg, a4, 0, 0, 0);
    }
    if (q < 2) {
#pragma unroll
      for (int jj = 0; jj < 4; ++jj) red[w * 128 + (q * 4 + jj) * 16 + fr] = a4[jj];
    }
    WAIT_L(0);
    BAR();
    if (tid < 128) {
      float s = 0.f;
#pragma unroll
      for (int ww = 0; ww < 8; ++ww) s += red[ww * 128 + tid];
      const int t = key_row(misc[24 + (tid >> 4)]);
      const float sig = __uint_as_float(misc[40 + (tid >> 4)]);
      const float upd = rbf(rbf(s) * sig);
      const float2 hh = ((const float2*)(lds + L_HH))[tid];
      out[(size_t)t * D + db * 16 + (tid & 15)] = (u16)f2bf(hh.x + rbf(hh.y + upd));
    }
  }
  asm volatile(".Lfused_code_hi:");
}

extern "C" void kernel_launch(void* const* d_in, const int* in_sizes, int n_in, void* d_out, int out_size, void* d_ws,
                              size_t ws_size, hipStream_t stream) {
  fused<<<256, 512, 0, stream>>>((const u16*)d_in[0], (const u16*)d_in[1], (const u16*)d_in[2], (const u16*)d_in[3], (const u16*)d_in[4],
                                 (const u16*)d_in[6], (const u16*)d_in[7], (const u16*)d_in[8], (const u16*)d_in[9], (u16*)d_out, (char*)d_ws);
}
```

```cpp
#include <hip/hip_runtime.h>
#include <hip/hip_bf16.h>
#include <stdint.h>
#include <utility>

typedef unsigned short u16;
typedef __attribute__((ext_vector_type(8))) short bf16x8;
typedef __attribute__((ext_vector_type(4))) short v4i16_t;
typedef __attribute__((ext_vector_type(4))) float f32x4;
typedef __attribute__((ext_vector_type(4))) unsigned u32x4;
typedef __attribute__((ext_vector_type(2))) unsigned u32x2;
#define GAS __attribute__((address_space(1)))
#define LAS __attribute__((address_space(3)))
typedef GAS unsigned gu32;
typedef GAS unsigned long long gu64;
#define RLX_AGENT __ATOMIC_RELAXED, __HIP_MEMORY_SCOPE_AGENT

constexpr int S = 2048, D = 512, F = 1792;

__device__ __forceinline__ float bf2f(unsigned v) { return __uint_as_float(v << 16); }
__device__ __forceinline__ float bflo(unsigned v) { return __uint_as_float(v << 16); }
__device__ __forceinline__ float bfhi(unsigned v) { return __uint_as_float(v & 0xffff0000u); }
__device__ __forceinline__ unsigned f2bf(float f) { __bf16 b = (__bf16)f; return (unsigned)__builtin_bit_cast(u16, b); }
__device__ __forceinline__ float rbf(float f) { return bf2f(f2bf(f)); }
__device__ __forceinline__ unsigned pack2(float lo, float hi) { return f2bf(lo) | (f2bf(hi) << 16); }

#define WAIT_V(n) asm volatile("s_waitcnt vmcnt(%0)" ::"n"(n) : "memory")
#define WAIT_L(n) asm volatile("s_waitcnt lgkmcnt(%0)" ::"n"(n) : "memory")
#define BAR() __builtin_amdgcn_s_barrier()
#define GLDS16(src, dst, aux) __builtin_amdgcn_global_load_lds((const unsigned*)(src), (unsigned*)(dst), 16, 0, aux)
#define TOUCH(reg, ptr) asm volatile("global_load_dword %0, %1, off" : "+v"(reg) : "v"(ptr) : "memory")
#define KEEP(reg) asm volatile("" ::"v"(reg))
constexpr int SC1 = 16;

template <int CTRL, int RM> __device__ __forceinline__ float dpp_f(float v) {
  return __builtin_bit_cast(float, __builtin_amdgcn_update_dpp(0, __builtin_bit_cast(int, v), CTRL, RM, 0xf, true));
}
__device__ __forceinline__ float wave_sum(float v) {
  v += dpp_f<0x111, 0xf>(v); v += dpp_f<0x112, 0xf>(v); v += dpp_f<0x114, 0xf>(v); v += dpp_f<0x118, 0xf>(v);
  v += dpp_f<0x142, 0xa>(v); v += dpp_f<0x143, 0xc>(v);
  return __builtin_bit_cast(float, __builtin_amdgcn_readlane(__builtin_bit_cast(int, v), 63));
}
template <int CTRL, int RM> __device__ __forceinline__ unsigned dpp_u(unsigned v) {
  return (unsigned)__builtin_amdgcn_update_dpp(0, (int)v, CTRL, RM, 0xf, true);
}
__device__ __forceinline__ unsigned umax(unsigned a, unsigned b) { return a > b ? a : b; }
__device__ __forceinline__ unsigned wave_max_u32(unsigned v) {
  v = umax(v, dpp_u<0x111, 0xf>(v)); v = umax(v, dpp_u<0x112, 0xf>(v)); v = umax(v, dpp_u<0x114, 0xf>(v)); v = umax(v, dpp_u<0x118, 0xf>(v));
  v = umax(v, dpp_u<0x142, 0xa>(v)); v = umax(v, dpp_u<0x143, 0xc>(v));
  return (unsigned)__builtin_amdgcn_readlane((int)v, 63);
}

constexpr unsigned WS_ABT = 0;
constexpr unsigned WS_Y = WS_ABT + 1024u * 2048 * 2;
constexpr unsigned WS_F1 = WS_Y + 4u * 2048 * 512 * 2;
constexpr unsigned WS_F2 = WS_F1 + 4096;
constexpr unsigned WS_GR = WS_F2 + 2048;
constexpr unsigned WS_GA = WS_GR + 1024 * 8;
constexpr unsigned WS_EP = WS_GA + 224 * 256;
constexpr unsigned WS_SEL = WS_EP + 256;
constexpr unsigned WS_HN = WS_SEL + 2048;
constexpr unsigned WS_END = WS_HN + 2048u * 2048;

constexpr int SPIN_MAX = 1 << 18;

constexpr int L_QR = 81920;
constexpr int L_RST = 65536;
constexpr int L_TAG = 66560;
constexpr int L_W = 16384;
constexpr int XS_STRIDE = 1024 + 16;
constexpr int L_XS = 81920;
constexpr int L_RED = L_XS + 8 * XS_STRIDE;
constexpr int L_MISC = L_RED + 8 * 128 * 4;
constexpr int L_HH = L_MISC + 1024;
constexpr int AS_STRIDE = F * 2 + 16;
constexpr int L_AS = L_HH + 1024;
static_assert(L_AS + 8 * AS_STRIDE <= 163840, "lds");

constexpr int ring_p(int T) { return (T % 5) * 16384; }
constexpr int ring_q(int T) { return L_QR + ((T + 3) % 5) * 16384; }
constexpr int koff(int T) { return T < 8 ? T * 128 : (T - 8) * 128 + 64; }
constexpr int x1slot(int s) { return L_QR + (s % 5) * 16384; }

struct C2Ctx { const char* lds; int poff[4][2]; int qoff[4][2]; };
struct Fr2 { bf16x8 pf[2][4], qf[2][4]; };
template <int T> __device__ __forceinline__ void c2_read(const C2Ctx& c, Fr2& f) {
#pragma unroll
  for (int ks = 0; ks < 2; ++ks) {
#pragma unroll
    for (int m = 0; m < 4; ++m) f.pf[ks][m] = *(const bf16x8*)(c.lds + ring_p(T) + c.poff[m][ks]);
#pragma unroll
    for (int n = 0; n < 4; ++n) f.qf[ks][n] = *(const bf16x8*)(c.lds + ring_q(T) + c.qoff[n][ks]);
  }
}
template <int T> __device__ __forceinline__ void c2_step(const C2Ctx& c, f32x4 (&acc)[4][4], Fr2& cur, Fr2& nxt) {
  WAIT_L(0);
  BAR();
  __builtin_amdgcn_sched_barrier(0);
  c2_read<T + 1>(c, nxt);
#pragma unroll
  for (int ks = 0; ks < 2; ++ks)
#pragma unroll
    for (int m = 0; m < 4; ++m)
#pragma unroll
      for (int n = 0; n < 4; ++n) acc[m][n] = __builtin_amdgcn_mfma_f32_16x16x32_bf16(cur.pf[ks][m], cur.qf[ks][n], acc[m][n], 0, 0, 0);
#pragma unroll
  for (int i = 0; i < 16; ++i) { __builtin_amdgcn_sched_group_barrier(0x100, 1, 0); __builtin_amdgcn_sched_group_barrier(0x8, 1, 0); }
  __builtin_amdgcn_sched_barrier(0);
}
template <int... Ts>
__device__ __forceinline__ void c2_steps(const C2Ctx& c, f32x4 (&acc)[4][4], Fr2& f0, Fr2& f1, std::integer_sequence<int, Ts...>) {
  ((Ts & 1 ? c2_step<Ts>(c, acc, f1, f0) : c2_step<Ts>(c, acc, f0, f1)), ...);
}

struct L1Ctx { const u16* xsrc[4]; const u16* wsrc[4]; char* lds; int w; };
template <int s> __device__ __forceinline__ void l1_issue(const L1Ctx& c) {
#pragma unroll
  for (int h = 0; h < 2; ++h) {
#pragma unroll
    for (int i = 0; i < 4; ++i) GLDS16(c.xsrc[i] + (size_t)(s >> 2) * 64 * D + (s & 3) * 128 + h * 64, c.lds + x1slot(s) + h * 8192 + (c.w * 4 + i) * 1024, 0);
    if constexpr (s < 4) {
#pragma unroll
      for (int i = 0; i < 4; ++i) GLDS16(c.wsrc[i] + s * 128 + h * 64, c.lds + (2 * s + h) * 8192 + (c.w * 4 + i) * 1024, 0);
    }
  }
}
struct Fr1 { bf16x8 pf[4][2], qf[4][2]; };
struct C1Ctx { const char* lds; int poff[2][2]; int qoff[2][2]; };
template <int s> __device__ __forceinline__ void c1_read(const C1Ctx& c, Fr1& f) {
#pragma unroll
  for (int ks = 0; ks < 4; ++ks) {
#pragma unroll
    for (int m = 0; m < 2; ++m) f.pf[ks][m] = *(const bf16x8*)(c.lds + x1slot(s) + (ks >> 1) * 8192 + c.poff[m][ks & 1]);
#pragma unroll
    for (int n = 0; n < 2; ++n) f.qf[ks][n] = *(const bf16x8*)(c.lds + (2 * (s & 3) + (ks >> 1)) * 8192 + c.qoff[n][ks & 1]);
  }
}
struct Acc1 { f32x4 acc[2][2], s1, s2; };
template <int s> __device__ __forceinline__ void c1_step(const C1Ctx& c, Acc1& a, f32x4 (&sw)[2], Fr1& cur, Fr1& nxt) {
  if constexpr (s == 7) WAIT_V(0);
  WAIT_L(0);
  BAR();
  __builtin_amdgcn_sched_barrier(0);
  if constexpr (s + 1 < 8) c1_read<s + 1>(c, nxt);
  const bf16x8 ones = {0x3f80, 0x3f80, 0x3f80, 0x3f80, 0x3f80, 0x3f80, 0x3f80, 0x3f80};
#pragma unroll
  for (int ks = 0; ks < 4; ++ks) {
#pragma unroll
    for (int m = 0; m < 2; ++m)
#pragma unroll
      for (int n = 0; n < 2; ++n) a.acc[m][n] = __builtin_amdgcn_mfma_f32_16x16x32_bf16(cur.pf[ks][m], cur.qf[ks][n], a.acc[m][n], 0, 0, 0);
    a.s1 = __builtin_amdgcn_mfma_f32_16x16x32_bf16(cur.pf[ks][0], ones, a.s1, 0, 0, 0);
    a.s2 = __builtin_amdgcn_mfma_f32_16x16x32_bf16(cur.pf[ks][0], cur.pf[ks][0], a.s2, 0, 0, 0);
    if constexpr (s < 4) {
#pragma unroll
      for (int n = 0; n < 2; ++n) sw[n] = __builtin_amdgcn_mfma_f32_16x16x32_bf16(ones, cur.qf[ks][n], sw[n], 0, 0, 0);
    }
  }
  if constexpr (s + 1 < 8) {
#pragma unroll
    for (int i = 0; i < 16; ++i) { __builtin_amdgcn_sched_group_barrier(0x100, 1, 0); __builtin_amdgcn_sched_group_barrier(0x8, 1, 0); }
  }
  __builtin_amdgcn_sched_barrier(0);
}

struct L2Ctx { const u16* psrc[8]; const u16* qsrc[8]; char* lds; int w; };
template <int T> __device__ __forceinline__ void l2_issueP(const L2Ctx& c) {
#pragma unroll
  for (int i = 0; i < 8; ++i) GLDS16(c.psrc[i] + koff(T), c.lds + ring_p(T) + (c.w * 8 + i) * 1024, SC1);
}
template <int T> __device__ __forceinline__ void l2_issueQ(const L2Ctx& c) {
#pragma unroll
  for (int i = 0; i < 8; ++i) GLDS16(c.qsrc[i] + koff(T), c.lds + ring_q(T) + (c.w * 8 + i) * 1024, 0);
}
constexpr int l2_cnt(int s) { return s < 5 ? 8 : 16; }
constexpr int l2_younger(int T) { int up = T + 4 < 15 ? T + 4 : 15; int n = 0; for (int s = T + 2; s <= up; ++s) n += l2_cnt(s); return n; }
template <int T> __device__ __forceinline__ void l2_step(const L2Ctx& c) {
  WAIT_V(l2_younger(T));
  BAR();
  if constexpr (T + 5 < 16) { l2_issueP<T + 5>(c); l2_issueQ<T + 5>(c); }
}
template <int... Ts> __device__ __forceinline__ void l2_steps(const L2Ctx& c, std::integer_sequence<int, Ts...>) { (l2_step<Ts>(c), ...); }

__device__ __forceinline__ u32x4 widen16(u32x2 a, u32x2 c) {
  const auto s0 = __builtin_amdgcn_permlane16_swap(a[0], c[0], false, false);
  const auto s1 = __builtin_amdgcn_permlane16_swap(a[1], c[1], false, false);
  return (u32x4){s0[0], s1[0], s0[1], s1[1]};
}

__device__ __forceinline__ void glds16_asm(const void* gsrc, unsigned lds_dst) {
  unsigned keep;
  asm volatile("s_mov_b32 %0, m0\n\ts_mov_b32 m0, %2\n\ts_nop 0\n\tglobal_load_lds_dwordx4 %1, off\n\ts_mov_b32 m0, %0" : "=&s"(keep) : "v"(gsrc), "s"(lds_dst) : "memory");
}

__device__ __forceinline__ void poll_words(const unsigned* p, unsigned tag) {
  for (int it = 0; it < SPIN_MAX; ++it) {
    const unsigned v = __hip_atomic_load((gu32*)p, RLX_AGENT);
    if (__all(v == tag)) break;
    __builtin_amdgcn_s_sleep(1);
  }
  __builtin_amdgcn_fence(__ATOMIC_ACQUIRE, "wavefront");
}

template <class R>
__device__ __forceinline__ void load_h(const u32x4 xv, R rs, int t, int l, float (&h)[8]) {
  const unsigned o = (unsigned)(t * D + l * 8) * 2;
  const u32x4 y0 = __builtin_amdgcn_raw_buffer_load_b128(rs, WS_Y + o, 0, SC1);
  const u32x4 y1 = __builtin_amdgcn_raw_buffer_load_b128(rs, WS_Y + 1u * S * D * 2 + o, 0, SC1);
  const u32x4 y2 = __builtin_amdgcn_raw_buffer_load_b128(rs, WS_Y + 2u * S * D * 2 + o, 0, SC1);
  const u32x4 y3 = __builtin_amdgcn_raw_buffer_load_b128(rs, WS_Y + 3u * S * D * 2 + o, 0, SC1);
#pragma unroll
  for (int e = 0; e < 4; ++e) {
    const unsigned yv = pack2((bflo(y0[e]) + bflo(y1[e])) - (bflo(y2[e]) + bflo(y3[e])), (bfhi(y0[e]) + bfhi(y1[e])) - (bfhi(y2[e]) + bfhi(y3[e])));
    const unsigned hv = pack2(bflo(xv[e]) + bflo(yv), bfhi(xv[e]) + bfhi(yv));
    h[2 * e] = bflo(hv); h[2 * e + 1] = bfhi(hv);
  }
}
__device__ __forceinline__ u32x4 layernorm8(const float (&h)[8]) {
  const float pv = __builtin_bit_cast(float, __builtin_amdgcn_readfirstlane(__builtin_bit_cast(int, h[0])));
  float s = 0.f, s2 = 0.f, v = 0.f, v2 = 0.f;
#pragma unroll
  for (int e = 0; e < 4; ++e) { const float a = h[2 * e] - pv, c = h[2 * e + 1] - pv; s += a; s2 += c; v += a * a; v2 += c * c; }
  const float md = wave_sum(s + s2) * (1.0f / D);
  v = fmaxf(wave_sum(v + v2) - md * md * (float)D, 0.f);
  const float mean = pv + md;
  const float mb = rbf(mean);
  const float sd = rbf(sqrtf(rbf(rbf(v * (1.0f / D)) + 1e-5f)));
  const float inv = 1.0f / sd;
  u32x4 o;
#pragma unroll
  for (int e = 0; e < 4; ++e) {
    const unsigned d = pack2(h[2 * e] - mb, h[2 * e + 1] - mb);
    const float a = bflo(d), c = bfhi(d);
    float qa = a * inv, qc = c * inv;
    qa = fmaf(fmaf(-qa, sd, a), inv, qa); qc = fmaf(fmaf(-qc, sd, c), inv, qc);
    o[e] = pack2(qa, qc);
  }
  return o;
}
__device__ __forceinline__ int key_row(unsigned key) { return (int)((0xffffu - (key & 0xffffu)) & (unsigned)(S - 1)); }

__global__ __launch_bounds__(512) void fused(const u16* __restrict__ x, const u16* __restrict__ ws_re, const u16* __restrict__ ws_im,
                                             const u16* __restrict__ wd_re, const u16* __restrict__ wd_im, const u16* __restrict__ w_r,
                                             const u16* __restrict__ w_gate, const u16* __restrict__ w_up, const u16* __restrict__ w_down,
                                             u16* __restrict__ out, char* __restrict__ ws) {
  __shared__ __attribute__((aligned(1024))) char lds[163840];
  const int b = blockIdx.x, xcd = b & 7, j = b >> 3;
  const int cc = xcd >> 2, et = j & 3, tt = (xcd & 3) * 4 + ((j >> 2) & 3), kh = j >> 4;
  const int g1 = (cc * 4 + et) * 2 + kh;
  const int e1 = cc * 8 + et * 2 + (tt & 1), s1 = kh * 8 + (tt >> 1);
  const int tid = threadIdx.x, w = __builtin_amdgcn_readfirstlane(tid >> 6), l = tid & 63, fr = l & 15, q = l >> 4;
  const u16* wsb = cc ? ws_im : ws_re;
  const u16* wdb = cc ? wd_im : wd_re;
  const bool down = j >= 28;
  const int fb = xcd * 28 + j, db = xcd * 4 + (j - 28);
  const auto rs = __builtin_amdgcn_make_buffer_rsrc(ws, 0, WS_END, 0x00020000);
  unsigned tag;
  unsigned long long code_lo; unsigned code_len;
  asm volatile("s_getpc_b64 %0\n.Lfused_code_lo:\n\ts_mov_b32 %1, .Lfused_code_hi-.Lfused_code_lo" : "=s"(code_lo), "=s"(code_len));

  const unsigned lbase = (unsigned)(uintptr_t)(LAS char*)lds;
  {
    const int row = w * 8 + (l >> 3);
    const size_t o = (size_t)row * D + (((l & 7) ^ ((row >> 1) & 7)) * 8);
    const u16* xp = x + (size_t)s1 * 128 * D + o;
    const u16* wp_ = wdb + (size_t)(e1 & 7) * 64 * D + o;
#pragma unroll
    for (int k = 0; k < 4; ++k) {
      glds16_asm(xp + k * 64, lbase + x1slot(k >> 1) + (k & 1) * 8192 + w * 1024);
      glds16_asm(wp_ + k * 64, lbase + k * 8192 + w * 1024);
    }
  }
  if (w < 4) {
    const int wp = w >> 1, wq = w & 1;
    unsigned r0 = 0;
    {
      const int row = tid >> 3, ln = tid & 7;
      const int xs_tile = ((j >> 3) & 1) * 8 + (xcd & 3) * 2 + ((j >> 2) & 1);
      const u16* p = j < 16 ? x + (size_t)(xs_tile * 128 + (j & 3) * 32 + row) * D + ln * 64 : wdb + (size_t)((j - 16) * 32 + row) * D + ln * 64;
      TOUCH(r0, p);
    }
    {
      C1Ctx c; c.lds = lds;
#pragma unroll
      for (int ks = 0; ks < 2; ++ks) {
#pragma unroll
        for (int m = 0; m < 2; ++m) { const int row = wp * 32 + (m ^ wq) * 16 + fr; c.poff[m][ks] = row * 128 + (((ks * 4 + q) ^ ((row >> 1) & 7)) * 16); }
#pragma unroll
        for (int n = 0; n < 2; ++n) { const int row = wq * 32 + n * 16 + fr; c.qoff[n][ks] = row * 128 + (((ks * 4 + q) ^ ((row >> 1) & 7)) * 16); }
      }
      f32x4 sw[2] = {};
      Fr1 f0, f1;
      auto epilogue = [&](const Acc1& a, int pass) {
        float2* rst = (float2*)(lds + L_RST) + pass * 64;
        if ((fr >> 2) == q) {
          const int jj = fr & 3;
          const float s1v = jj == 0 ? a.s1[0] : jj == 1 ? a.s1[1] : jj == 2 ? a.s1[2] : a.s1[3];
          const float s2v = jj == 0 ? a.s2[0] : jj == 1 ? a.s2[1] : jj == 2 ? a.s2[2] : a.s2[3];
          const float mean = s1v * (1.0f / D);
          const float var = fmaxf(s2v * (1.0f / D) - mean * mean, 0.f);
          const float sd = rbf(sqrtf(rbf(rbf(var) + 1e-5f)));
          rst[wp * 32 + wq * 16 + fr] = make_float2(rbf(mean), 1.0f / sd);
        }
        WAIT_L(0);
        BAR();
        u32x2 pk[2][2];
#pragma unroll
        for (int m = 0; m < 2; ++m) {
          const int p0 = wp * 32 + (m ^ wq) * 16 + q * 4;
          const f32x4 sa = *(const f32x4*)(rst + p0), sb = *(const f32x4*)(rst + p0 + 2);
#pragma unroll
          for (int n = 0; n < 2; ++n) {
            const float swv = sw[n][0];
            const float v0 = (a.acc[m][n][0] - sa[0] * swv) * sa[1], v1 = (a.acc[m][n][1] - sa[2] * swv) * sa[3];
            const float v2 = (a.acc[m][n][2] - sb[0] * swv) * sb[1], v3 = (a.acc[m][n][3] - sb[2] * swv) * sb[3];
            pk[m][n] = (u32x2){pack2(v0, v1), pack2(v2, v3)};
          }
        }
        const int scol = s1 * 128 + pass * 64 + wp * 32 + ((q & 1) ^ wq) * 16 + (q >> 1) * 8;
#pragma unroll
        for (int n = 0; n < 2; ++n)
          __builtin_amdgcn_raw_buffer_store_b128(widen16(pk[0][n], pk[1][n]), rs, WS_ABT + (unsigned)((e1 * 64 + wq * 32 + n * 16 + fr) * S + scol) * 2, 0, SC1);
      };
      WAIT_V(5);
      BAR();
      c1_read<0>(c, f0);
      WAIT_V(1);
      {
        Acc1 a = {};
        c1_step<0>(c, a, sw, f0, f1); c1_step<1>(c, a, sw, f1, f0); c1_step<2>(c, a, sw, f0, f1); c1_step<3>(c, a, sw, f1, f0);
        epilogue(a, 0);
      }
      {
        Acc1 a = {};
        c1_step<4>(c, a, sw, f0, f1); c1_step<5>(c, a, sw, f1, f0); c1_step<6>(c, a, sw, f0, f1); c1_step<7>(c, a, sw, f1, f0);
        epilogue(a, 1);
      }
      tag = __builtin_amdgcn_readfirstlane(*(const unsigned*)(lds + L_TAG));
      WAIT_V(0);
      KEEP(r0);
      BAR();
    }
    C2Ctx c; c.lds = lds;
#pragma unroll
    for (int ks = 0; ks < 2; ++ks) {
#pragma unroll
      for (int m = 0; m < 4; ++m) { const int row = wp * 64 + m * 16 + fr; c.poff[m][ks] = row * 128 + (((ks * 4 + q) ^ ((row >> 1) & 7)) * 16); }
#pragma unroll
      for (int n = 0; n < 4; ++n) { const int row = wq * 64 + n * 16 + fr; c.qoff[n][ks] = row * 128 + (((ks * 4 + q) ^ ((row >> 1) & 7)) * 16); }
    }
    f32x4 acc[4][4] = {};
    Fr2 f0, f1;
    BAR();
    c2_read<0>(c, f0);
    c2_steps(c, acc, f0, f1, std::make_integer_sequence<int, 15>{});
    {
      const unsigned yo = WS_Y + (unsigned)(cc * 2 + kh) * S * D * 2;
      const int chunk = (q >> 1) + 2 * (q & 1);
#pragma unroll
      for (int n = 0; n < 4; ++n) {
#pragma unroll
        for (int ks = 0; ks < 2; ++ks)
#pragma unroll
          for (int m = 0; m < 4; ++m) acc[m][n] = __builtin_amdgcn_mfma_f32_16x16x32_bf16(f1.pf[ks][m], f1.qf[ks][n], acc[m][n], 0, 0, 0);
#pragma unroll
        for (int mp = 0; mp < 2; ++mp) {
          const u32x2 a = {pack2(acc[2 * mp][n][0], acc[2 * mp][n][1]), pack2(acc[2 * mp][n][2], acc[2 * mp][n][3])};
          const u32x2 c2 = {pack2(acc[2 * mp + 1][n][0], acc[2 * mp + 1][n][1]), pack2(acc[2 * mp + 1][n][2], acc[2 * mp + 1][n][3])};
          const int row = wq * 64 + n * 16 + fr;
          __builtin_amdgcn_raw_buffer_store_b128(widen16(a, c2), rs, yo + (unsigned)((tt * 128 + row) * D + et * 128 + wp * 64 + mp * 32 + chunk * 8) * 2, 0, SC1);
        }
      }
    }
    WAIT_V(0);
  } else if (w < 6) {
    const int lw = w - 4;
    const int r8 = l >> 3, ch = l & 7;
    {
      L1Ctx c; c.lds = lds; c.w = lw;
      const u16* P = x + (size_t)s1 * 128 * D;
      const u16* Q = wdb + (size_t)(e1 & 7) * 64 * D;
#pragma unroll
      for (int i = 0; i < 4; ++i) {
        const int row = (lw * 4 + i) * 8 + r8;
        const size_t o = (size_t)row * D + ((ch ^ ((row >> 1) & 7)) * 8);
        c.xsrc[i] = P + o; c.wsrc[i] = Q + o;
      }
      l1_issue<2>(c);
      WAIT_V(20); BAR();
      WAIT_V(16); BAR();
      l1_issue<3>(c);
      WAIT_V(16); BAR();
      l1_issue<4>(c); l1_issue<5>(c);
      WAIT_V(16); BAR();
      l1_issue<6>(c); l1_issue<7>(c);
      WAIT_V(24); BAR();
      BAR();
      WAIT_V(16); BAR();
      WAIT_V(8); BAR();
      WAIT_V(0); BAR();
    }
    L2Ctx c2;
    c2.lds = lds; c2.w = lw;
    {
      const u16* P = (const u16*)(ws + WS_ABT) + (size_t)(cc * 512 + et * 128) * S + kh * 1024;
      const u16* Q = wsb + (size_t)tt * 128 * S + kh * 1024;
#pragma unroll
      for (int i = 0; i < 8; ++i) {
        const int row = (lw * 8 + i) * 8 + r8;
        const size_t o = (size_t)row * S + ((ch ^ ((row >> 1) & 7)) * 8);
        c2.psrc[i] = P + o; c2.qsrc[i] = Q + o;
      }
    }
    tag = __builtin_amdgcn_readfirstlane(*(const unsigned*)(lds + L_TAG));
    BAR();
    BAR();
    poll_words((const unsigned*)(ws + WS_F1) + g1 * 32 + fr, tag);
    l2_issueP<0>(c2); l2_issueP<1>(c2); l2_issueP<2>(c2);
    BAR();
    WAIT_V(16);
    BAR();
    l2_issueP<3>(c2); l2_issueP<4>(c2);
    l2_steps(c2, std::make_integer_sequence<int, 15>{});
  } else if (w == 6) {
    const int r8 = l >> 3, ch = l & 7;
    const unsigned ep = __hip_atomic_load((gu32*)(ws + WS_EP), RLX_AGENT);
    const u16* Q = wsb + (size_t)tt * 128 * S + kh * 1024;
    const u16* qa = Q + (size_t)r8 * S + ((ch ^ (r8 >> 1)) * 8);
    const u16* qb = Q + (size_t)(8 + r8) * S + ((ch ^ (4 + (r8 >> 1))) * 8);
    auto issueQ = [&](int T) {
#pragma unroll
      for (int i = 0; i < 8; ++i) {
        GLDS16(qa + (size_t)i * 16 * S + T * 128, lds + L_QR + ((T + 3) % 5) * 16384 + (2 * i) * 1024, 0);
        GLDS16(qb + (size_t)i * 16 * S + T * 128, lds + L_QR + ((T + 3) % 5) * 16384 + (2 * i + 1) * 1024, 0);
      }
    };
    WAIT_V(5);
    BAR();
    WAIT_V(1);
    BAR();
    tag = __builtin_amdgcn_readfirstlane(ep * 1664525u + 1013904223u);
    *(unsigned*)(lds + L_TAG) = tag;
    WAIT_L(0);
    BAR(); BAR(); BAR();
    BAR();
    BAR(); issueQ(0);
    BAR(); issueQ(1);
    BAR(); issueQ(2);
    BAR(); BAR();
    BAR(); issueQ(3); issueQ(4);
    WAIT_V(32);
    BAR();
    const unsigned* fB = (const unsigned*)(ws + WS_F1) + 512 + g1 * 32 + fr;
    BAR(); BAR();
    const unsigned vB = __hip_atomic_load((gu32*)fB, RLX_AGENT);
    WAIT_V(17);
    BAR();
    if (!__all(vB == tag)) poll_words(fB, tag);
    WAIT_V(0);
    BAR();
#pragma unroll
    for (int T = 4; T < 15; ++T) BAR();
  } else {
    unsigned r0 = 0, r1 = 0;
    auto ffn_ptr = [&](int i) -> const char* {
      const int li = j * 192 + i * 64 + l;
      if (li < 4096) { const int mrow = (li & 2047) >> 2, c4 = li & 3; return (const char*)((li < 2048 ? w_gate : w_up) + (size_t)mrow * F + xcd * 224 + c4 * 64); }
      const int rr = li - 4096;
      return (const char*)(w_down + (size_t)(rr < F ? rr : 0) * D + xcd * 64);
    };
    {
      const unsigned off = (unsigned)(j * 16 + fr) * 128u;
      const char* cp = (const char*)code_lo + ((code_len <= 65536u && off < code_len) ? off : 0u);
      TOUCH(r0, cp);
    }
    const char* fp0 = ffn_ptr(0); const char* fp1 = ffn_ptr(1); const char* fp2 = ffn_ptr(2);
    WAIT_V(5);
    BAR();
    WAIT_V(1);
    BAR();
    BAR(); TOUCH(r1, fp0);
    tag = __builtin_amdgcn_readfirstlane(*(const unsigned*)(lds + L_TAG));
    BAR(); TOUCH(r1, fp1);
    BAR(); TOUCH(r1, fp2);
    BAR();
    BAR(); BAR(); BAR();
    BAR();
    if (l == 0) __hip_atomic_store((gu32*)(ws + WS_F1) + g1 * 32 + tt, tag, RLX_AGENT);
    BAR();
    BAR();
    if (l == 0) __hip_atomic_store((gu32*)(ws + WS_F1) + 512 + g1 * 32 + tt, tag, RLX_AGENT);
#pragma unroll
    for (int T = 0; T < 16; ++T) BAR();
    WAIT_V(0);
    KEEP(r0); KEEP(r1);
  }
  const int t_own = tt * 128 + g1 * 8 + w;
  const u32x4 wr = *(const u32x4*)(w_r + l * 8);
  const u32x4 x_own = *(const u32x4*)(x + (size_t)t_own * D + l * 8);
  BAR();
  if (tid == 0) __hip_atomic_store((gu32*)(ws + WS_F2) + tt * 32 + g1, tag, RLX_AGENT);
  if (w == 0) poll_words((const unsigned*)(ws + WS_F2) + tt * 32 + fr, tag);
  BAR();
  unsigned* misc = (unsigned*)(lds + L_MISC);
  u32x4 own;
  {
    float h[8];
    load_h(x_own, rs, t_own, l, h);
    const u32x4 hn = layernorm8(h);
    float r = 0.f, r2 = 0.f;
#pragma unroll
    for (int e = 0; e < 4; ++e) { r += bflo(hn[e]) * bflo(wr[e]); r2 += bfhi(hn[e]) * bfhi(wr[e]); }
    r = wave_sum(r + r2);
    const unsigned rb = f2bf(r);
    const unsigned ord = (rb & 0x8000u) ? (~rb & 0xffffu) : (rb | 0x8000u);
    if (l == 0) { ((u16*)misc)[w] = (u16)ord; if (w == 0) misc[8] = 0; }
    WAIT_L(0);
    BAR();
    if (tid < 4) __hip_atomic_store((gu64*)(ws + WS_GR) + (tt * 16 + g1) * 4 + tid, ((unsigned long long)tag << 32) | misc[tid], RLX_AGENT);
    const unsigned ov = ((const u16*)misc)[l & 7];
    if (!__any(ov > ord || (ov == ord && (l & 7) < w))) {
      __builtin_amdgcn_raw_buffer_store_b128((u32x4){hn[0], tag, hn[1], tag}, rs, WS_HN + (unsigned)t_own * 2048 + l * 32, 0, SC1);
      __builtin_amdgcn_raw_buffer_store_b128((u32x4){hn[2], tag, hn[3], tag}, rs, WS_HN + (unsigned)t_own * 2048 + l * 32 + 16, 0, SC1);
    }
#pragma unroll
    for (int e = 0; e < 4; ++e) own[e] = pack2(h[2 * e] + bflo(hn[e]), h[2 * e + 1] + bfhi(hn[e]));
  }
  {
    const u16* wsrc = down ? w_down + (size_t)(l >> 1) * D + db * 16 + (l & 1) * 8 : ((l & 1) ? w_up : w_gate) + (size_t)(l >> 1) * F + fb * 8;
    if (down) {
#pragma unroll
      for (int i = 0; i < 7; ++i) glds16_asm(wsrc + (size_t)(w * 7 + i) * 32 * D, lbase + L_W + (w * 7 + i) * 1024);
    } else {
#pragma unroll
      for (int i = 0; i < 2; ++i) glds16_asm(wsrc + (size_t)(w * 2 + i) * 32 * F, lbase + L_W + (w * 2 + i) * 1024);
    }
  }
  unsigned key;
  bool sel_own;
  bool fast = false;
  if (!down) {
    unsigned kk[4];
    {
      u32x4 a;
      for (int it = 0; it < SPIN_MAX; ++it) {
        asm volatile("" ::: "memory");
        a = __builtin_amdgcn_raw_buffer_load_b128(rs, WS_GR + (unsigned)tid * 16, 0, SC1);
        if (__all(a[1] == tag && a[3] == tag)) break;
        __builtin_amdgcn_s_sleep(1);
      }
      __builtin_amdgcn_fence(__ATOMIC_ACQUIRE, "wavefront");
      const unsigned ib = 0xffffu - (unsigned)tid * 4;
      kk[0] = (a[0] << 16) | ib; kk[1] = (a[0] & 0xffff0000u) | (ib - 1); kk[2] = (a[2] << 16) | (ib - 2); kk[3] = (a[2] & 0xffff0000u) | (ib - 3);
    }
    {
      const unsigned m = wave_max_u32(umax(umax(kk[0], kk[1]), umax(kk[2], kk[3])));
      if (l == 0) misc[16 + w] = m;
    }
    WAIT_L(0);
    BAR();
    if (b == 0 && tid == 0) __hip_atomic_store((gu32*)(ws + WS_EP), tag, RLX_AGENT);
    {
      const u32x4 ma = *(const u32x4*)(misc + 16), mb2 = *(const u32x4*)(misc + 20);
      const unsigned lo = min(min(min(ma[0], ma[1]), min(ma[2], ma[3])), min(min(mb2[0], mb2[1]), min(mb2[2], mb2[3])));
      if (l < 16) misc[64 + w * 16 + l] = 0u;
      unsigned base = 0;
#pragma unroll
      for (int e = 0; e < 4; ++e) {
        const unsigned long long bm = __ballot(kk[e] >= lo);
        const unsigned pos = base + __builtin_amdgcn_mbcnt_hi((unsigned)(bm >> 32), __builtin_amdgcn_mbcnt_lo((unsigned)bm, 0u));
        if (kk[e] >= lo && pos < 16) misc[64 + w * 16 + pos] = kk[e];
        base += (unsigned)__builtin_popcountll(bm);
      }
      if (base > 16 && l == 0) misc[8] = 1u;
    }
    WAIT_L(0);
    BAR();
    const unsigned ovf = __builtin_amdgcn_readfirstlane(misc[8]);
    if (ovf == 0) {
      const unsigned cv = misc[64 + w * 16 + (l >> 2)];
      int rank = 0;
#pragma unroll
      for (int i = 0; i < 8; ++i) {
        const u32x4 c4 = *(const u32x4*)(misc + 64 + (l & 3) * 32 + i * 4);
#pragma unroll
        for (int e = 0; e < 4; ++e) rank += (c4[e] > cv) ? 1 : 0;
      }
      rank += (int)dpp_u<0xB1, 0xf>((unsigned)rank);
      rank += (int)dpp_u<0x4E, 0xf>((unsigned)rank);
      if (cv != 0u && rank < 8 && (l & 3) == 0) misc[32 + rank] = cv;
      WAIT_L(0);
      BAR();
      const unsigned sv = misc[32 + (l & 7)];
      if (b < 8 && tid < 8) __hip_atomic_store((gu64*)(ws + WS_SEL) + b * 32 + tid, ((unsigned long long)tag << 32) | sv, RLX_AGENT);
      key = (unsigned)__builtin_amdgcn_readlane((int)sv, w);
      sel_own = __any(key_row(sv) == t_own);
      fast = !__any(sv > key && (key_row(sv) >> 3) == (key_row(key) >> 3));
    } else {
      unsigned mine = 0;
#pragma unroll 1
      for (int it = 0; it < 8; ++it) {
        const unsigned m = wave_max_u32(umax(umax(kk[0], kk[1]), umax(kk[2], kk[3])));
#pragma unroll
        for (int e = 0; e < 4; ++e) kk[e] = (kk[e] == m) ? 0u : kk[e];
        if (l == it) mine = m;
      }
      if (l < 8) misc[192 + w * 8 + l] = mine;
      WAIT_L(0);
      BAR();
      unsigned cv = misc[192 + l];
      key = 0; sel_own = false;
#pragma unroll 1
      for (int it = 0; it < 8; ++it) {
        const unsigned m = wave_max_u32(cv);
        if (it == w) key = m;
        sel_own |= key_row(m) == t_own;
        cv = (cv == m) ? 0u : cv;
      }
      if (b < 8 && l == 0) __hip_atomic_store((gu64*)(ws + WS_SEL) + b * 32 + w, ((unsigned long long)tag << 32) | key, RLX_AGENT);
    }
  } else {
    if (w == 0) {
      unsigned long long g;
      for (int it = 0; it < SPIN_MAX; ++it) {
        g = __hip_atomic_load((gu64*)(ws + WS_SEL) + xcd * 32 + (l & 7), RLX_AGENT);
        if (__all((unsigned)(g >> 32) == tag)) break;
        __builtin_amdgcn_s_sleep(1);
      }
      if (l < 8) misc[32 + l] = (unsigned)g;
      WAIT_L(0);
    }
    BAR();
    const unsigned sv = misc[32 + (l & 7)];
    key = (unsigned)__builtin_amdgcn_readlane((int)sv, w);
    sel_own = __any(key_row(sv) == t_own);
    fast = !__any(sv > key && (key_row(sv) >> 3) == (key_row(key) >> 3));
  }
  const int t_sel = key_row(key);
  bf16x8 bw[16];
  if (!down && w == 0) {
    const char* bp = lds + L_W + (q * 8 + (fr >> 2)) * 32 + (fr & 3) * 8;
#pragma unroll
    for (int ks = 0; ks < 16; ++ks) {
      const v4i16_t g0 = __builtin_amdgcn_ds_read_tr16_b64_v4i16((LAS v4i16_t*)(bp + ks * 1024));
      const v4i16_t g1v = __builtin_amdgcn_ds_read_tr16_b64_v4i16((LAS v4i16_t*)(bp + ks * 1024 + 128));
      bw[ks] = (bf16x8){g0[0], g0[1], g0[2], g0[3], g1v[0], g1v[1], g1v[2], g1v[3]};
    }
  }
  {
    float h[8];
    u32x4 hn;
    if (down || !fast) {
      const u32x4 xs = *(const u32x4*)(x + (size_t)t_sel * D + l * 8);
      load_h(xs, rs, t_sel, l, h);
    }
    if (fast) {
      u32x4 a, c;
      for (int it = 0; it < SPIN_MAX; ++it) {
        asm volatile("" ::: "memory");
        a = __builtin_amdgcn_raw_buffer_load_b128(rs, WS_HN + (unsigned)t_sel * 2048 + l * 32, 0, SC1);
        c = __builtin_amdgcn_raw_buffer_load_b128(rs, WS_HN + (unsigned)t_sel * 2048 + l * 32 + 16, 0, SC1);
        if (__all(a[1] == tag && a[3] == tag && c[1] == tag && c[3] == tag)) break;
        __builtin_amdgcn_s_sleep(1);
      }
      hn = (u32x4){a[0], a[2], c[0], c[2]};
    } else {
      hn = layernorm8(h);
    }
    if (!down) {
      *(u32x4*)(lds + L_XS + w * XS_STRIDE + l * 16) = hn;
    } else {
      if ((l >> 1) == db) {
        float2* hh = (float2*)(lds + L_HH) + w * 16 + (l & 1) * 8;
#pragma unroll
        for (int e = 0; e < 4; ++e) { hh[2 * e] = make_float2(h[2 * e], bflo(hn[e])); hh[2 * e + 1] = make_float2(h[2 * e + 1], bfhi(hn[e])); }
      }
      if (l == 0) {
        const unsigned ord = key >> 16;
        const unsigned rb = (ord & 0x8000u) ? (ord & 0x7fffu) : (~ord & 0xffffu);
        misc[24 + w] = key;
        misc[40 + w] = __float_as_uint(rbf(1.0f / (1.0f + expf(-bf2f(rb)))));
      }
    }
    if (!sel_own) *(u32x4*)(out + (size_t)t_own * D + l * 8) = own;
  }
  if (!down) {
    WAIT_L(0);
    BAR();
    if (w == 0) {
      f32x4 a4[2] = {};
      const char* ap = lds + L_XS + (fr & 7) * XS_STRIDE + q * 16;
#pragma unroll
      for (int ks = 0; ks < 16; ++ks) {
        const bf16x8 a = *(const bf16x8*)(ap + ks * 64);
        a4[ks & 1] = __builtin_amdgcn_mfma_f32_16x16x32_bf16(a, bw[ks], a4[ks & 1], 0, 0, 0);
      }
      const f32x4 sum = a4[0] + a4[1];
      const bool hi = fr >= 8;
      unsigned actb[2];
#pragma unroll
      for (int k = 0; k < 2; ++k) {
        const float send = hi ? sum[k] : sum[2 + k];
        const float recv = dpp_f<0x128, 0xf>(send);
        const float g = rbf(hi ? recv : sum[k]), u = rbf(hi ? sum[2 + k] : recv);
        const float sg = rbf(g / (1.0f + expf(-g)));
        actb[k] = f2bf(sg * u);
      }
      const unsigned n0 = dpp_u<0xB1, 0xf>(actb[0]), n1 = dpp_u<0xB1, 0xf>(actb[1]);
      const unsigned val = (fr & 1) ? (n1 | (actb[1] << 16)) : (actb[0] | (n0 << 16));
      const int tok = q * 4 + (hi ? 2 : 0) + (fr & 1);
      if (q < 2) __hip_atomic_store((gu64*)(ws + WS_GA) + fb * 32 + tok * 4 + ((fr & 7) >> 1), ((unsigned long long)tag << 32) | val, RLX_AGENT);
    }
  } else {
    float* red = (float*)(lds + L_RED);
    for (int it = 0; it < SPIN_MAX; ++it) {
      asm volatile("" ::: "memory");
      bool ok = true;
#pragma unroll
      for (int i = 0; i < 7; ++i) {
        const int rec = w * 28 + i * 4 + q;
        const u32x4 a = __builtin_amdgcn_raw_buffer_load_b128(rs, WS_GA + (unsigned)rec * 256 + fr * 16, 0, SC1);
        ok &= (a[1] == tag) & (a[3] == tag);
        *(u32x2*)(lds + L_AS + (fr >> 1) * AS_STRIDE + rec * 16 + (fr & 1) * 8) = (u32x2){a[0], a[2]};
      }
      if (__all(ok)) break;
      __builtin_amdgcn_s_sleep(1);
    }
    WAIT_L(0);
    f32x4 a4 = {};
#pragma unroll
    for (int i = 0; i < 7; ++i) {
      const int ks = w * 7 + i;
      const bf16x8 a = *(const bf16x8*)(lds + L_AS + (fr & 7) * AS_STRIDE + ks * 64 + q * 16);
      const int r0 = ks * 32 + q * 8 + (fr >> 2);
      const int coff = (fr & 3) * 8;
      const v4i16_t g0 = __builtin_amdgcn_ds_read_tr16_b64_v4i16((LAS v4i16_t*)(lds + L_W + r0 * 32 + coff));
      const v4i16_t g1v = __builtin_amdgcn_ds_read_tr16_b64_v4i16((LAS v4i16_t*)(lds + L_W + (r0 + 4) * 32 + coff));
      const bf16x8 bg = {g0[0], g0[1], g0[2], g0[3], g1v[0], g1v[1], g1v[2], g1v[3]};
      a4 = __builtin_amdgcn_mfma_f32_16x16x32_bf16(a, bg, a4, 0, 0, 0);
    }
    if (q < 2) {
#pragma unroll
      for (int jj = 0; jj < 4; ++jj) red[w * 128 + (q * 4 + jj) * 16 + fr] = a4[jj];
    }
    WAIT_L(0);
    BAR();
    if (tid < 128) {
      float s = 0.f;
#pragma unroll
      for (int ww = 0; ww < 8; ++ww) s += red[ww * 128 + tid];
      const int t = key_row(misc[24 + (tid >> 4)]);
      const float sig = __uint_as_float(misc[40 + (tid >> 4)]);
      const float upd = rbf(rbf(s) * sig);
      const float2 hh = ((const float2*)(lds + L_HH))[tid];
      out[(size_t)t * D + db * 16 + (tid & 15)] = (u16)f2bf(hh.x + rbf(hh.y + upd));
    }
  }
  asm volatile(".Lfused_code_hi:");
}

extern "C" void kernel_launch(void* const* d_in, const int* in_sizes, int n_in, void* d_out, int out_size, void* d_ws,
                              size_t ws_size, hipStream_t stream) {
  fused<<<256, 512, 0, stream>>>((const u16*)d_in[0], (const u16*)d_in[1], (const u16*)d_in[2], (const u16*)d_in[3], (const u16*)d_in[4],
                                 (const u16*)d_in[6], (const u16*)d_in[7], (const u16*)d_in[8], (const u16*)d_in[9], (u16*)d_out, (char*)d_ws);
}
```
